```python
import torch

def _softmax(x, axis=-1):
    return torch.softmax(x, dim=axis)
def _gelu(x):
    return torch.nn.functional.gelu(x, approximate="tanh")
def _silu(x):
    return torch.nn.functional.silu(x)
def _sigmoid(x):
    return torch.sigmoid(x)
def _layernorm(x, axis=-1, eps=1e-5):
    m = x.mean(dim=axis, keepdim=True)
    v = x.var(dim=axis, keepdim=True, unbiased=False)
    return (x - m) / torch.sqrt(v + eps)
def _scatter_add(tgt, idx, src, axis=0):
    return tgt.clone().index_add_(axis, idx, src)
def _segment_sum(src, seg, n):
    out = torch.zeros((n,) + src.shape[1:], dtype=src.dtype)
    return out.index_add_(0, seg, src)
def _pad(x, pw):
    flat = tuple(v for pair in reversed(pw) for v in pair)
    return torch.nn.functional.pad(x, flat)

kernel_name = 'parallel_sum__sliding_block__moe_arch_mod__prompt'

def setup_inputs():

    _w = (lambda a: (lambda t: t.to(torch.bfloat16) if t.is_floating_point() else t)(torch.as_tensor(a)))
    def _A_setup():
        B, S, D = 1, 2048, 640
        return {
            "x": _w((lambda *s: torch.randn(*s, dtype=torch.bfloat16) * 0.05)(B, S, D)),
            "wqkv": _w((lambda *s: torch.randn(*s, dtype=torch.bfloat16) * 0.05)(D, 3 * D)),
            "wo": _w((lambda *s: torch.randn(*s, dtype=torch.bfloat16) * 0.05)(D, D)),
        }
    _A_in = _A_setup()

    def _B_setup():
        B, S, D, F, C = 1, 2048, 640, 1536, 2
        return {
            "x": _w((lambda *s: torch.randn(*s, dtype=torch.bfloat16) * 0.05)(B, S, D)),
            "w_r": _w((lambda *s: torch.randn(*s, dtype=torch.bfloat16) * 0.05)(D)),
            "w_gate": _w((lambda *s: torch.randn(*s, dtype=torch.bfloat16) * 0.05)(D, F)),
            "w_up": _w((lambda *s: torch.randn(*s, dtype=torch.bfloat16) * 0.05)(D, F)),
            "w_down": _w((lambda *s: torch.randn(*s, dtype=torch.bfloat16) * 0.05)(F, D)),
        }
    _B_in = _B_setup()

    return {
        "A_x": _A_in["x"],
        "A_wqkv": _A_in["wqkv"],
        "A_wo": _A_in["wo"],
        "B_x": _B_in["x"],
        "B_w_r": _B_in["w_r"],
        "B_w_gate": _B_in["w_gate"],
        "B_w_up": _B_in["w_up"],
        "B_w_down": _B_in["w_down"],
    }

def reference(A_x, A_wqkv, A_wo, B_x, B_w_r, B_w_gate, B_w_up, B_w_down):
    def _A_ref(x, wqkv, wo):
        B, S, D = x.shape
        H = 32; Dh = D // H; W = 256
        qkv = (lambda x, *d: x.permute(*d))((x @ wqkv).reshape(B, S, 3, H, Dh), 2, 0, 3, 1, 4)
        q, k, v = qkv[0], qkv[1], qkv[2]
        scores = torch.einsum("bhqd,bhkd->bhqk", q, k) * (Dh ** -0.5)
        idx = torch.arange(S)
        mask = (idx[:, None] - idx[None, :] >= 0) & (idx[:, None] - idx[None, :] < W)
        scores = torch.where(mask[None, None], scores, -1e9)
        p = _softmax(scores, axis=-1)
        o = torch.einsum("bhqk,bhkd->bhqd", p, v)
        return (lambda x, *d: x.permute(*d))(o, 0, 2, 1, 3).reshape(B, S, D) @ wo

    def _B_ref(x, w_r, w_gate, w_up, w_down):
        B, S, D = x.shape
        C = 2
        r = x @ w_r
        idx = torch.argsort(-r, axis=-1)[:, :C]
        idx_e = (lambda x, n, axis: torch.repeat_interleave(x, n, dim=axis))(idx.reshape(B, C, 1), D, axis=2)
        x_sel = (lambda x, i, axis: torch.take_along_dim(x, i, dim=axis))(x, idx_e, axis=1)
        g = _silu(x_sel @ w_gate)
        u = x_sel @ w_up
        y = (g * u) @ w_down
        r_sel = _sigmoid((lambda x, i, axis: torch.take_along_dim(x, i, dim=axis))(r, idx, axis=1))
        y = y * r_sel.reshape(B, C, 1)
        b_off = (torch.arange(B) * S).reshape(B, 1)
        flat_idx = (idx + b_off).reshape(B * C)
        zero = (x * 0.0).reshape(B * S, D)
        upd = _scatter_add(zero, flat_idx, y.reshape(B * C, D), axis=0)
        return x + upd.reshape(B, S, D)

    x = A_x
    xn = _layernorm(x, axis=-1)
    return x + _A_ref(xn, A_wqkv, A_wo) + _B_ref(xn, B_w_r, B_w_gate, B_w_up, B_w_down)
```

```cpp
#include <hip/hip_runtime.h>
#include <hip/hip_bf16.h>
#include <stdint.h>
typedef unsigned short u16;
__device__ __forceinline__ float bf2f(u16 v){ return __uint_as_float(((unsigned)v)<<16); }
__device__ __forceinline__ u16 f2bf(float f){ __hip_bfloat16 b = __float2bfloat16(f); return *(u16*)&b; }
__device__ __forceinline__ float rbf(float f){ return bf2f(f2bf(f)); }
constexpr int S=2048,D=640,H=32,DH=20,W=256,F=1536;

__global__ void k_ln(const u16* x,const u16* wr,u16* xn,float* r){
  int row=blockIdx.x, t=threadIdx.x;
  float v[10]; float s=0;
  for(int i=0;i<10;i++){ v[i]=bf2f(x[row*D+t+64*i]); s+=v[i]; }
  for(int m=32;m;m>>=1) s+=__shfl_xor(s,m);
  float mean=s/D; float q=0;
  for(int i=0;i<10;i++){ float d=v[i]-mean; q+=d*d; }
  for(int m=32;m;m>>=1) q+=__shfl_xor(q,m);
  float var=rbf(q/D); float mb=rbf(mean);
  float sd=rbf(sqrtf(rbf(var+1e-5f)));
  float rr=0;
  for(int i=0;i<10;i++){ float y=rbf(rbf(v[i]-mb)/sd); xn[row*D+t+64*i]=f2bf(y); rr+=y*bf2f(wr[t+64*i]); }
  for(int m=32;m;m>>=1) rr+=__shfl_xor(rr,m);
  if(t==0) r[row]=rbf(rr);
}
__global__ void k_gemm(const u16* A,const u16* B,u16* C,int M,int N,int K){
  __shared__ float As[16][17], Bs[16][17];
  int tx=threadIdx.x&15, ty=threadIdx.x>>4; int row=blockIdx.y*16+ty, col=blockIdx.x*16+tx; float acc=0;
  for(int k0=0;k0<K;k0+=16){ As[ty][tx]=bf2f(A[row*K+k0+tx]); Bs[ty][tx]=bf2f(B[(k0+ty)*N+col]); __syncthreads();
    for(int k=0;k<16;k++) acc+=As[ty][k]*Bs[k][tx]; __syncthreads(); }
  C[row*N+col]=f2bf(acc);
}
__global__ void k_attn(const u16* qkv,u16* o){
  int q=blockIdx.x, h=blockIdx.y, t=threadIdx.x;
  __shared__ float p[256]; __shared__ float red[8];
  int key=q-t; float s=-1e30f;
  if(key>=0){ float a=0; for(int d=0;d<DH;d++) a+=bf2f(qkv[q*1920+h*DH+d])*bf2f(qkv[key*1920+640+h*DH+d]); s=rbf(rbf(a)*0.22360679774997896f); }
  float m=s; for(int k=32;k;k>>=1) m=fmaxf(m,__shfl_xor(m,k));
  if((t&63)==0) red[t>>6]=m; __syncthreads(); m=fmaxf(fmaxf(red[0],red[1]),fmaxf(red[2],red[3]));
  float e=key>=0?__expf(s-m):0.f; float l=e; for(int k=32;k;k>>=1) l+=__shfl_xor(l,k);
  if((t&63)==0) red[4+(t>>6)]=l; __syncthreads(); l=red[4]+red[5]+red[6]+red[7];
  p[t]=rbf(e/l); __syncthreads();
  if(t<DH){ float a=0; for(int i=0;i<256 && q-i>=0;i++) a+=p[i]*bf2f(qkv[(q-i)*1920+1280+h*DH+t]); o[q*D+h*DH+t]=f2bf(a); }
}
__global__ void k_top2(const float* r,int* idx){
  if(threadIdx.x==0){ int i0=-1,i1=-1; float b0=-1e38f,b1=-1e38f; for(int i=0;i<S;i++){ float v=r[i]; if(v>b0){b1=b0;i1=i0;b0=v;i0=i;} else if(v>b1){b1=v;i1=i;} } idx[0]=i0; idx[1]=i1; }
}
__global__ void k_ffn1(const u16* xn,const int* idx,const u16* wg,const u16* wu,float* hbuf){
  int f=blockIdx.x*64+threadIdx.x, c=blockIdx.y; int row=idx[c]; float g=0,u=0;
  for(int k=0;k<D;k++){ float a=bf2f(xn[row*D+k]); g+=a*bf2f(wg[k*F+f]); u+=a*bf2f(wu[k*F+f]); }
  g=rbf(g); u=rbf(u); g=rbf(g/(1.f+__expf(-g))); hbuf[c*F+f]=rbf(g*u);
}
__global__ void k_ffn2(const float* hbuf,const int* idx,const float* r,const u16* wd,float* y){
  int d=blockIdx.x*64+threadIdx.x, c=blockIdx.y; float a=0;
  for(int k=0;k<F;k++) a+=hbuf[c*F+k]*bf2f(wd[k*D+d]);
  float sg=rbf(1.f/(1.f+__expf(-r[idx[c]]))); y[c*D+d]=rbf(rbf(a)*sg);
}
__global__ void k_final(const u16* x,const u16* xn,const u16* ao,const int* idx,const float* y,u16* out){
  int i=blockIdx.x*256+threadIdx.x; int row=i/D, d=i%D; float b=bf2f(xn[i]);
  if(row==idx[0]) b=rbf(b+y[d]); else if(row==idx[1]) b=rbf(b+y[D+d]);
  out[i]=f2bf(rbf(bf2f(x[i])+bf2f(ao[i]))+b);
}
extern "C" void kernel_launch(void* const* d_in, const int* in_sizes, int n_in, void* d_out, int out_size, void* d_ws, size_t ws_size, hipStream_t stream){
  const u16* x=(const u16*)d_in[0]; const u16* wqkv=(const u16*)d_in[1]; const u16* wo=(const u16*)d_in[2];
  const u16* wr=(const u16*)d_in[4]; const u16* wg=(const u16*)d_in[5]; const u16* wu=(const u16*)d_in[6]; const u16* wd=(const u16*)d_in[7];
  char* ws=(char*)d_ws; u16* xn=(u16*)ws; u16* qkv=(u16*)(ws+(4<<20)); u16* o=(u16*)(ws+(16<<20)); u16* ao=(u16*)(ws+(20<<20));
  float* r=(float*)(ws+(24<<20)); int* idx=(int*)(ws+(25<<20)); float* hb=(float*)(ws+(26<<20)); float* y=(float*)(ws+(27<<20));
  k_ln<<<S,64,0,stream>>>(x,wr,xn,r);
  k_gemm<<<dim3(1920/16,S/16),256,0,stream>>>(xn,wqkv,qkv,S,1920,D);
  k_attn<<<dim3(S,H),256,0,stream>>>(qkv,o);
  k_gemm<<<dim3(D/16,S/16),256,0,stream>>>(o,wo,ao,S,D,D);
  k_top2<<<1,64,0,stream>>>(r,idx);
  k_ffn1<<<dim3(F/64,2),64,0,stream>>>(xn,idx,wg,wu,hb);
  k_ffn2<<<dim3(D/64,2),64,0,stream>>>(hb,idx,r,wd,y);
  k_final<<<S*D/256,256,0,stream>>>(x,xn,ao,idx,y,(u16*)d_out);
}
```

```cpp
#include <hip/hip_runtime.h>
#include <hip/hip_bf16.h>
#include <stdint.h>
typedef unsigned short u16;
__device__ __forceinline__ float bf2f(u16 v){ return __uint_as_float(((unsigned)v)<<16); }
__device__ __forceinline__ u16 f2bf(float f){ __hip_bfloat16 b = __float2bfloat16(f); return *(u16*)&b; }
__device__ __forceinline__ float rbf(float f){ return bf2f(f2bf(f)); }
constexpr int S=2048,D=640,H=32,DH=20,W=256,F=1536;

__global__ void k_ln(const u16* x,const u16* wr,u16* xn,float* r){
  int row=blockIdx.x, t=threadIdx.x;
  float v[10]; float s=0;
  for(int i=0;i<10;i++){ v[i]=bf2f(x[row*D+t+64*i]); s+=v[i]; }
  for(int m=32;m;m>>=1) s+=__shfl_xor(s,m);
  float mean=s/D; float q=0;
  for(int i=0;i<10;i++){ float d=v[i]-mean; q+=d*d; }
  for(int m=32;m;m>>=1) q+=__shfl_xor(q,m);
  float var=rbf(q/D); float mb=rbf(mean);
  float sd=rbf(sqrtf(rbf(var+1e-5f)));
  float rr=0;
  for(int i=0;i<10;i++){ float y=rbf(rbf(v[i]-mb)/sd); xn[row*D+t+64*i]=f2bf(y); rr+=y*bf2f(wr[t+64*i]); }
  for(int m=32;m;m>>=1) rr+=__shfl_xor(rr,m);
  if(t==0) r[row]=rbf(rr);
}
__global__ void k_gemm(const u16* A,const u16* B,u16* C,int M,int N,int K){
  __shared__ float As[16][17], Bs[16][17];
  int tx=threadIdx.x&15, ty=threadIdx.x>>4; int row=blockIdx.y*16+ty, col=blockIdx.x*16+tx; float acc=0;
  for(int k0=0;k0<K;k0+=16){ As[ty][tx]=bf2f(A[row*K+k0+tx]); Bs[ty][tx]=bf2f(B[(k0+ty)*N+col]); __syncthreads();
    for(int k=0;k<16;k++) acc+=As[ty][k]*Bs[k][tx]; __syncthreads(); }
  C[row*N+col]=f2bf(acc);
}
__global__ void k_attn(const u16* qkv,u16* o){
  int q=blockIdx.x, h=blockIdx.y, t=threadIdx.x;
  __shared__ float p[256]; __shared__ float red[8];
  int key=q-t; float s=-1e30f;
  if(key>=0){ float a=0; for(int d=0;d<DH;d++) a+=bf2f(qkv[q*1920+h*DH+d])*bf2f(qkv[key*1920+640+h*DH+d]); s=rbf(rbf(a)*0.22360679774997896f); }
  float m=s; for(int k=32;k;k>>=1) m=fmaxf(m,__shfl_xor(m,k));
  if((t&63)==0) red[t>>6]=m; __syncthreads(); m=fmaxf(fmaxf(red[0],red[1]),fmaxf(red[2],red[3]));
  float e=key>=0?__expf(s-m):0.f; float l=e; for(int k=32;k;k>>=1) l+=__shfl_xor(l,k);
  if((t&63)==0) red[4+(t>>6)]=l; __syncthreads(); l=red[4]+red[5]+red[6]+red[7];
  p[t]=rbf(e/l); __syncthreads();
  if(t<DH){ float a=0; for(int i=0;i<256 && q-i>=0;i++) a+=p[i]*bf2f(qkv[(q-i)*1920+1280+h*DH+t]); o[q*D+h*DH+t]=f2bf(a); }
}
__global__ void k_top2(const float* r,int* idx){
  if(threadIdx.x==0){ int i0=-1,i1=-1; float b0=-1e38f,b1=-1e38f; for(int i=0;i<S;i++){ float v=r[i]; if(v>b0){b1=b0;i1=i0;b0=v;i0=i;} else if(v>b1){b1=v;i1=i;} } idx[0]=i0; idx[1]=i1; }
}
__global__ void k_ffn1(const u16* xn,const int* idx,const u16* wg,const u16* wu,float* hbuf){
  int f=blockIdx.x*64+threadIdx.x, c=blockIdx.y; int row=idx[c]; float g=0,u=0;
  for(int k=0;k<D;k++){ float a=bf2f(xn[row*D+k]); g+=a*bf2f(wg[k*F+f]); u+=a*bf2f(wu[k*F+f]); }
  g=rbf(g); u=rbf(u); g=rbf(g/(1.f+__expf(-g))); hbuf[c*F+f]=rbf(g*u);
}
__global__ void k_ffn2(const float* hbuf,const int* idx,const float* r,const u16* wd,float* y){
  int d=blockIdx.x*64+threadIdx.x, c=blockIdx.y; float a=0;
  for(int k=0;k<F;k++) a+=hbuf[c*F+k]*bf2f(wd[k*D+d]);
  float sg=rbf(1.f/(1.f+__expf(-r[idx[c]]))); y[c*D+d]=rbf(rbf(a)*sg);
}
__global__ void k_final(const u16* x,const u16* xn,const u16* ao,const int* idx,const float* y,u16* out){
  int i=blockIdx.x*256+threadIdx.x; int row=i/D, d=i%D; float b=bf2f(xn[i]);
  if(row==idx[0]) b=rbf(b+y[d]); else if(row==idx[1]) b=rbf(b+y[D+d]);
  out[i]=f2bf(rbf(bf2f(x[i])+bf2f(ao[i]))+b);
}
#define PROBE 0
#define AMP 100

typedef unsigned u32x4 __attribute__((ext_vector_type(4)));
#define GAS __attribute__((address_space(1)))
#define LAS __attribute__((address_space(3)))
__device__ __forceinline__ unsigned long long rt(){ return __builtin_amdgcn_s_memrealtime(); }
__global__ __launch_bounds__(512) void probe(const u16* x,const u16* w,const u16* bx,char* ws,unsigned* sink){
  extern __shared__ __attribute__((aligned(16))) char smem[];
  unsigned long long t0=rt();
  const int tid=threadIdx.x, lane=tid&63, wv=tid>>6; int b=blockIdx.x;
  asm volatile("s_waitcnt vmcnt(0)":::"memory");
  __syncthreads();
  unsigned long long t1=rt();
  unsigned v=((unsigned*)smem)[tid]; if(v==0x12345678) sink[tid]=v;
  unsigned long long tgt=t1+(t1-t0)*AMP; while(rt()<tgt) __builtin_amdgcn_s_sleep(4);
}
extern "C" void kernel_launch(void* const* d_in, const int* in_sizes, int n_in, void* d_out, int out_size, void* d_ws, size_t ws_size, hipStream_t stream){
  const u16* x=(const u16*)d_in[0]; const u16* wqkv=(const u16*)d_in[1]; const u16* wo=(const u16*)d_in[2];
  const u16* wr=(const u16*)d_in[4]; const u16* wg=(const u16*)d_in[5]; const u16* wu=(const u16*)d_in[6]; const u16* wd=(const u16*)d_in[7];
  char* ws=(char*)d_ws; u16* xn=(u16*)ws; u16* qkv=(u16*)(ws+(4<<20)); u16* o=(u16*)(ws+(16<<20)); u16* ao=(u16*)(ws+(20<<20));
  float* r=(float*)(ws+(24<<20)); int* idx=(int*)(ws+(25<<20)); float* hb=(float*)(ws+(26<<20)); float* y=(float*)(ws+(27<<20));
  hipFuncSetAttribute((const void*)probe,hipFuncAttributeMaxDynamicSharedMemorySize,128*1024);
  probe<<<256,512,128*1024,stream>>>(x,wqkv,(const u16*)d_in[3],ws+(64<<20),(unsigned*)(ws+(60<<20)));
  k_ln<<<S,64,0,stream>>>(x,wr,xn,r);
  k_gemm<<<dim3(1920/16,S/16),256,0,stream>>>(xn,wqkv,qkv,S,1920,D);
  k_attn<<<dim3(S,H),256,0,stream>>>(qkv,o);
  k_gemm<<<dim3(D/16,S/16),256,0,stream>>>(o,wo,ao,S,D,D);
  k_top2<<<1,64,0,stream>>>(r,idx);
  k_ffn1<<<dim3(F/64,2),64,0,stream>>>(xn,idx,wg,wu,hb);
  k_ffn2<<<dim3(D/64,2),64,0,stream>>>(hb,idx,r,wd,y);
  k_final<<<S*D/256,256,0,stream>>>(x,xn,ao,idx,y,(u16*)d_out);
}
```

```cpp
#include <hip/hip_runtime.h>
#include <stdint.h>

typedef unsigned short u16;
typedef unsigned u32;
typedef unsigned long long u64;
typedef u32 u32x2 __attribute__((ext_vector_type(2)));
typedef u32 u32x4 __attribute__((ext_vector_type(4)));
typedef short s16x4 __attribute__((ext_vector_type(4)));
typedef short s16x8 __attribute__((ext_vector_type(8)));
typedef float f32x2 __attribute__((ext_vector_type(2)));
typedef float f32x4 __attribute__((ext_vector_type(4)));
typedef float f32x16 __attribute__((ext_vector_type(16)));
typedef __bf16 bf16x2 __attribute__((ext_vector_type(2)));
#define LAS __attribute__((address_space(3)))
#define GAS __attribute__((address_space(1)))
#define DI __device__ __forceinline__
#define RLX __ATOMIC_RELAXED, __HIP_MEMORY_SCOPE_AGENT
#define SB0() __builtin_amdgcn_sched_barrier(0)

#define ALWAYS_MAX 0
#define P1X 0
#define CODE_LINES 250

constexpr u32 WS_EPOCH = 0;
constexpr u32 WS_QFLAG = 4096;
constexpr u32 WS_OFLAG = 65536;
constexpr u32 WS_TOP2  = 327680;
constexpr u32 WS_STATS = 331776;
constexpr u32 WS_RR    = 348160;
constexpr u32 WS_Y     = 393216;
constexpr u32 WS_QKV   = 1u << 20;
constexpr u32 WS_O     = 10u << 20;
constexpr u32 WS_C4    = 12u << 20;
constexpr u32 WS_TS    = 16u << 20;
constexpr u32 WS_SPAN  = 13u << 20;

constexpr u32 SLOT = 24576;
constexpr u32 L_BP = 4 * SLOT;
constexpr u32 L_WOA = 0;
constexpr u32 L_KVA = 51200, L_KVB = 88576, PLANE = 6208;
constexpr u32 L_WOB7 = 125952;
constexpr u32 L_XR = 141312;
constexpr u32 L_MISC = 155648;
constexpr u32 L_CNT = L_MISC;
constexpr u32 L_ONE = L_MISC + 1664;
constexpr u32 L_PF  = L_MISC + 2560;
constexpr u32 L_YB  = L_MISC + 2816;
constexpr u32 L_CP  = L_MISC + 4096;
constexpr u32 LDS_BYTES = 163840;
static_assert((L_ONE & 255) == 128 && (L_KVA & 255) == 0 && (L_KVB & 255) == 0, "bank quarters");

constexpr u32 SPIN_CAP = 1u << 17;

template <int I> struct IC { static constexpr int v = I; };
template <int I, int N, class F> DI void static_for(F&& f) { if constexpr (I < N) { f(IC<I>{}); static_for<I + 1, N>(f); } }
template <class T> DI LAS T* lp(u32 a) { return (LAS T*)(uintptr_t)a; }
DI u32 pk(float a, float b) { f32x2 v = {a, b}; return __builtin_bit_cast(u32, __builtin_convertvector(v, bf16x2)); }
DI float blo(u32 v) { return __uint_as_float(v << 16); }
DI float bhi(u32 v) { return __uint_as_float(v & 0xffff0000u); }
DI float rbf(float a) { return blo(pk(a, a)); }
DI u32 uni(u32 v) { return __builtin_amdgcn_readfirstlane(v); }
template <int C> DI float dppf(float v) { return __uint_as_float(__builtin_amdgcn_mov_dpp(__float_as_uint(v), C, 0xf, 0xf, true)); }
DI float x16sum(float v) { auto r = __builtin_amdgcn_permlane16_swap(__float_as_uint(v), __float_as_uint(v), false, false); return __uint_as_float(r[0]) + __uint_as_float(r[1]); }
DI float x32sum(float v) { auto r = __builtin_amdgcn_permlane32_swap(__float_as_uint(v), __float_as_uint(v), false, false); return __uint_as_float(r[0]) + __uint_as_float(r[1]); }
DI float wsum(float v) {
  v += dppf<0xB1>(v); v += dppf<0x4E>(v); v += dppf<0x141>(v); v += dppf<0x140>(v);
  return x32sum(x16sum(v));
}
DI float sel(float a, float b, u64 m) { return __builtin_amdgcn_inverse_ballot_w64(m) ? b : a; }
DI float bsel(float a, float b, u32 m) { return __uint_as_float((__float_as_uint(b) & m) | (__float_as_uint(a) & ~m)); }
DI bf16x2 asb(u32 v) { return __builtin_bit_cast(bf16x2, v); }
DI f32x16 mfma32(s16x8 a, s16x8 b, f32x16 c) { return __builtin_amdgcn_mfma_f32_32x32x16_bf16(a, b, c, 0, 0, 0); }
DI f32x4 mfma16(s16x8 a, s16x8 b, f32x4 c) { return __builtin_amdgcn_mfma_f32_16x16x32_bf16(a, b, c, 0, 0, 0); }
DI s16x8 lds128(u32 a) { return *lp<s16x8>(a); }
DI s16x8 ldstr(u32 a, u32 b) {
  s16x4 lo = __builtin_amdgcn_ds_read_tr16_b64_v4i16(lp<s16x4>(a)), hi = __builtin_amdgcn_ds_read_tr16_b64_v4i16(lp<s16x4>(b));
  return __builtin_shufflevector(lo, hi, 0, 1, 2, 3, 4, 5, 6, 7);
}
DI void dma16(u32 lds, u32 voff, const void* sbase) {
  asm volatile("s_mov_b32 m0, %0\n\ts_nop 0\n\tglobal_load_lds_dwordx4 %1, %2" :: "s"(lds), "v"(voff), "s"(sbase) : "memory");
}
DI void dma16c(u32 lds, u32 voff, const void* sbase) {
  asm volatile("s_mov_b32 m0, %0\n\ts_nop 0\n\tglobal_load_lds_dwordx4 %1, %2 sc1" :: "s"(lds), "v"(voff), "s"(sbase) : "memory");
}
#define WAITV(n) asm volatile("s_waitcnt vmcnt(" #n ")" ::: "memory")
#define WAITBAR(n) asm volatile("s_waitcnt vmcnt(%0) lgkmcnt(0)\n\ts_barrier" :: "i"(n) : "memory")
#define WAITBAR2(n0, n1) asm volatile("s_waitcnt vmcnt(" #n1 ") lgkmcnt(0)\n\ts_cmp_lg_u32 %0, 0\n\ts_cbranch_scc1 1f\n\ts_waitcnt vmcnt(" #n0 ")\n1:\n\ts_barrier" :: "s"(X) : "memory", "scc")
#define WAITBAR1(n1) asm volatile("s_cmp_lg_u32 %0, 0\n\ts_cbranch_scc0 1f\n\ts_waitcnt vmcnt(" #n1 ")\n1:\n\ts_waitcnt lgkmcnt(0)\n\ts_barrier" :: "s"(X) : "memory", "scc")
#define WAITBARV(n) asm volatile("s_waitcnt vmcnt(%0)\n\ts_barrier" :: "i"(n) : "memory")
#define BAR() asm volatile("s_waitcnt lgkmcnt(0)\n\ts_barrier" ::: "memory")
DI u64 gld64(const char* ws, u32 off) { return __hip_atomic_load((GAS u64*)(ws + off), RLX); }
DI u64 gld64w(const char* ws, u32 off) {
  u64 r; asm volatile("global_load_dwordx2 %0, %1, %2 sc1\n\ts_waitcnt vmcnt(0)" : "=&v"(r) : "v"(off), "s"(ws) : "memory"); return r;
}
#define NOUNROLL _Pragma("unroll 1")
DI void gst64(char* ws, u32 off, u64 v) { __hip_atomic_store((GAS u64*)(ws + off), v, RLX); }
DI void lds_arrive(u32 a) {
  u32 va = a, one = 1;
  asm volatile("s_waitcnt lgkmcnt(0)\n\ts_mov_b64 exec, 1\n\tds_add_u32 %0, %1\n\ts_mov_b64 exec, -1" :: "v"(va), "v"(one) : "memory");
}
DI void lds_wait(u32 a, u32 n) {
  u32 va = a, t, c;
  asm volatile("1:\n\tds_read_b32 %0, %2\n\ts_waitcnt lgkmcnt(0)\n\tv_readfirstlane_b32 %1, %0\n\ts_cmp_ge_u32 %1, %3\n\ts_cbranch_scc1 2f\n\ts_sleep 1\n\ts_branch 1b\n2:"
               : "=&v"(t), "=&s"(c) : "v"(va), "s"(n) : "memory", "scc");
}
#define POLL_TXT(T, N, OFF, WS, TAG) "s_mov_b32 " N ", 0x20000\n1:\n\tglobal_load_dwordx2 " T ", " OFF ", " WS " sc1\n\ts_waitcnt vmcnt(0)\n\t" \
    "v_cmp_ne_u64 vcc, " TAG ", " T "\n\ts_cbranch_vccz 2f\n\ts_sleep 1\n\ts_sub_u32 " N ", " N ", 1\n\ts_cbranch_scc0 1b\n2:\n\t"
DI void poll_flags(const char* ws, u32 off, u64 tag) {
  u64 t; u32 n;
  asm volatile(POLL_TXT("%0", "%1", "%2", "%3", "%4") : "=&v"(t), "=&s"(n) : "v"(off), "s"(ws), "s"(tag) : "memory", "scc", "vcc");
}
DI u32 poll_gran(const char* ws, u32 off, u32 tag) {
  u64 g;
  NOUNROLL for (u32 s = 0; s < SPIN_CAP; ++s) { g = gld64w(ws, off); if (__all((u32)(g >> 32) == tag)) break; __builtin_amdgcn_s_sleep(1); }
  return (u32)g;
}
DI void store_flag(char* ws, u32 off, u64 val, u64 mask) {
  asm volatile("s_mov_b64 exec, %3\n\tglobal_store_dwordx2 %0, %1, %2 sc1\n\ts_mov_b64 exec, -1" :: "v"(off), "v"(val), "s"(ws), "s"(mask) : "memory");
}
#define PINV(x) asm volatile("" : "+v"(x))
#define PINS(x) asm volatile("" : "+s"(x))

#define UOFF 0
#define STAMP(i) do {} while (0)

DI void wo_offsets(u32 (&wv)[8], int wl, int lane) {
#pragma unroll
  for (int k = 0; k < 8; ++k) {
    const int n = k < 5 ? k : k < 7 ? 3 * (wl & 1) + k - 5 : (wl & 1 ? 4 : 2);
    const u32 P = 64 * n + lane, r = P / 10u, pc = P - 10 * r, g = r >> 3, j = (r & 7) ^ ((g & 1) << 2);
    wv[k] = ((k >= 5 ? 40 * g + 16 : 8 * (g + (g >> 1))) + j) * 1280 + pc * 16;
    PINV(wv[k]);
  }
}
template <int PART>
DI void stage_wo(const char* woc, int H, int wl, const u32 (&wv)[8]) {
  auto at = [&](int ks) { return H == 0 ? L_WOA + ks * 5120 : ks < 7 ? L_KVA + ks * 5120 : L_WOB7 + (ks - 7) * 5120; };
  const int n2 = 3 * (wl & 1), k2 = wl >> 1;
  const char *s0 = woc + (320 * H + 40 * wl) * 1280, *s2 = woc + (320 * H + 160 * k2) * 1280;
#define WO_D(M, V, S) "s_add_u32 m0, " M "\n\ts_nop 0\n\tglobal_load_lds_dwordx4 " V ", " S "\n\t"
#define WO_5 "s_nop 4\n\t" WO_D("%7, 0", "%0", "%5") WO_D("m0, 1024", "%1", "%5") WO_D("m0, 1024", "%2", "%5") WO_D("m0, 1024", "%3", "%5") WO_D("m0, 1024", "%4", "%5")
  if (PART != 1)
    asm volatile(WO_5 WO_D("%8, 0", "%9", "%6")
                 :: "v"(wv[0]), "v"(wv[1]), "v"(wv[2]), "v"(wv[3]), "v"(wv[4]), "s"(s0), "s"(s2), "s"(at(wl)), "s"(at(8 + k2) + (wl & 1 ? 4 : 2) * 1024), "v"(wv[7]) : "memory", "scc");
  if (PART != 0)
    asm volatile(WO_5 WO_D("%8, 0", "%9", "%6") WO_D("m0, 1024", "%10", "%6")
                 :: "v"(wv[0]), "v"(wv[1]), "v"(wv[2]), "v"(wv[3]), "v"(wv[4]), "s"(s0 + 160 * 1280), "s"(s2), "s"(at(wl + 4)), "s"(at(8 + k2) + n2 * 1024), "v"(wv[5]), "v"(wv[6]) : "memory", "scc");
}

constexpr int slice_of(int v) { return v < 6 ? v : (v >= 9 && v < 13) ? v - 3 : -1; }
constexpr int page_of(int v) { return v < 9 ? v - 6 : v - 10; }
constexpr int OP_GR = 41, OP_PF = 43;
constexpr int sched_op(int u, int i) {
  if (u == 0) return i < 3 ? 6 + i : -1;
  if (u <= 3) return i < 3 ? 3 * (u + 2) + i : 30 + u - 1;
  if (u <= 6) return i < 3 ? 3 * (u + 2) + i : -1;
  if (u == 7) return i < 3 ? 27 + i : OP_GR;
  if (u == 8) return i < 3 ? 33 + i : -1;
  if (u == 9) return i == 0 ? 39 : -1;
  if (u == 10) return i < 3 ? 36 + i : -1;
  if (u == 11) return i == 0 ? OP_PF : -1;
  return -1;
}
constexpr int younger(int op, int u) {
  int n = op < 6 ? 5 - op + 1 : 0; bool seen = op < 6;
  for (int v = 0; v < u; ++v) for (int i = 0; i < 4; ++i) { const int o = sched_op(v, i); if (o < 0) continue; if (seen) ++n; if (o == op) seen = true; }
  return n;
}
constexpr int need_op(int u) {
  return slice_of(u) >= 0 ? 3 * slice_of(u) + 2 : u == 6 ? 32 : u == 13 ? 38 : -1;
}
template <bool FULL>
DI f32x16 attn_item(s16x8 q0, s16x8 q1, u32 ka, u32 kb, u32 ko, u32 va, u32 vo, u32 vstep, const u32 (&dm)[8], const u32 (&mk)[16], int nfull, float negm, float cold) {
  f32x16 ci, co, O;
#pragma unroll
  for (int i = 0; i < 16; ++i) { ci[i] = FULL ? 0.f : negm; co[i] = FULL ? 0.f : cold; O[i] = 0.f; }
  f32x16 S[2];
  s16x8 kn0, kn1, pend, vpend;
  {
    const s16x8 kd0 = lds128(ka + 4096), ko0 = lds128(ka + ko), kd1 = lds128(kb + 4096), ko1 = lds128(kb + ko), k10 = lds128(ka + 3584), k11 = lds128(kb + 3584);
    const s16x8 v0 = ldstr(va, va + 128), vo0 = ldstr(vo, vo + 128), vo1 = ldstr(vo + 256, vo + 384), v1 = ldstr(va + 256, va + 384);
    kn0 = lds128(ka + 3072); kn1 = lds128(kb + 3072);
    SB0();
    f32x16 sd = mfma32(kd0, q0, ci), so = mfma32(ko0, q0, co);
    sd = mfma32(kd1, q1, sd); so = mfma32(ko1, q1, so);
    S[1] = mfma32(k10, q0, ci);
    SB0();
    u32 p[8], pd[8];
#pragma unroll
    for (int i = 0; i < 8; ++i) {
      p[i] = pk(__builtin_amdgcn_exp2f(bsel(so[2 * i], sd[2 * i], mk[2 * i])), __builtin_amdgcn_exp2f(bsel(so[2 * i + 1], sd[2 * i + 1], mk[2 * i + 1])));
      pd[i] = p[i] & dm[i]; p[i] ^= pd[i];
      if (i == 1) { S[1] = mfma32(k11, q1, S[1]); PINV(S[1]); SB0(); }
      if (i == 3) { O = mfma32(v0, __builtin_bit_cast(s16x8, (u32x4){pd[0], pd[1], pd[2], pd[3]}), O); PINV(O); SB0(); }
      if (i == 5) { O = mfma32(vo0, __builtin_bit_cast(s16x8, (u32x4){p[0], p[1], p[2], p[3]}), O); PINV(O); SB0(); }
    }
    O = mfma32(vo1, __builtin_bit_cast(s16x8, (u32x4){p[4], p[5], p[6], p[7]}), O);
    pend = __builtin_bit_cast(s16x8, (u32x4){pd[4], pd[5], pd[6], pd[7]}); vpend = v1;
  }
#pragma unroll
  for (int e = 1; e <= 7; ++e) {
    if (!FULL && e > nfull) break;
    const f32x16& sc = S[e & 1];
    va -= vstep;
    SB0();
    f32x16 sn = ci;
    if (e < 7) sn = mfma32(kn0, q0, ci);
    const s16x8 v0 = ldstr(va, va + 128), v1 = ldstr(va + 256, va + 384);
    u32 p[8];
    float ex[16];
#pragma unroll
    for (int i = 0; i < 4; ++i) ex[i] = __builtin_amdgcn_exp2f(sc[i]);
    SB0();
    if (e < 7) sn = mfma32(kn1, q1, sn);
    if (e < 6) { kn0 = lds128(ka + 3072 - e * 512); kn1 = lds128(kb + 3072 - e * 512); }
#pragma unroll
    for (int i = 4; i < 8; ++i) ex[i] = __builtin_amdgcn_exp2f(sc[i]);
    p[0] = pk(ex[0], ex[1]); p[1] = pk(ex[2], ex[3]);
    SB0();
    O = mfma32(vpend, pend, O); PINV(O);
    SB0();
#pragma unroll
    for (int i = 8; i < 12; ++i) ex[i] = __builtin_amdgcn_exp2f(sc[i]);
    p[2] = pk(ex[4], ex[5]); p[3] = pk(ex[6], ex[7]);
    SB0();
    O = mfma32(v0, __builtin_bit_cast(s16x8, (u32x4){p[0], p[1], p[2], p[3]}), O); PINV(O);
    SB0();
#pragma unroll
    for (int i = 12; i < 16; ++i) ex[i] = __builtin_amdgcn_exp2f(sc[i]);
    p[4] = pk(ex[8], ex[9]); p[5] = pk(ex[10], ex[11]); p[6] = pk(ex[12], ex[13]); p[7] = pk(ex[14], ex[15]);
    pend = __builtin_bit_cast(s16x8, (u32x4){p[4], p[5], p[6], p[7]}); vpend = v1;
    S[(e + 1) & 1] = sn;
    SB0();
  }
  return mfma32(vpend, pend, O);
}
DI float attn_negmax(s16x8 q0, s16x8 q1, u32 ka, u32 kb, int nfull, bool has_old, int c, int hh) {
  f32x16 z;
#pragma unroll
  for (int i = 0; i < 16; ++i) z[i] = 0.f;
  float m = -__builtin_inff();
  NOUNROLL for (int e = 0; e <= nfull + (has_old ? 1 : 0); ++e) {
    const f32x16 s = mfma32(lds128(kb + 4096 - e * 512), q1, mfma32(lds128(ka + 4096 - e * 512), q0, z));
    int lo = (e == 8 ? c + 1 : 0) - 4 * hh, n = (e == 0 ? c : 31) - 4 * hh - lo;
    PINV(lo); PINV(n);
#pragma unroll
    for (int i = 0; i < 16; ++i) { const int k = (i & 3) + 8 * (i >> 2) - lo; m = fmaxf(m, (k >= 0 && k <= n) ? s[i] : m); }
  }
  auto r = __builtin_amdgcn_permlane32_swap(__float_as_uint(m), __float_as_uint(m), false, false);
  return -fmaxf(__uint_as_float(r[0]), __uint_as_float(r[1]));
}

DI void row_stats(u32x4 xa, u32 xb, u32x4 wa, u32 wb, char* ws, int srow, u32 tag, int tid) {
  const int lane = tid & 63;
  const u32 xu[5] = {xa[0], xa[1], xa[2], xa[3], xb}, wu5[5] = {wa[0], wa[1], wa[2], wa[3], wb};
  f32x2 v[5], s2 = {0.f, 0.f};
#pragma unroll
  for (int i = 0; i < 5; ++i) { v[i] = (f32x2){blo(xu[i]), bhi(xu[i])}; s2 += v[i]; }
  const float mean = wsum(s2[0] + s2[1]) * (1.f / 640.f), mb = rbf(mean);
  f32x2 q2 = {0.f, 0.f};
  float r = 0.f;
#pragma unroll
  for (int i = 0; i < 5; ++i) {
    const f32x2 d = v[i] - mean, e = v[i] - mb;
    q2 += d * d;
    r = __builtin_amdgcn_fdot2_f32_bf16(asb(pk(e[0], e[1])), asb(wu5[i]), r, false);
  }
  float q = q2[0] + q2[1];
  q += dppf<0xB1>(q); r += dppf<0xB1>(r); q += dppf<0x4E>(q); r += dppf<0x4E>(r);
  q += dppf<0x141>(q); r += dppf<0x141>(r); q += dppf<0x140>(q); r += dppf<0x140>(r);
  { auto t = __builtin_amdgcn_permlane16_swap(__float_as_uint(q), __float_as_uint(r), false, false); q = __uint_as_float(t[0]) + __uint_as_float(t[1]); }
  q = x32sum(q);
  r = __uint_as_float(__builtin_amdgcn_readlane(__float_as_uint(q), 16));
  q = __uint_as_float(__builtin_amdgcn_readlane(__float_as_uint(q), 0));
  const float sd = rbf(__builtin_amdgcn_sqrtf(rbf(rbf(q * (1.f / 640.f)) + 1e-5f)));
  const u32 pay = lane == 0 ? ((__float_as_uint(mb) >> 16) | __float_as_uint(sd)) : __float_as_uint(r * __builtin_amdgcn_rcpf(sd));
  store_flag(ws, (lane == 0 ? WS_STATS : WS_RR) + srow * 8, ((u64)tag << 32) | pay, 3);
  if (tid < 16) *lp<u32>(L_CNT + tid * 4) = 0;
  if (tid < 144) *lp<u32>(L_ONE + tid * 4) = 0x3F803F80u;
}

__global__ void __launch_bounds__(512, 2)
fused_block(const u16* __restrict__ x, const u16* __restrict__ wqkv, const u16* __restrict__ wr, char* ws,
            const u16* wo_, const u16* wg_, const u16* wu_, const u16* wd_, u16* out_) {
  (void)wo_; (void)wg_; (void)wu_; (void)wd_; (void)out_;
  auto karg = [](int i) { const __attribute__((address_space(4))) u64* k = (const __attribute__((address_space(4))) u64*)__builtin_amdgcn_kernarg_segment_ptr(); PINS(k); return (char*)k[i]; };
  extern __shared__ __attribute__((aligned(16))) char smem[];
  (void)smem;
  STAMP(0);
  const int b = blockIdx.x, xc = b & 7, bi = b >> 3;
  const int tid = threadIdx.x, lane = tid & 63, w = uni(tid >> 6);
  const int c = lane & 31, hh = lane >> 5;
  const bool helper = bi >= 30;

  if (w == 7) {
    u64 pc; asm volatile("s_getpc_b64 %0" : "=s"(pc));
    constexpr int PER = (CODE_LINES + 31) / 32;
    u32 ln = bi * PER + (lane % PER); ln = ln < CODE_LINES ? ln : CODE_LINES - 1;
    asm volatile("s_mov_b32 m0, %0\n\ts_nop 0\n\tglobal_load_lds_dword %1, %2" :: "s"(L_PF), "v"(ln * 128), "s"(pc & ~127ull) : "memory");
  }
  const int srow = b * 8 + w;
#define ROW_LOADS() u32x4 xa, wa; u32 xb, wb; \
    asm volatile("s_nop 4\n\tglobal_load_dwordx4 %0, %5, %6\n\tglobal_load_dword %1, %5, %6 offset:16\n\t" \
                 "global_load_dwordx4 %2, %5, %7\n\tglobal_load_dword %3, %5, %7 offset:16\n\ts_load_dwordx2 %4, %8, 0x0" \
                 : "=&v"(xa), "=&v"(xb), "=&v"(wa), "=&v"(wb), "=&s"(tag64) : "v"(lane * 20), "s"(x + srow * 640), "s"(wr), "s"(ws) : "memory")
#define ROW_STATS(n) \
    asm volatile("s_waitcnt vmcnt(" #n ") lgkmcnt(0)" : "+v"(xa), "+v"(xb), "+v"(wa), "+v"(wb), "+s"(tag64) :: "memory"); \
    tag64 = ((u64)uni((u32)((tag64 + 1) >> 32)) << 32) | uni((u32)tag64 + 1); tag = (u32)tag64; \
    row_stats(xa, xb, wa, wb, ws, srow, tag, tid)
  u64 tag64; u32 tag;
  const __amdgpu_buffer_rsrc_t wsr = __builtin_amdgcn_make_buffer_rsrc(ws, 0, WS_SPAN, 0x00020000);
  const int X = w >> 2, wl = w & 3;

  u64 sgr = 0;
#define RES_PREP(bb) u32 xo_[4]; const char* xs_; \
    { const int row_ = 64 * (((bb) & 7) + 8 * ((bb) >> 6)) + 16 * wl; \
      _Pragma("unroll") for (int n_ = 0; n_ < 3; ++n_) { const u32 P_ = 64 * n_ + lane, r_ = P_ / 10u; xo_[n_] = (r_ < 16 ? r_ : 15) * 1280 + (P_ - 10 * r_) * 16; PINV(xo_[n_]); } \
      xo_[3] = X ? WS_STATS + (row_ + (lane & 15)) * 8 : WS_TOP2 + ((bb) & 15) * 128; PINV(xo_[3]); \
      xs_ = karg(0) + (u32)(row_ * 1280 + (((bb) >> 3) & 7) * 160); PINS(xs_); }
#define RES_ISSUE() asm volatile("s_cmp_eq_u32 %8, 0\n\tglobal_load_dwordx2 %0, %1, %2 sc1\n\ts_cbranch_scc1 1f\n\t" \
                 WO_D("%7, 0", "%3", "%6") WO_D("m0, 1024", "%4", "%6") WO_D("m0, 1024", "%5", "%6") "\n1:" \
                 : "=&v"(sgr) : "v"(xo_[3]), "s"(ws), "v"(xo_[0]), "v"(xo_[1]), "v"(xo_[2]), "s"(xs_), "s"(L_XR + wl * 3072), "s"(X) : "memory", "scc")

  if (!helper) {
    ROW_LOADS();
    const int Lx = (xc >> 1) * 30 + bi, uj = Lx >> 3, rtile = (xc & 1) * 8 + (Lx & 7);
    const int uA = (uj / 5) * 10 + uj % 5;
    const int rot = (rtile * 3 + uj) % 10;
    const u32 vx = (8 * w + (lane >> 3)) * 1280 + (((lane & 7) ^ ((4 * w + (lane >> 4)) & 7)) << 4);
    const u32 vw = (8 * w + (lane >> 3)) * 3840 + (((lane & 7) ^ ((lane >> 2) & 4)) << 4);
    const char* xbase = (const char*)x + rtile * (128 * 1280);
    const char* wbase = (const char*)wqkv + uA * 128;
    auto piece = [&](int op) {
      const int a = op / 3, p = op % 3, pg = op - 30;
      int sl = (op < 30 ? a : pg) + rot; sl = sl >= 10 ? sl - 10 : sl;
      if (op >= 30) dma16(L_BP + (pg == 9 ? 6 : pg % 6) * 8192 + w * 1024, vw, wbase + 640 + sl * (64 * 3840));
      else if (p < 2) dma16((a % 4) * SLOT + p * 8192 + w * 1024, vx, xbase + sl * 128 + p * (64 * 1280));
      else dma16((a % 4) * SLOT + 16384 + w * 1024, vw, wbase + sl * (64 * 3840));
    };
    piece(0); piece(1); piece(2); piece(3); piece(4); piece(5);
    const int tt = wl, fh = X;
    const u32 key = (c >> 1) & 7;
    u32 xad[2][4];
#pragma unroll
    for (int i = 0; i < 4; ++i) { xad[0][i] = (32 * tt + c) * 128 + (((2 * i + hh) ^ key) << 4); xad[1][i] = xad[0][i] + 2 * SLOT; PINV(xad[0][i]); PINV(xad[1][i]); }
    const u32 q4 = (lane >> 2) & 3, p4 = lane & 3, g2 = (lane >> 4) & 1;
    u32 wadA = (8 * hh + q4) * 128 + (((4 * fh + 2 * (p4 & 1) + g2) ^ ((q4 >> 1) << 2)) << 4) + (p4 >> 1) * 8, wadB = wadA + L_BP, wadC = wadA + 2 * SLOT;
    const char* pfa;
    {
      const u32 l0 = (bi * 8 + w) * 38 + (lane < 37 ? lane : 37), l = l0 < 8960 ? l0 : 8959, m = l / 1920u, g = l - 1920 * m, r = g / 3u;
      const char *wg = karg(5), *wu = karg(6), *wd = karg(7), *wo = karg(4);
      pfa = m >= 3 ? wo + (l - 5760) * 128 : m == 2 ? wd + xc * (192 * 1280) + g * 128 : (m ? wu : wg) + r * 3072 + xc * 384 + (g - 3 * r) * 128;
    }
    u32 goff = WS_STATS + (rtile * 128 + 32 * tt + c) * 8;
    u32 wadT = (32 * X + 8 * (lane >> 4) + q4) * 128 + (((2 * wl + (p4 >> 1)) ^ ((q4 >> 1) << 2)) << 4) + (p4 & 1) * 8;
    u32 wadV = wadT + L_BP; wadT += 16384;
    u32 wadU = wadT + 2 * SLOT;
    u32 qoff = WS_QKV + (8 * uA + 4 * fh + 2 * hh) * 32768 + (rtile * 128 + 32 * tt + c) * 16;
    u32 csl = L_CP + (32 * fh + c) * 4, cswr = L_CP + X * 256 + (16 * wl + (lane & 15)) * 4;
    u32 csm = L_CP + 1024 + w * 128 + c * 4, csr = L_CP + 1024 + w * 128 + hh * 64;
    u32x4 onev = {0x3F803F80u, 0x3F803F80u, 0x3F803F80u, 0x3F803F80u};
    PINV(wadA); PINV(wadB); PINV(wadC); PINV(wadT); PINV(wadU); PINV(wadV); PINV(pfa); PINV(goff); PINV(qoff); PINV(csl); PINV(cswr); PINV(csm); PINV(csr); PINV(onev);
    u32 wv[8];
    wo_offsets(wv, wl, lane);
    ROW_STATS(6);
    if ((P1X & 8) && X == 1) __builtin_amdgcn_s_setprio(1);
    SB0();
    s16x8 xf[40], wf[2][4], wt[8], cf, cg;
    const s16x8 ones = __builtin_bit_cast(s16x8, onev);
    f32x16 acc, acb;
    f32x4 acs = {0.f, 0.f, 0.f, 0.f}, act = acs;
#pragma unroll
    for (int i = 0; i < 16; ++i) { acc[i] = 0.f; acb[i] = 0.f; }
    u64 gr = 0;
    float mneg = 0.f, rsq = 0.f;
    u32 qscb = uA < 10 ? 0x3ea52b5cu : 0x3f800000u;
    PINS(qscb);
    u32 eo[8];
    f32x4 cp[4];
    float c0 = 0.f, c1 = 0.f;
    auto epi2 = [&](float a0, float a1, float k0, float k1) {
      u32 r; float t;
      asm("v_fma_f32 %0, %2, %4, %6\n\tv_fma_f32 %1, %2, %5, %7\n\tv_mul_f32 %0, %3, %0\n\tv_mul_f32 %1, %3, %1\n\tv_cvt_pk_bf16_f32 %0, %0, %1"
          : "=&v"(r), "=&v"(t) : "v"(mneg), "v"(rsq), "v"(k0), "v"(k1), "v"(a0), "v"(a1));
      return r;
    };
    auto epi_store = [&](int second, int k) {
      __builtin_amdgcn_raw_buffer_store_b128((u32x4){eo[4 * k], eo[4 * k + 1], eo[4 * k + 2], eo[4 * k + 3]}, wsr, qoff + k * 32768 + second * (40 * 32768), 0, 16);
    };
    static_for<0, 21>([&](auto uc) __attribute__((always_inline)) {
      constexpr int u = decltype(uc)::v;
      constexpr int sa = slice_of(u), sb = page_of(u), pa = slice_of(u - 1), pb = page_of(u - 1);
      constexpr int G = u < 7 ? u : u < 9 ? -1 : u < 14 ? u - 2 : u == 16 ? 12 : u == 19 ? 13 : -1;
      if constexpr (u < 20 && need_op(u) >= 0) WAITBARV(younger(need_op(u), u));
      if (u == 7) asm volatile("s_barrier" ::: "memory");
      if (u == 15) asm volatile("s_waitcnt lgkmcnt(6)\n\ts_barrier" ::: "memory");
      if (u == 19) asm volatile("s_waitcnt lgkmcnt(4)\n\ts_barrier" ::: "memory");
      if (G == 11) {
        PINV(gr);
        u32 pay = (u32)gr;
        if (!P1X && !__all((u32)(gr >> 32) == tag)) pay = poll_gran(ws, goff, tag);
        mneg = -blo(pay); rsq = __builtin_amdgcn_rcpf(bhi(pay)) * __uint_as_float(qscb); PINV(mneg); PINV(rsq);
      }
      if (u >= UOFF && u < UOFF + 16) STAMP(16 + (u >= UOFF && u < UOFF + 16 ? u - UOFF : 0));
      SB0();
#pragma unroll
      for (int i = 0; i < 4; ++i) {
        if constexpr (u > 0) {
          if constexpr (pa >= 0) acc = mfma32(wf[(u - 1) & 1][i], xf[4 * pa + i], acc);
          else acb = mfma32(u > 13 ? wt[(4 * (u - 14) + i) & 7] : wf[(u - 1) & 1][i], xf[4 * pb + i], acb);
        }
        SB0();
        if constexpr (u < 20) {
          if constexpr (sa >= 0) {
            const u32 wbs = (sa & 2 ? wadC : wadA) + (sa & 1) * SLOT + 16384 + i * 2048;
            xf[4 * sa + i] = lds128(xad[(sa >> 1) & 1][i] + (sa & 1) * SLOT); wf[u & 1][i] = ldstr(wbs, wbs + 512);
          } else if constexpr (u < 13) { const u32 pbase = wadB + sb * 8192 + i * 2048; wf[u & 1][i] = ldstr(pbase, pbase + 512); }
          else {
            auto rd = [&](int f) { const int pn = 3 + f / 4; const u32 pbase = wadB + (pn == 9 ? 6 : pn % 6) * 8192 + (f & 3) * 2048; wt[f & 7] = ldstr(pbase, pbase + 512); };
            if constexpr (u == 13) { rd(i == 0 ? 0 : i == 1 ? 2 : i == 2 ? 3 : 5); if (!(i & 1)) rd(i == 0 ? 1 : 4); }
            else if (4 * (u - 14) + i + 6 < 28) rd(4 * (u - 14) + i + 6);
          }
        }
        if (i == 3) {
          if constexpr (u > 0 && pa >= 0) acs = mfma16(ones, cf, acs);
          if constexpr ((u >= 7 && u <= 9) || (u >= 14 && u <= 17)) act = mfma16(ones, cf, act);
          if constexpr (sa >= 0) { const u32 cb = (sa & 2 ? wadU : wadT) + (sa & 1) * SLOT; cf = ldstr(cb, cb + 512); }
          if constexpr (u >= 6 && u <= 8) cf = ldstr(wadV + sb * 8192, wadV + sb * 8192 + 512);
          if constexpr (u >= 13 && u <= 16) { constexpr int pn = 3 + 2 * (u - 13); const u32 cb = wadV + (pn == 9 ? 6 : pn % 6) * 8192; cf = ldstr(cb, cb + 512); }
        }
        if (i == 1) {
          if constexpr (u >= 14 && u <= 16) act = mfma16(ones, cg, act);
          if constexpr (u >= 13 && u <= 15) { constexpr int pn = 4 + 2 * (u - 13); const u32 cb = wadV + (pn % 6) * 8192; cg = ldstr(cb, cb + 512); }
        }
        const int op = sched_op(u, i);
        if (op >= 0 && op < 40) piece(op);
        if (op == OP_GR) asm volatile("global_load_dwordx2 %0, %1, %2 sc1" : "=v"(gr) : "v"(goff), "s"(ws) : "memory");
        if (op == OP_PF) asm volatile("s_mov_b32 m0, %0\n\ts_nop 0\n\tglobal_load_lds_dword %1, off" :: "s"(L_PF), "v"(pfa) : "memory");
        if constexpr (u == 14 || u == 18) { if (i == 1) *lp<float>(cswr + (u == 14 ? 0 : 512)) = u == 14 ? acs[0] : act[0]; }
        if constexpr (u == 15 || u == 19) {
          constexpr u32 o = u == 15 ? 0 : 512;
          if (i == 0) { c0 = *lp<float>(csl + o); c1 = *lp<float>(csl + o + 256); }
          if (i == 2) *lp<float>(csm) = c0 + c1;
          if (i == 3) {
#pragma unroll
            for (int k = 0; k < 4; ++k) cp[k] = *lp<f32x4>(csr + 16 * k);
          }
        }
        if constexpr (u == 16 || u == 17) {
          constexpr int q = 2 * (u - 16);
          eo[2 * q + i] = epi2(acc[4 * q + 2 * i], acc[4 * q + 2 * i + 1], cp[q + (i >> 1)][2 * (i & 1)], cp[q + (i >> 1)][2 * (i & 1) + 1]);
          if (i == 3) epi_store(0, u - 16);
        }
        SB0();
      }
    });
    STAMP(4);
    u32 fl = WS_QFLAG + (rtile * 30 + uA) * 128; u64 tagv = tag64;
    PINV(fl); PINV(tagv);
#pragma unroll
    for (int i = 0; i < 4; ++i) {
      eo[2 * i]     = pk(fmaf(mneg, cp[i][0], acb[4 * i]) * rsq, fmaf(mneg, cp[i][1], acb[4 * i + 1]) * rsq);
      eo[2 * i + 1] = pk(fmaf(mneg, cp[i][2], acb[4 * i + 2]) * rsq, fmaf(mneg, cp[i][3], acb[4 * i + 3]) * rsq);
      if (i & 1) epi_store(1, i >> 1);
    }
    SB0();
    STAMP(3);
    WAITBAR(2);
    if (w == 0) store_flag(ws, fl, tagv, 1);
    const char* woc = karg(4) + (bi & 7) * 160; PINS(woc);
    if (X == 1) stage_wo<0>(woc, 0, wl, wv);
    else {
      const u32 l = (bi * 4 + wl) * 27 + (lane < 26 ? lane : 26);
      asm volatile("s_mov_b32 m0, %0\n\ts_nop 4\n\tglobal_load_lds_dword %1, %2" :: "s"(L_PF), "v"((3200 + (l < 3200 ? l : 3199)) * 128), "s"(woc - (bi & 7) * 160) : "memory");
    }
    {
    int b2 = blockIdx.x; PINS(b2);
    const int xc = b2 & 7, bi = b2 >> 3;
    const int odd = bi >= 15, hl = 2 * xc + odd, grp = bi - 15 * odd, head = 16 * X + hl;
    const int oc0 = 5 * (8 * X + xc);
    const int oA = oc0 + 3 * odd, oG = oc0 + 2;
    const u32 kv = X == 0 ? L_KVA : L_KVB;
    const int g0 = grp == 0;
    u32 pollo;
    {
      const int l4 = lane & 15, isq = (l4 + 4) >> 4, b1 = (l4 >> 1) & 1, ri = l4 >> 2;
      const int oct = oc0 + 2 * odd + 2 * (l4 & 1) + (80 + 80 * b1) * (1 - isq);
      const int rt = g0 ? (isq ? b1 : (ri + 1) >> 1) : grp - 1 + ri - isq;
      pollo = WS_QFLAG + (rt * 30 + (oct >> 3)) * 128;
    }
    const int t0 = g0 ? wl : 4 * (grp + 1) + wl, t1 = g0 ? 7 - wl : t0;
    u32 qo0 = WS_QKV + (oA + hh) * 32768 + c * 16 + t0 * 512, qo1 = WS_QKV + oG * 32768 + c * 16 + t0 * 512, l16 = lane * 16;
    u32 qd = (t1 - t0) * 512;
    const int tp0 = (3 - g0) * (wl >> 1);
    const char* kvs = ws + (u32)(WS_QKV + 80 * (1 + (wl & 1)) * 32768 + ((g0 ? 0 : 128 * (grp - 1)) + 64 * tp0) * 16);
    const char* sA = kvs + (u32)(oA * 32768); const char* sB = sA + 32768; const char* sG = kvs + (u32)(oG * 32768);
    u32 mA = kv + 3 * (wl & 1) * PLANE + tp0 * 1024;
    RES_PREP(b2);
    fl += 5 * 128;
    PINV(pollo); PINV(qo0); PINV(qo1); PINV(l16); PINV(fl); PINS(sA); PINS(sB); PINS(sG); PINS(mA); PINS(qd);
    asm volatile("s_waitcnt vmcnt(6) lgkmcnt(0)\n\ts_cmp_ge_u32 %0, 4\n\ts_cbranch_scc1 1f\n\ts_waitcnt vmcnt(2)\n\ts_cmp_eq_u32 %0, 0\n\ts_cbranch_scc1 1f\n\ts_waitcnt vmcnt(1)\n1:\n\ts_barrier"
                 :: "s"(w) : "memory", "scc");
    if (w == 4) store_flag(ws, fl, tagv, 1);
    STAMP(5);
    RES_ISSUE();
    if (X == 1) stage_wo<1>(woc, 0, wl, wv);
    __builtin_amdgcn_s_setprio(1);
    u32x4 qa, qb, qa2, qb2;
    {
      u64 pt; u32 pn;
      asm volatile(POLL_TXT("%4", "%5", "%6", "%7", "%8")
                   "s_mov_b32 m0, %9\n\tglobal_load_dwordx4 %0, %14, %7 sc1\n\tglobal_load_lds_dwordx4 %13, %10 sc1\n\tglobal_load_lds_dwordx4 %13, %10 offset:1024 sc1\n\t"
                   "s_add_u32 m0, %9, 6208\n\tglobal_load_dwordx4 %1, %15, %7 sc1\n\tglobal_load_lds_dwordx4 %13, %11 sc1\n\tglobal_load_lds_dwordx4 %13, %11 offset:1024 sc1\n\t"
                   "s_add_u32 m0, %9, 12416\n\ts_cmp_lg_u32 %16, 0\n\tglobal_load_lds_dwordx4 %13, %12 sc1\n\tglobal_load_lds_dwordx4 %13, %12 offset:1024 sc1\n\t"
                   "s_cbranch_scc1 3f\n\t"
                   "global_load_lds_dwordx4 %13, %12 offset:2048 sc1\n\ts_mov_b32 m0, %9\n\ts_nop 0\n\tglobal_load_lds_dwordx4 %13, %10 offset:2048 sc1\n\t"
                   "s_add_u32 m0, %9, 6208\n\ts_nop 0\n\tglobal_load_lds_dwordx4 %13, %11 offset:2048 sc1\n\ts_branch 4f\n"
                   "3:\n\tv_add_u32 %14, %16, %14\n\tv_add_u32 %15, %16, %15\n\tglobal_load_dwordx4 %2, %14, %7 sc1\n\tglobal_load_dwordx4 %3, %15, %7 sc1\n"
                   "4:"
                   : "=&v"(qa), "=&v"(qb), "=&v"(qa2), "=&v"(qb2), "=&v"(pt), "=&s"(pn), "+v"(pollo), "+s"(ws), "+s"(tag64), "+s"(mA), "+s"(sA), "+s"(sB), "+s"(sG),
                     "+v"(l16), "+v"(qo0), "+v"(qo1), "+s"(qd)
                   :: "memory", "scc", "vcc");
    }
    u32 mk[16], dm[8];
    {
      int d = 4 * hh - 1 - c;
#pragma unroll
      for (int i = 0; i < 16; ++i) asm("v_add_u32 %0, %2, %1\n\tv_ashrrev_i32 %0, 31, %0" : "=&v"(mk[i]) : "v"(d), "i"((i & 3) + 8 * (i >> 2)));
    }
#pragma unroll
    for (int i = 0; i < 8; ++i) dm[i] = (mk[2 * i] & 0xffffu) | (mk[2 * i + 1] & 0xffff0000u);
    u32 hmask = (u32)-hh, kml = (u32)((hh | odd) - 1), kmh = (u32)((hh | (odd ^ 1)) - 1);
    const u32 realv = (u32)(((g2 & ((p4 | (p4 >> 1)) & 1))) - 1);
    u32 vlane = hh * 64 + q4 * 16 + ((realv & (kv + (3 + 2 * g2 + (p4 & 1)) * PLANE + (g2 * odd + (p4 >> 1)) * 8)) | (~realv & L_ONE));
    u32 vstep = realv & 512u;
    const int dl0 = g0 ? 0 : 8 + wl;
    u32 vold = vlane + (realv & (u32)(wl * 512));
    vlane += realv & (u32)(dl0 * 512);
    u32 kla = kv + hh * PLANE + c * 16 + (dl0 - 8) * 512, klb = kv + 2 * PLANE + c * 16 + (dl0 - 8) * 512;
    u32 oo = WS_O + X * (1u << 20) + t0 * 16384 + (c >> 4) * 8192 + xc * 1024 + (2 * odd + hh) * 256 + (c & 15) * 16;
    u32 co4 = (WS_C4 + X * (1u << 18) + t0 * 4096 + (c >> 4) * 2048 + hl * 128 + (c & 15) * 8) | (hmask & 0x80000000u);
    u32 ofl = WS_OFLAG + (t0 * 32 + head) * 128 + (lane & 1) * (qd * 8);
    u32 cnta = L_CNT + 4 * X, one = 1, tmpv, tmps;
    asm volatile("s_waitcnt vmcnt(0)\n\ts_mov_b64 exec, 1\n\tds_add_u32 %2, %3\n\ts_mov_b64 exec, -1\n"
                 "5:\n\tds_read_b32 %0, %2\n\ts_waitcnt lgkmcnt(0)\n\tv_readfirstlane_b32 %1, %0\n\ts_cmp_ge_u32 %1, 4\n\ts_cbranch_scc0 5b"
                 : "=&v"(tmpv), "=&s"(tmps), "+v"(cnta), "+v"(one), "+v"(qa), "+v"(qb), "+v"(qa2), "+v"(qb2), "+v"(sgr),
                   "+v"(kml), "+v"(kmh), "+v"(hmask), "+v"(vlane), "+v"(vold), "+v"(vstep), "+v"(kla), "+v"(klb), "+v"(oo), "+v"(co4), "+v"(ofl),
                   "+v"(dm[0]), "+v"(dm[1]), "+v"(dm[2]), "+v"(dm[3]), "+v"(dm[4]), "+v"(dm[5]), "+v"(dm[6]), "+v"(dm[7])
                 :: "memory", "scc");
    STAMP(7);
    {
      auto in_range = [&](const f32x16& O) {
        const float l = bsel(O[12], O[8], hmask);
        const float t = ((O[0] + O[1]) + (O[2] + O[3])) + ((O[4] + O[5]) + (O[6] + O[7])) + ((O[8] + O[9]) + (O[10] + O[11]));
        return __all(l > 0x1p-40f && l < 0x1p40f && __builtin_fabsf(t) < __builtin_inff());
      };
      auto put = [&](const f32x16& O, u32 ao, u32 ac) {
        const float inv = __builtin_amdgcn_rcpf(bsel(O[12], O[8], hmask));
        __builtin_amdgcn_raw_buffer_store_b128((u32x4){pk(O[0] * inv, O[1] * inv), pk(O[2] * inv, O[3] * inv), pk(O[4] * inv, O[5] * inv), pk(O[6] * inv, O[7] * inv)}, wsr, ao, 0, 16);
        __builtin_amdgcn_raw_buffer_store_b64((u32x2){pk(O[8] * inv, O[9] * inv), pk(O[10] * inv, O[11] * inv)}, wsr, ac, 0, 16);
      };
      bool general = ALWAYS_MAX || g0;
      if (!general) {
        const f32x16 O = attn_item<true>(__builtin_bit_cast(s16x8, qa), __builtin_bit_cast(s16x8, (u32x4){qb[0] & kml, qb[1] & kml, qb[2] & kmh, qb[3] & kmh}),
                                         kla, klb, 0, vlane, vold, vstep, dm, mk, 7, 0.f, 0.f);
        STAMP(12);
        if (in_range(O)) put(O, oo, co4); else general = true;
      }
      if (general) {
        int cc = c, hc = hh; PINV(cc); PINV(hc);
        NOUNROLL for (int it = 0; it <= g0; ++it) {
          const s16x8 q0 = __builtin_bit_cast(s16x8, qa);
          const s16x8 q1 = __builtin_bit_cast(s16x8, (u32x4){qb[0] & kml, qb[1] & kml, qb[2] & kmh, qb[3] & kmh});
          const int tq = it ? t1 : t0, nfull = g0 ? tq : 7;
          const u32 kd = g0 ? tq * 512 : 0, ka = kla + kd, kb = klb + kd, va = vlane + (vstep ? kd : 0u);
          bool shift = ALWAYS_MAX || !g0;
          f32x16 O;
          NOUNROLL for (;;) {
            const float negm = shift ? attn_negmax(q0, q1, ka, kb, nfull, !g0, cc, hc) : 0.f;
            O = attn_item<false>(q0, q1, ka, kb, g0 ? 4096u : 0u, va, g0 ? va : vold, vstep, dm, mk, nfull, negm, g0 ? -__builtin_inff() : negm);
            if (shift || in_range(O)) break;
            shift = true;
          }
          put(O, oo + (tq - t0) * 16384, co4 + (tq - t0) * 4096);
          qa = qa2; qb = qb2;
        }
      }
    }
    STAMP(8);
    WAITV(0);
    store_flag(ws, ofl, tagv, g0 ? 3 : 1);
    STAMP(9);
    if (X == 0) {
      lds_arrive(L_CNT + 8);
      __builtin_amdgcn_s_setprio(0);
      lds_wait(L_CNT + 8, 4);
      stage_wo<2>(woc, 1, wl, wv);
    } else {
      lds_arrive(L_CNT + 20);
    }
    }
  } else {
    ROW_LOADS();
    const int hg = xc * 2 + (bi - 30);
    const char *wg = karg(5), *wu = karg(6), *wd = karg(7);
    u32x4 hv[40];
    const int pc = (lane & 15) - (hg & 1) * 4, rg = lane >> 4;
    const bool act = pc >= 0 && pc < 12;
    const u32 gvo = (8 * w + 2 * rg) * 3072 + hg * 192 + (act ? pc : 0) * 16;
#define GU_ISSUE(n, r) asm volatile("global_load_dwordx4 %0, %4, %5\n\tglobal_load_dwordx4 %1, %4, %5 offset:3072\n\t" \
                       "global_load_dwordx4 %2, %4, %6\n\tglobal_load_dwordx4 %3, %4, %6 offset:3072" \
                       : "=&v"(hv[4 * (r)]), "=&v"(hv[4 * (r) + 1]), "=&v"(hv[4 * (r) + 2]), "=&v"(hv[4 * (r) + 3]) \
                       : "v"(gvo), "s"((const char*)wg + (n) * (64 * 3072)), "s"((const char*)wu + (n) * (64 * 3072)) : "memory")
    GU_ISSUE(0, 0); GU_ISSUE(1, 1);
    ROW_STATS(8);
    GU_ISSUE(2, 2); GU_ISSUE(3, 3); GU_ISSUE(4, 4);
    u32x4 ra, rb;
    const u32 roff = WS_RR + tid * 32;
    asm volatile("global_load_dwordx4 %0, %2, %3 sc1\n\tglobal_load_dwordx4 %1, %2, %3 offset:16 sc1" : "=&v"(ra), "=&v"(rb) : "v"(roff), "s"(ws) : "memory");
    STAMP(2);
    if (w >= 2) { GU_ISSUE(5, 5); }
    asm volatile("s_waitcnt vmcnt(4)\n\ts_cmp_ge_u32 %2, 2\n\ts_cbranch_scc1 1f\n\ts_waitcnt vmcnt(0)\n1:" : "+v"(ra), "+v"(rb) : "s"(w) : "memory", "scc");
    NOUNROLL for (u32 sp = 0; sp < SPIN_CAP && !__all(ra[1] == tag && ra[3] == tag && rb[1] == tag && rb[3] == tag); ++sp) {
      __builtin_amdgcn_s_sleep(1);
      asm volatile("global_load_dwordx4 %0, %2, %3 sc1\n\tglobal_load_dwordx4 %1, %2, %3 offset:16 sc1\n\ts_waitcnt vmcnt(0)" : "=&v"(ra), "=&v"(rb) : "v"(roff), "s"(ws) : "memory");
    }
    auto mkey = [](u32 f, u32 row) { return ((f ^ (((int)f >> 31) | 0x80000000u)) & 0xfffff800u) | (2047u - row); };
    auto mx = [](u32 p, u32 q) { return p > q ? p : q; };
    auto mn = [](u32 p, u32 q) { return p < q ? p : q; };
    u32 k1, k2;
    {
      const u32 e0 = mkey(ra[0], 4 * tid), e1 = mkey(ra[2], 4 * tid + 1), e2 = mkey(rb[0], 4 * tid + 2), e3 = mkey(rb[2], 4 * tid + 3);
      k1 = mx(mx(e0, e1), mx(e2, e3));
      k2 = mx(mn(mx(e0, e1), mx(e2, e3)), mx(mn(e0, e1), mn(e2, e3)));
    }
#define TOP2_STEP(o1, o2) do { const u32 p_ = (o1), q_ = (o2); k2 = mx(mn(k1, p_), mx(k2, q_)); k1 = mx(k1, p_); } while (0)
#define TOP2_DPP(C) TOP2_STEP(__builtin_amdgcn_mov_dpp(k1, C, 0xf, 0xf, true), __builtin_amdgcn_mov_dpp(k2, C, 0xf, 0xf, true))
    TOP2_DPP(0xB1); TOP2_DPP(0x4E); TOP2_DPP(0x141); TOP2_DPP(0x140);
    LAS u32x2* kk = lp<u32x2>(4096);
    if ((lane & 15) == 0) kk[4 * w + rg] = (u32x2){k1, k2};
    BAR();
    { const u32x2 e = kk[lane & 15], f = kk[16 + (lane & 15)]; k1 = e[0]; k2 = e[1]; TOP2_STEP(f[0], f[1]); }
    TOP2_DPP(0xB1); TOP2_DPP(0x4E); TOP2_DPP(0x141); TOP2_DPP(0x140);
    k1 = uni(k1); k2 = uni(k2);
    if (w >= 2) { GU_ISSUE(6, 6); GU_ISSUE(7, 7); }
    const u32 i0 = 2047u - (k1 & 2047u), i1 = 2047u - (k2 & 2047u);
    if (tid == 0) { gst64(ws, WS_TOP2 + hg * 128, ((u64)tag << 32) | i0 | (i1 << 16)); if (hg == 15) gst64(ws, WS_EPOCH, tag64); }
    STAMP(3);
    LAS u32* xp = lp<u32>(8192);
    if (w < 2) {
      const u32 row = w ? i1 : i0;
      u32 xrow[5];
#pragma unroll
      for (int i = 0; i < 5; ++i) xrow[i] = ((const u32*)x)[row * 320 + i * 64 + lane];
      const u32 sg = poll_gran(ws, WS_STATS + row * 8, tag);
      const float mb = blo(sg), rs = __builtin_amdgcn_rcpf(bhi(sg));
#pragma unroll
      for (int i = 0; i < 5; ++i) {
        const u32 t = pk(blo(xrow[i]) - mb, bhi(xrow[i]) - mb);
        xp[(i * 64 + lane) * 2 + w] = pk(blo(t) * rs, bhi(t) * rs);
      }
      GU_ISSUE(5, 5); GU_ISSUE(6, 6); GU_ISSUE(7, 7);
    }
    BAR();
    float ga[32];
#pragma unroll
    for (int i = 0; i < 32; ++i) ga[i] = 0.f;
#pragma unroll
    for (int n = 0; n < 10; ++n) {
      const int r = n;
      if (n == 1) GU_ISSUE(8, 8);
      if (n == 2) GU_ISSUE(9, 9);
      if (n >= 3 && n < 8) {
#pragma unroll
        for (int k = 3 * (n - 3); k < 3 * (n - 2); ++k) dma16(24576 + (k * 8 + w) * 1024, lane * 16, (const char*)wd + hg * (96 * 1280) + (k * 8 + w) * 1024);
      }
      asm volatile("s_waitcnt vmcnt(%4)" : "+v"(hv[4 * r]), "+v"(hv[4 * r + 1]), "+v"(hv[4 * r + 2]), "+v"(hv[4 * r + 3]) : "i"(n < 5 ? 63 : 4 * (9 - n) + (n < 7 ? 3 * (n - 2) : 15)) : "memory");
      const u32x2 xv = *(const LAS u32x2*)(xp + (32 * n + 4 * w + rg) * 2);
      const bf16x2 x0 = asb(xv[0]), x1 = asb(xv[1]);
#pragma unroll
      for (int m = 0; m < 2; ++m)
#pragma unroll
        for (int i = 0; i < 4; ++i) {
          const bf16x2 lo = asb(__builtin_amdgcn_perm(hv[4 * r + 2 * m + 1][i], hv[4 * r + 2 * m][i], 0x05040100u));
          const bf16x2 hi = asb(__builtin_amdgcn_perm(hv[4 * r + 2 * m + 1][i], hv[4 * r + 2 * m][i], 0x07060302u));
          ga[16 * m + 2 * i] = __builtin_amdgcn_fdot2_f32_bf16(lo, x0, ga[16 * m + 2 * i], false);
          ga[16 * m + 8 + 2 * i] = __builtin_amdgcn_fdot2_f32_bf16(lo, x1, ga[16 * m + 8 + 2 * i], false);
          ga[16 * m + 2 * i + 1] = __builtin_amdgcn_fdot2_f32_bf16(hi, x0, ga[16 * m + 2 * i + 1], false);
          ga[16 * m + 8 + 2 * i + 1] = __builtin_amdgcn_fdot2_f32_bf16(hi, x1, ga[16 * m + 8 + 2 * i + 1], false);
        }
#pragma unroll
      for (int i = 0; i < 32; i += 8) asm volatile("" : "+v"(ga[i]), "+v"(ga[i + 1]), "+v"(ga[i + 2]), "+v"(ga[i + 3]), "+v"(ga[i + 4]), "+v"(ga[i + 5]), "+v"(ga[i + 6]), "+v"(ga[i + 7]));
    }
    STAMP(4);
    LAS float* red = lp<float>(11264);
    {
      float h16[16], h8[8];
#pragma unroll
      for (int i = 0; i < 16; ++i) { auto r = __builtin_amdgcn_permlane32_swap(__float_as_uint(ga[i]), __float_as_uint(ga[i + 16]), false, false); h16[i] = __uint_as_float(r[0]) + __uint_as_float(r[1]); }
#pragma unroll
      for (int i = 0; i < 8; ++i) { auto r = __builtin_amdgcn_permlane16_swap(__float_as_uint(h16[i]), __float_as_uint(h16[i + 8]), false, false); h8[i] = __uint_as_float(r[0]) + __uint_as_float(r[1]); }
      if (act) {
        LAS float* d = red + w * 384 + rg * 96 + pc * 8;
        *(LAS f32x4*)d = (f32x4){h8[0], h8[1], h8[2], h8[3]}; *(LAS f32x4*)(d + 4) = (f32x4){h8[4], h8[5], h8[6], h8[7]};
      }
    }
    BAR();
    STAMP(6);
    LAS u16* hs = lp<u16>(4096);
    if (tid < 192) {
      float g = 0.f, u = 0.f;
#pragma unroll
      for (int k = 0; k < 8; ++k) { g += red[k * 384 + tid]; u += red[k * 384 + 192 + tid]; }
      g = rbf(g); u = rbf(u);
      const float hval = rbf(g * __builtin_amdgcn_rcpf(1.f + __expf(-g))) * u;
      const int t = tid >= 96, f = tid - 96 * t;
      hs[(f >> 1) * 4 + t * 2 + (f & 1)] = (u16)pk(hval, hval);
    }
    STAMP(12);
    WAITBAR(0);
    STAMP(7);
    float y[2][8];
#pragma unroll
    for (int i = 0; i < 8; ++i) y[0][i] = y[1][i] = 0.f;
    const int rgp = tid / 80, cp = tid - 80 * rgp;
    if (tid < 480) {
#pragma unroll
      for (int i = 0; i < 8; ++i) {
        const u32 ad = 24576 + (12 * i + 2 * rgp) * 1280 + cp * 16;
        const u32x4 d0 = *lp<u32x4>(ad), d1 = *lp<u32x4>(ad + 1280);
        const u32x2 hp = *lp<u32x2>(4096 + (6 * i + rgp) * 8);
        const bf16x2 h0 = asb(hp[0]), h1 = asb(hp[1]);
#pragma unroll
        for (int k = 0; k < 4; ++k) {
          const bf16x2 dl_ = asb(__builtin_amdgcn_perm(d1[k], d0[k], 0x05040100u));
          const bf16x2 dh_ = asb(__builtin_amdgcn_perm(d1[k], d0[k], 0x07060302u));
          y[0][2 * k] = __builtin_amdgcn_fdot2_f32_bf16(dl_, h0, y[0][2 * k], false); y[1][2 * k] = __builtin_amdgcn_fdot2_f32_bf16(dl_, h1, y[1][2 * k], false);
          y[0][2 * k + 1] = __builtin_amdgcn_fdot2_f32_bf16(dh_, h0, y[0][2 * k + 1], false); y[1][2 * k + 1] = __builtin_amdgcn_fdot2_f32_bf16(dh_, h1, y[1][2 * k + 1], false);
        }
      }
    }
    STAMP(8);
    BAR();
    LAS float* r3 = lp<float>(24576);
    if (tid < 480) {
#pragma unroll
      for (int t = 0; t < 2; ++t) {
        LAS float* d = r3 + rgp * 1280 + t * 640 + cp * 8;
        *(LAS f32x4*)d = (f32x4){y[t][0], y[t][1], y[t][2], y[t][3]}; *(LAS f32x4*)(d + 4) = (f32x4){y[t][4], y[t][5], y[t][6], y[t][7]};
      }
    }
    BAR();
    if (tid < 320) {
      const u32 kq = (tid >= 160 ? k2 : k1) & 0xfffff800u;
      const float rsel = rbf(__uint_as_float(kq ^ ((kq >> 31) ? 0x80000000u : ~0u)));
      const float sg = rbf(__builtin_amdgcn_rcpf(1.f + __expf(-rsel)));
      f32x4 s = *(const LAS f32x4*)(r3 + tid * 4);
#pragma unroll
      for (int k = 1; k < 6; ++k) s += *(const LAS f32x4*)(r3 + k * 1280 + tid * 4);
      const u32 off = WS_Y + hg * 10240 + tid * 32;
      __builtin_amdgcn_raw_buffer_store_b128((u32x4){__float_as_uint(s[0] * sg), tag, __float_as_uint(s[1] * sg), tag}, wsr, off, 0, 16);
      __builtin_amdgcn_raw_buffer_store_b128((u32x4){__float_as_uint(s[2] * sg), tag, __float_as_uint(s[3] * sg), tag}, wsr, off + 16, 0, 16);
    }
    BAR();
    STAMP(5);
    u32 wv[8];
    wo_offsets(wv, wl, lane);
    RES_PREP(b);
    RES_ISSUE();
    stage_wo<2>(karg(4) + (bi & 7) * 160, X, wl, wv);
    asm volatile("s_waitcnt vmcnt(0) lgkmcnt(0)\n\ts_barrier" : "+v"(sgr) :: "memory");
    if (X == 1) { lds_arrive(L_CNT + 20); lds_arrive(L_CNT + 4); }
    STAMP(9);
  }

  {
    int b3 = blockIdx.x; PINS(b3);
    const int rt3 = (b3 & 7) + 8 * (b3 >> 6), ct3 = (b3 >> 3) & 7;
    const int tk = lane & 15, g = lane >> 4;
    const int t16 = 4 * rt3 + wl, row = 16 * t16 + tk;
    const u32 wfa = (8 * g + 4 * (g & 1) + ((lane >> 2) & 3)) * 160 + (lane & 3) * 8, wfb = (8 * g + 4 * ((g & 1) ^ 1) + ((lane >> 2) & 3)) * 160 + (lane & 3) * 8;
    const u32 res = L_KVB + wl * 5120 + lane * 16;
    f32x4 acc[5];
    const u32 wb0 = X == 0 ? L_WOA : L_KVA, wb7 = X == 0 ? L_WOA + 7 * 5120 : L_WOB7;
    char* out = karg(8);
    u32 ob = WS_O + X * (1u << 20) + t16 * 8192 + lane * 16, cb = WS_C4 + X * (1u << 18) + t16 * 2048 + g * 256 + tk * 8;
    u32 ofo = (row * 640 + 80 * ct3 + 16 * (g & 1) + 8 * (g >> 1)) * 2, ofo2 = (row * 640 + 80 * ct3 + 64 + 4 * g) * 2;
    u32 w8a = wb7 + 5120 + wfa, w8b = wb7 + 5120 + wfb, resv = res;
    u32 ob2 = ob + 4096, pfo = WS_OFLAG + ((t16 >> 1) * 32 + 16 * X + tk) * 128;
    u32 w0a = wb0 + wfa, w0b = wb0 + wfb;
    PINV(ofo); PINV(ofo2); PINV(w0a); PINV(w0b); PINV(w8a); PINV(w8b); PINV(resv); PINV(ob); PINV(ob2); PINV(cb); PINV(pfo); PINS(out);
    bool early = false;
    if (X == 0) {
      u32 tp = (u32)sgr;
      if (!__all((u32)(sgr >> 32) == tag)) tp = poll_gran(ws, WS_TOP2 + (b3 & 15) * 128, tag);
      tp = uni(tp);
#pragma unroll
      for (int t = 0; t < 5; ++t) acc[t] = (f32x4){0.f, 0.f, 0.f, 0.f};
      early = (int)((tp & 0xffff) >> 4) == t16 || (int)(tp >> 20) == t16;
      if (early) { WAITV(0); lds_arrive(L_CNT + 12); __builtin_amdgcn_s_setprio(1); }
#pragma unroll 1
      for (int cidx = 0; cidx < 2; ++cidx) {
        const u32 id = cidx ? tp >> 16 : tp & 0xffff;
        if ((int)(id >> 4) != t16) continue;
        LAS float* yb = lp<float>(L_YB + wl * 320);
#pragma unroll 1
        for (int p = 0; p < 2; ++p) {
          const int col = p * 64 + lane;
          const u32 off = WS_Y + cidx * 5120 + (80 * ct3 + (col < 80 ? col : 79)) * 8;
          float s = 0.f;
          NOUNROLL for (u32 sp = 0; sp < SPIN_CAP; ++sp) {
            u64 gv[16]; bool ok = true; s = 0.f;
#pragma unroll
            for (int k = 0; k < 16; ++k) gv[k] = gld64(ws, off + k * 10240);
#pragma unroll
            for (int k = 0; k < 16; ++k) { ok = ok && (u32)(gv[k] >> 32) == tag; s += __uint_as_float((u32)gv[k]); }
            if (__all(ok)) break;
            __builtin_amdgcn_s_sleep(1);
          }
          if (col < 80) yb[col] = s;
        }
        asm volatile("s_waitcnt lgkmcnt(0)" ::: "memory");
        if (tk == (int)(id & 15)) {
#pragma unroll
          for (int t = 0; t < 5; ++t) acc[t] += *(const LAS f32x4*)(yb + 16 * t + 4 * g);
        }
        asm volatile("s_waitcnt lgkmcnt(0)" ::: "memory");
      }
    } else {
      u32 sg = (u32)sgr;
      if (!__all((u32)(sgr >> 32) == tag)) sg = poll_gran(ws, WS_STATS + row * 8, tag);
      const float mb = blo(sg), rs = __builtin_amdgcn_rcpf(bhi(sg));
#pragma unroll
      for (int t = 0; t < 5; ++t) {
        const u32x2 xr = *lp<u32x2>(L_XR + wl * 3072 + tk * 160 + g * 8 + t * 32);
#pragma unroll
        for (int i = 0; i < 2; ++i) {
          const float a0 = blo(xr[i]), a1 = bhi(xr[i]);
          const u32 d = pk(a0 - mb, a1 - mb), n = pk(blo(d) * rs, bhi(d) * rs);
          acc[t][2 * i] = a0 + blo(n); acc[t][2 * i + 1] = a1 + bhi(n);
        }
        PINV(acc[t]);
      }
    }
    STAMP(1);
    lds_wait(X == 0 ? L_CNT + 4 : L_CNT + 12, 4);
    STAMP(2);
    s16x8 wq[8][5];
#pragma unroll
    for (int ks = 0; ks < 8; ++ks)
#pragma unroll
      for (int t = 0; t < 5; ++t) wq[ks][t] = ldstr((ks < 7 ? w0a + ks * 5120 : w8a - 5120) + t * 32, (ks < 7 ? w0b + ks * 5120 : w8b - 5120) + t * 32);
    if (X == 0 && !early) { WAITV(0); lds_arrive(L_CNT + 12); __builtin_amdgcn_s_setprio(1); }
    STAMP(10);
    u32x4 of[10];
    {
      u64 pt; u32 pn;
      asm volatile(POLL_TXT("%10", "%11", "%12", "%13", "%14")
                   "global_load_dwordx4 %0, %15, %13 sc1\n\tglobal_load_dwordx4 %1, %15, %13 offset:1024 sc1\n\tglobal_load_dwordx4 %2, %15, %13 offset:2048 sc1\n\t"
                   "global_load_dwordx4 %3, %15, %13 offset:3072 sc1\n\tglobal_load_dwordx4 %4, %16, %13 sc1\n\tglobal_load_dwordx4 %5, %16, %13 offset:1024 sc1\n\t"
                   "global_load_dwordx4 %6, %16, %13 offset:2048 sc1\n\tglobal_load_dwordx4 %7, %16, %13 offset:3072 sc1\n\t"
                   "global_load_dwordx2 v[240:241], %17, %13 sc1\n\tglobal_load_dwordx2 v[242:243], %17, %13 offset:128 sc1\n\t"
                   "global_load_dwordx2 v[244:245], %17, %13 offset:1024 sc1\n\tglobal_load_dwordx2 v[246:247], %17, %13 offset:1152 sc1\n\t"
                   : "=&v"(of[0]), "=&v"(of[1]), "=&v"(of[2]), "=&v"(of[3]), "=&v"(of[4]), "=&v"(of[5]), "=&v"(of[6]), "=&v"(of[7]),
                     "=&{v[240:243]}"(of[8]), "=&{v[244:247]}"(of[9]), "=&v"(pt), "=&s"(pn), "+v"(pfo), "+s"(ws), "+s"(tag64), "+v"(ob), "+v"(ob2), "+v"(cb)
                   :: "memory", "scc", "vcc");
    }
    if (X == 1) {
      lds_wait(L_CNT + 32 + 4 * wl, 1);
#pragma unroll
      for (int t = 0; t < 5; ++t) acc[t] += *lp<f32x4>(resv + t * 1024);
    }
    s16x8 w8[2][5];
#pragma unroll
    for (int ks = 0; ks < 10; ++ks) {
      if (ks == 0) asm volatile("s_waitcnt vmcnt(10)" : "+v"(of[0]), "+v"(of[1]) :: "memory");
      if (ks == 2) asm volatile("s_waitcnt vmcnt(7)" : "+v"(of[2]), "+v"(of[3]), "+v"(of[4]) :: "memory");
      if (ks == 5) asm volatile("s_waitcnt vmcnt(4)" : "+v"(of[5]), "+v"(of[6]), "+v"(of[7]) :: "memory");
      if (ks == 8) asm volatile("s_waitcnt vmcnt(0)" : "+v"(of[8]), "+v"(of[9]) :: "memory");
      SB0();
#pragma unroll
      for (int t = 0; t < 5; ++t) {
        acc[t] = mfma16(ks < 8 ? wq[ks < 8 ? ks : 0][t] : w8[ks & 1][t], __builtin_bit_cast(s16x8, of[ks]), acc[t]);
        if (ks == 2 || ks == 3) w8[ks & 1][t] = ldstr(w8a + (ks & 1) * 5120 + t * 32, w8b + (ks & 1) * 5120 + t * 32);
      }
      SB0();
    }
    STAMP(30);
    if (X == 0) {
      lds_wait(L_CNT + 20, 4);
#pragma unroll
      for (int t = 0; t < 5; ++t) *lp<f32x4>(resv + t * 1024) = acc[t];
      lds_arrive(L_CNT + 32 + 4 * wl);
    } else {
      u32 o[10];
#pragma unroll
      for (int t = 0; t < 5; ++t) { o[2 * t] = pk(acc[t][0], acc[t][1]); o[2 * t + 1] = pk(acc[t][2], acc[t][3]); }
#pragma unroll
      for (int t = 0; t < 4; t += 2) {
        auto r0 = __builtin_amdgcn_permlane16_swap(o[2 * t], o[2 * t + 2], false, false);
        auto r1 = __builtin_amdgcn_permlane16_swap(o[2 * t + 1], o[2 * t + 3], false, false);
        *(GAS u32x4*)(out + ofo + 32 * t) = (u32x4){r0[0], r1[0], r0[1], r1[1]};
      }
      *(GAS u32x2*)(out + ofo2) = (u32x2){o[8], o[9]};
    }
    STAMP(31);
  }
}

extern "C" void kernel_launch(void* const* d_in, const int* in_sizes, int n_in, void* d_out, int out_size, void* d_ws, size_t ws_size,
                              hipStream_t stream) {
  (void)in_sizes; (void)n_in; (void)out_size; (void)ws_size;
  (void)hipFuncSetAttribute((const void*)fused_block, hipFuncAttributeMaxDynamicSharedMemorySize, LDS_BYTES);
  fused_block<<<256, 512, LDS_BYTES, stream>>>((const u16*)d_in[0], (const u16*)d_in[1], (const u16*)d_in[4], (char*)d_ws, (const u16*)d_in[2],
                                               (const u16*)d_in[5], (const u16*)d_in[6], (const u16*)d_in[7], (u16*)d_out);
}
```

```cpp
#define STAMPS
#define AMPI 16
#define SMASK 0x14001u
#include <hip/hip_runtime.h>
#include <stdint.h>

typedef unsigned short u16;
typedef unsigned u32;
typedef unsigned long long u64;
typedef u32 u32x2 __attribute__((ext_vector_type(2)));
typedef u32 u32x4 __attribute__((ext_vector_type(4)));
typedef short s16x4 __attribute__((ext_vector_type(4)));
typedef short s16x8 __attribute__((ext_vector_type(8)));
typedef float f32x2 __attribute__((ext_vector_type(2)));
typedef float f32x4 __attribute__((ext_vector_type(4)));
typedef float f32x16 __attribute__((ext_vector_type(16)));
typedef __bf16 bf16x2 __attribute__((ext_vector_type(2)));
#define LAS __attribute__((address_space(3)))
#define GAS __attribute__((address_space(1)))
#define DI __device__ __forceinline__
#define RLX __ATOMIC_RELAXED, __HIP_MEMORY_SCOPE_AGENT
#define SB0() __builtin_amdgcn_sched_barrier(0)

#define ALWAYS_MAX 0
#define P1X 0
#define CODE_LINES 250

constexpr u32 WS_EPOCH = 0;
constexpr u32 WS_QFLAG = 4096;
constexpr u32 WS_OFLAG = 65536;
constexpr u32 WS_TOP2  = 327680;
constexpr u32 WS_STATS = 331776;
constexpr u32 WS_RR    = 348160;
constexpr u32 WS_Y     = 393216;
constexpr u32 WS_QKV   = 1u << 20;
constexpr u32 WS_O     = 10u << 20;
constexpr u32 WS_C4    = 12u << 20;
constexpr u32 WS_TS    = 16u << 20;
constexpr u32 WS_SPAN  = 13u << 20;

constexpr u32 SLOT = 24576;
constexpr u32 L_BP = 4 * SLOT;
constexpr u32 L_WOA = 0;
constexpr u32 L_KVA = 51200, L_KVB = 88576, PLANE = 6208;
constexpr u32 L_WOB7 = 125952;
constexpr u32 L_XR = 141312;
constexpr u32 L_MISC = 155648;
constexpr u32 L_CNT = L_MISC;
constexpr u32 L_ONE = L_MISC + 1664;
constexpr u32 L_PF  = L_MISC + 2560;
constexpr u32 L_YB  = L_MISC + 2816;
constexpr u32 L_CP  = L_MISC + 4096;
constexpr u32 LDS_BYTES = 163840;
static_assert((L_ONE & 255) == 128 && (L_KVA & 255) == 0 && (L_KVB & 255) == 0, "bank quarters");

constexpr u32 SPIN_CAP = 1u << 17;

template <int I> struct IC { static constexpr int v = I; };
template <int I, int N, class F> DI void static_for(F&& f) { if constexpr (I < N) { f(IC<I>{}); static_for<I + 1, N>(f); } }
template <class T> DI LAS T* lp(u32 a) { return (LAS T*)(uintptr_t)a; }
DI u32 pk(float a, float b) { f32x2 v = {a, b}; return __builtin_bit_cast(u32, __builtin_convertvector(v, bf16x2)); }
DI float blo(u32 v) { return __uint_as_float(v << 16); }
DI float bhi(u32 v) { return __uint_as_float(v & 0xffff0000u); }
DI float rbf(float a) { return blo(pk(a, a)); }
DI u32 uni(u32 v) { return __builtin_amdgcn_readfirstlane(v); }
template <int C> DI float dppf(float v) { return __uint_as_float(__builtin_amdgcn_mov_dpp(__float_as_uint(v), C, 0xf, 0xf, true)); }
DI float x16sum(float v) { auto r = __builtin_amdgcn_permlane16_swap(__float_as_uint(v), __float_as_uint(v), false, false); return __uint_as_float(r[0]) + __uint_as_float(r[1]); }
DI float x32sum(float v) { auto r = __builtin_amdgcn_permlane32_swap(__float_as_uint(v), __float_as_uint(v), false, false); return __uint_as_float(r[0]) + __uint_as_float(r[1]); }
DI float wsum(float v) {
  v += dppf<0xB1>(v); v += dppf<0x4E>(v); v += dppf<0x141>(v); v += dppf<0x140>(v);
  return x32sum(x16sum(v));
}
DI float sel(float a, float b, u64 m) { return __builtin_amdgcn_inverse_ballot_w64(m) ? b : a; }
DI float bsel(float a, float b, u32 m) { return __uint_as_float((__float_as_uint(b) & m) | (__float_as_uint(a) & ~m)); }
DI bf16x2 asb(u32 v) { return __builtin_bit_cast(bf16x2, v); }
DI f32x16 mfma32(s16x8 a, s16x8 b, f32x16 c) { return __builtin_amdgcn_mfma_f32_32x32x16_bf16(a, b, c, 0, 0, 0); }
DI f32x4 mfma16(s16x8 a, s16x8 b, f32x4 c) { return __builtin_amdgcn_mfma_f32_16x16x32_bf16(a, b, c, 0, 0, 0); }
DI s16x8 lds128(u32 a) { return *lp<s16x8>(a); }
DI s16x8 ldstr(u32 a, u32 b) {
  s16x4 lo = __builtin_amdgcn_ds_read_tr16_b64_v4i16(lp<s16x4>(a)), hi = __builtin_amdgcn_ds_read_tr16_b64_v4i16(lp<s16x4>(b));
  return __builtin_shufflevector(lo, hi, 0, 1, 2, 3, 4, 5, 6, 7);
}
DI void dma16(u32 lds, u32 voff, const void* sbase) {
  asm volatile("s_mov_b32 m0, %0\n\ts_nop 0\n\tglobal_load_lds_dwordx4 %1, %2" :: "s"(lds), "v"(voff), "s"(sbase) : "memory");
}
DI void dma16c(u32 lds, u32 voff, const void* sbase) {
  asm volatile("s_mov_b32 m0, %0\n\ts_nop 0\n\tglobal_load_lds_dwordx4 %1, %2 sc1" :: "s"(lds), "v"(voff), "s"(sbase) : "memory");
}
#define WAITV(n) asm volatile("s_waitcnt vmcnt(" #n ")" ::: "memory")
#define WAITBAR(n) asm volatile("s_waitcnt vmcnt(%0) lgkmcnt(0)\n\ts_barrier" :: "i"(n) : "memory")
#define WAITBAR2(n0, n1) asm volatile("s_waitcnt vmcnt(" #n1 ") lgkmcnt(0)\n\ts_cmp_lg_u32 %0, 0\n\ts_cbranch_scc1 1f\n\ts_waitcnt vmcnt(" #n0 ")\n1:\n\ts_barrier" :: "s"(X) : "memory", "scc")
#define WAITBAR1(n1) asm volatile("s_cmp_lg_u32 %0, 0\n\ts_cbranch_scc0 1f\n\ts_waitcnt vmcnt(" #n1 ")\n1:\n\ts_waitcnt lgkmcnt(0)\n\ts_barrier" :: "s"(X) : "memory", "scc")
#define WAITBARV(n) asm volatile("s_waitcnt vmcnt(%0)\n\ts_barrier" :: "i"(n) : "memory")
#define BAR() asm volatile("s_waitcnt lgkmcnt(0)\n\ts_barrier" ::: "memory")
DI u64 gld64(const char* ws, u32 off) { return __hip_atomic_load((GAS u64*)(ws + off), RLX); }
DI u64 gld64w(const char* ws, u32 off) {
  u64 r; asm volatile("global_load_dwordx2 %0, %1, %2 sc1\n\ts_waitcnt vmcnt(0)" : "=&v"(r) : "v"(off), "s"(ws) : "memory"); return r;
}
#define NOUNROLL _Pragma("unroll 1")
DI void gst64(char* ws, u32 off, u64 v) { __hip_atomic_store((GAS u64*)(ws + off), v, RLX); }
DI void lds_arrive(u32 a) {
  u32 va = a, one = 1;
  asm volatile("s_waitcnt lgkmcnt(0)\n\ts_mov_b64 exec, 1\n\tds_add_u32 %0, %1\n\ts_mov_b64 exec, -1" :: "v"(va), "v"(one) : "memory");
}
DI void lds_wait(u32 a, u32 n) {
  u32 va = a, t, c;
  asm volatile("1:\n\tds_read_b32 %0, %2\n\ts_waitcnt lgkmcnt(0)\n\tv_readfirstlane_b32 %1, %0\n\ts_cmp_ge_u32 %1, %3\n\ts_cbranch_scc1 2f\n\ts_sleep 1\n\ts_branch 1b\n2:"
               : "=&v"(t), "=&s"(c) : "v"(va), "s"(n) : "memory", "scc");
}
#define POLL_TXT(T, N, OFF, WS, TAG) "s_mov_b32 " N ", 0x20000\n1:\n\tglobal_load_dwordx2 " T ", " OFF ", " WS " sc1\n\ts_waitcnt vmcnt(0)\n\t" \
    "v_cmp_ne_u64 vcc, " TAG ", " T "\n\ts_cbranch_vccz 2f\n\ts_sleep 1\n\ts_sub_u32 " N ", " N ", 1\n\ts_cbranch_scc0 1b\n2:\n\t"
DI void poll_flags(const char* ws, u32 off, u64 tag) {
  u64 t; u32 n;
  asm volatile(POLL_TXT("%0", "%1", "%2", "%3", "%4") : "=&v"(t), "=&s"(n) : "v"(off), "s"(ws), "s"(tag) : "memory", "scc", "vcc");
}
DI u32 poll_gran(const char* ws, u32 off, u32 tag) {
  u64 g;
  NOUNROLL for (u32 s = 0; s < SPIN_CAP; ++s) { g = gld64w(ws, off); if (__all((u32)(g >> 32) == tag)) break; __builtin_amdgcn_s_sleep(1); }
  return (u32)g;
}
DI void store_flag(char* ws, u32 off, u64 val, u64 mask) {
  asm volatile("s_mov_b64 exec, %3\n\tglobal_store_dwordx2 %0, %1, %2 sc1\n\ts_mov_b64 exec, -1" :: "v"(off), "v"(val), "s"(ws), "s"(mask) : "memory");
}
#define PINV(x) asm volatile("" : "+v"(x))
#define PINS(x) asm volatile("" : "+s"(x))

#define UOFF 0
#define STAMP(i) do { if ((SMASK >> (i)) & 1) { SB0(); st[i] = __builtin_amdgcn_s_memrealtime(); SB0(); } } while (0)

DI void wo_offsets(u32 (&wv)[8], int wl, int lane) {
#pragma unroll
  for (int k = 0; k < 8; ++k) {
    const int n = k < 5 ? k : k < 7 ? 3 * (wl & 1) + k - 5 : (wl & 1 ? 4 : 2);
    const u32 P = 64 * n + lane, r = P / 10u, pc = P - 10 * r, g = r >> 3, j = (r & 7) ^ ((g & 1) << 2);
    wv[k] = ((k >= 5 ? 40 * g + 16 : 8 * (g + (g >> 1))) + j) * 1280 + pc * 16;
    PINV(wv[k]);
  }
}
template <int PART>
DI void stage_wo(const char* woc, int H, int wl, const u32 (&wv)[8]) {
  auto at = [&](int ks) { return H == 0 ? L_WOA + ks * 5120 : ks < 7 ? L_KVA + ks * 5120 : L_WOB7 + (ks - 7) * 5120; };
  const int n2 = 3 * (wl & 1), k2 = wl >> 1;
  const char *s0 = woc + (320 * H + 40 * wl) * 1280, *s2 = woc + (320 * H + 160 * k2) * 1280;
#define WO_D(M, V, S) "s_add_u32 m0, " M "\n\ts_nop 0\n\tglobal_load_lds_dwordx4 " V ", " S "\n\t"
#define WO_5 "s_nop 4\n\t" WO_D("%7, 0", "%0", "%5") WO_D("m0, 1024", "%1", "%5") WO_D("m0, 1024", "%2", "%5") WO_D("m0, 1024", "%3", "%5") WO_D("m0, 1024", "%4", "%5")
  if (PART != 1)
    asm volatile(WO_5 WO_D("%8, 0", "%9", "%6")
                 :: "v"(wv[0]), "v"(wv[1]), "v"(wv[2]), "v"(wv[3]), "v"(wv[4]), "s"(s0), "s"(s2), "s"(at(wl)), "s"(at(8 + k2) + (wl & 1 ? 4 : 2) * 1024), "v"(wv[7]) : "memory", "scc");
  if (PART != 0)
    asm volatile(WO_5 WO_D("%8, 0", "%9", "%6") WO_D("m0, 1024", "%10", "%6")
                 :: "v"(wv[0]), "v"(wv[1]), "v"(wv[2]), "v"(wv[3]), "v"(wv[4]), "s"(s0 + 160 * 1280), "s"(s2), "s"(at(wl + 4)), "s"(at(8 + k2) + n2 * 1024), "v"(wv[5]), "v"(wv[6]) : "memory", "scc");
}

constexpr int slice_of(int v) { return v < 6 ? v : (v >= 9 && v < 13) ? v - 3 : -1; }
constexpr int page_of(int v) { return v < 9 ? v - 6 : v - 10; }
constexpr int OP_GR = 41, OP_PF = 43;
constexpr int sched_op(int u, int i) {
  if (u == 0) return i < 3 ? 6 + i : -1;
  if (u <= 3) return i < 3 ? 3 * (u + 2) + i : 30 + u - 1;
  if (u <= 6) return i < 3 ? 3 * (u + 2) + i : -1;
  if (u == 7) return i < 3 ? 27 + i : OP_GR;
  if (u == 8) return i < 3 ? 33 + i : -1;
  if (u == 9) return i == 0 ? 39 : -1;
  if (u == 10) return i < 3 ? 36 + i : -1;
  if (u == 11) return i == 0 ? OP_PF : -1;
  return -1;
}
constexpr int younger(int op, int u) {
  int n = op < 6 ? 5 - op + 1 : 0; bool seen = op < 6;
  for (int v = 0; v < u; ++v) for (int i = 0; i < 4; ++i) { const int o = sched_op(v, i); if (o < 0) continue; if (seen) ++n; if (o == op) seen = true; }
  return n;
}
constexpr int need_op(int u) {
  return slice_of(u) >= 0 ? 3 * slice_of(u) + 2 : u == 6 ? 32 : u == 13 ? 38 : -1;
}
template <bool FULL>
DI f32x16 attn_item(s16x8 q0, s16x8 q1, u32 ka, u32 kb, u32 ko, u32 va, u32 vo, u32 vstep, const u32 (&dm)[8], const u32 (&mk)[16], int nfull, float negm, float cold) {
  f32x16 ci, co, O;
#pragma unroll
  for (int i = 0; i < 16; ++i) { ci[i] = FULL ? 0.f : negm; co[i] = FULL ? 0.f : cold; O[i] = 0.f; }
  f32x16 S[2];
  s16x8 kn0, kn1, pend, vpend;
  {
    const s16x8 kd0 = lds128(ka + 4096), ko0 = lds128(ka + ko), kd1 = lds128(kb + 4096), ko1 = lds128(kb + ko), k10 = lds128(ka + 3584), k11 = lds128(kb + 3584);
    const s16x8 v0 = ldstr(va, va + 128), vo0 = ldstr(vo, vo + 128), vo1 = ldstr(vo + 256, vo + 384), v1 = ldstr(va + 256, va + 384);
    kn0 = lds128(ka + 3072); kn1 = lds128(kb + 3072);
    SB0();
    f32x16 sd = mfma32(kd0, q0, ci), so = mfma32(ko0, q0, co);
    sd = mfma32(kd1, q1, sd); so = mfma32(ko1, q1, so);
    S[1] = mfma32(k10, q0, ci);
    SB0();
    u32 p[8], pd[8];
#pragma unroll
    for (int i = 0; i < 8; ++i) {
      p[i] = pk(__builtin_amdgcn_exp2f(bsel(so[2 * i], sd[2 * i], mk[2 * i])), __builtin_amdgcn_exp2f(bsel(so[2 * i + 1], sd[2 * i + 1], mk[2 * i + 1])));
      pd[i] = p[i] & dm[i]; p[i] ^= pd[i];
      if (i == 1) { S[1] = mfma32(k11, q1, S[1]); PINV(S[1]); SB0(); }
      if (i == 3) { O = mfma32(v0, __builtin_bit_cast(s16x8, (u32x4){pd[0], pd[1], pd[2], pd[3]}), O); PINV(O); SB0(); }
      if (i == 5) { O = mfma32(vo0, __builtin_bit_cast(s16x8, (u32x4){p[0], p[1], p[2], p[3]}), O); PINV(O); SB0(); }
    }
    O = mfma32(vo1, __builtin_bit_cast(s16x8, (u32x4){p[4], p[5], p[6], p[7]}), O);
    pend = __builtin_bit_cast(s16x8, (u32x4){pd[4], pd[5], pd[6], pd[7]}); vpend = v1;
  }
#pragma unroll
  for (int e = 1; e <= 7; ++e) {
    if (!FULL && e > nfull) break;
    const f32x16& sc = S[e & 1];
    va -= vstep;
    SB0();
    f32x16 sn = ci;
    if (e < 7) sn = mfma32(kn0, q0, ci);
    const s16x8 v0 = ldstr(va, va + 128), v1 = ldstr(va + 256, va + 384);
    u32 p[8];
    float ex[16];
#pragma unroll
    for (int i = 0; i < 4; ++i) ex[i] = __builtin_amdgcn_exp2f(sc[i]);
    SB0();
    if (e < 7) sn = mfma32(kn1, q1, sn);
    if (e < 6) { kn0 = lds128(ka + 3072 - e * 512); kn1 = lds128(kb + 3072 - e * 512); }
#pragma unroll
    for (int i = 4; i < 8; ++i) ex[i] = __builtin_amdgcn_exp2f(sc[i]);
    p[0] = pk(ex[0], ex[1]); p[1] = pk(ex[2], ex[3]);
    SB0();
    O = mfma32(vpend, pend, O); PINV(O);
    SB0();
#pragma unroll
    for (int i = 8; i < 12; ++i) ex[i] = __builtin_amdgcn_exp2f(sc[i]);
    p[2] = pk(ex[4], ex[5]); p[3] = pk(ex[6], ex[7]);
    SB0();
    O = mfma32(v0, __builtin_bit_cast(s16x8, (u32x4){p[0], p[1], p[2], p[3]}), O); PINV(O);
    SB0();
#pragma unroll
    for (int i = 12; i < 16; ++i) ex[i] = __builtin_amdgcn_exp2f(sc[i]);
    p[4] = pk(ex[8], ex[9]); p[5] = pk(ex[10], ex[11]); p[6] = pk(ex[12], ex[13]); p[7] = pk(ex[14], ex[15]);
    pend = __builtin_bit_cast(s16x8, (u32x4){p[4], p[5], p[6], p[7]}); vpend = v1;
    S[(e + 1) & 1] = sn;
    SB0();
  }
  return mfma32(vpend, pend, O);
}
DI float attn_negmax(s16x8 q0, s16x8 q1, u32 ka, u32 kb, int nfull, bool has_old, int c, int hh) {
  f32x16 z;
#pragma unroll
  for (int i = 0; i < 16; ++i) z[i] = 0.f;
  float m = -__builtin_inff();
  NOUNROLL for (int e = 0; e <= nfull + (has_old ? 1 : 0); ++e) {
    const f32x16 s = mfma32(lds128(kb + 4096 - e * 512), q1, mfma32(lds128(ka + 4096 - e * 512), q0, z));
    int lo = (e == 8 ? c + 1 : 0) - 4 * hh, n = (e == 0 ? c : 31) - 4 * hh - lo;
    PINV(lo); PINV(n);
#pragma unroll
    for (int i = 0; i < 16; ++i) { const int k = (i & 3) + 8 * (i >> 2) - lo; m = fmaxf(m, (k >= 0 && k <= n) ? s[i] : m); }
  }
  auto r = __builtin_amdgcn_permlane32_swap(__float_as_uint(m), __float_as_uint(m), false, false);
  return -fmaxf(__uint_as_float(r[0]), __uint_as_float(r[1]));
}

DI void row_stats(u32x4 xa, u32 xb, u32x4 wa, u32 wb, char* ws, int srow, u32 tag, int tid) {
  const int lane = tid & 63;
  const u32 xu[5] = {xa[0], xa[1], xa[2], xa[3], xb}, wu5[5] = {wa[0], wa[1], wa[2], wa[3], wb};
  f32x2 v[5], s2 = {0.f, 0.f};
#pragma unroll
  for (int i = 0; i < 5; ++i) { v[i] = (f32x2){blo(xu[i]), bhi(xu[i])}; s2 += v[i]; }
  const float mean = wsum(s2[0] + s2[1]) * (1.f / 640.f), mb = rbf(mean);
  f32x2 q2 = {0.f, 0.f};
  float r = 0.f;
#pragma unroll
  for (int i = 0; i < 5; ++i) {
    const f32x2 d = v[i] - mean, e = v[i] - mb;
    q2 += d * d;
    r = __builtin_amdgcn_fdot2_f32_bf16(asb(pk(e[0], e[1])), asb(wu5[i]), r, false);
  }
  float q = q2[0] + q2[1];
  q += dppf<0xB1>(q); r += dppf<0xB1>(r); q += dppf<0x4E>(q); r += dppf<0x4E>(r);
  q += dppf<0x141>(q); r += dppf<0x141>(r); q += dppf<0x140>(q); r += dppf<0x140>(r);
  { auto t = __builtin_amdgcn_permlane16_swap(__float_as_uint(q), __float_as_uint(r), false, false); q = __uint_as_float(t[0]) + __uint_as_float(t[1]); }
  q = x32sum(q);
  r = __uint_as_float(__builtin_amdgcn_readlane(__float_as_uint(q), 16));
  q = __uint_as_float(__builtin_amdgcn_readlane(__float_as_uint(q), 0));
  const float sd = rbf(__builtin_amdgcn_sqrtf(rbf(rbf(q * (1.f / 640.f)) + 1e-5f)));
  const u32 pay = lane == 0 ? ((__float_as_uint(mb) >> 16) | __float_as_uint(sd)) : __float_as_uint(r * __builtin_amdgcn_rcpf(sd));
  store_flag(ws, (lane == 0 ? WS_STATS : WS_RR) + srow * 8, ((u64)tag << 32) | pay, 3);
  if (tid < 16) *lp<u32>(L_CNT + tid * 4) = 0;
  if (tid < 144) *lp<u32>(L_ONE + tid * 4) = 0x3F803F80u;
}

__global__ void __launch_bounds__(512, 2)
fused_block(const u16* __restrict__ x, const u16* __restrict__ wqkv, const u16* __restrict__ wr, char* ws,
            const u16* wo_, const u16* wg_, const u16* wu_, const u16* wd_, u16* out_) {
  (void)wo_; (void)wg_; (void)wu_; (void)wd_; (void)out_;
  auto karg = [](int i) { const __attribute__((address_space(4))) u64* k = (const __attribute__((address_space(4))) u64*)__builtin_amdgcn_kernarg_segment_ptr(); PINS(k); return (char*)k[i]; };
  extern __shared__ __attribute__((aligned(16))) char smem[];
  (void)smem;
  u64 st[32];
  for (int i = 0; i < 32; ++i) st[i] = 0;
  STAMP(0);
  const u64 cyc0 = __builtin_amdgcn_s_memtime();
  const int b = blockIdx.x, xc = b & 7, bi = b >> 3;
  const int tid = threadIdx.x, lane = tid & 63, w = uni(tid >> 6);
  const int c = lane & 31, hh = lane >> 5;
  const bool helper = bi >= 30;

  if (w == 7) {
    u64 pc; asm volatile("s_getpc_b64 %0" : "=s"(pc));
    constexpr int PER = (CODE_LINES + 31) / 32;
    u32 ln = bi * PER + (lane % PER); ln = ln < CODE_LINES ? ln : CODE_LINES - 1;
    asm volatile("s_mov_b32 m0, %0\n\ts_nop 0\n\tglobal_load_lds_dword %1, %2" :: "s"(L_PF), "v"(ln * 128), "s"(pc & ~127ull) : "memory");
  }
  const int srow = b * 8 + w;
#define ROW_LOADS() u32x4 xa, wa; u32 xb, wb; \
    asm volatile("s_nop 4\n\tglobal_load_dwordx4 %0, %5, %6\n\tglobal_load_dword %1, %5, %6 offset:16\n\t" \
                 "global_load_dwordx4 %2, %5, %7\n\tglobal_load_dword %3, %5, %7 offset:16\n\ts_load_dwordx2 %4, %8, 0x0" \
                 : "=&v"(xa), "=&v"(xb), "=&v"(wa), "=&v"(wb), "=&s"(tag64) : "v"(lane * 20), "s"(x + srow * 640), "s"(wr), "s"(ws) : "memory")
#define ROW_STATS(n) \
    asm volatile("s_waitcnt vmcnt(" #n ") lgkmcnt(0)" : "+v"(xa), "+v"(xb), "+v"(wa), "+v"(wb), "+s"(tag64) :: "memory"); \
    tag64 = ((u64)uni((u32)((tag64 + 1) >> 32)) << 32) | uni((u32)tag64 + 1); tag = (u32)tag64; \
    row_stats(xa, xb, wa, wb, ws, srow, tag, tid)
  u64 tag64; u32 tag;
  const __amdgpu_buffer_rsrc_t wsr = __builtin_amdgcn_make_buffer_rsrc(ws, 0, WS_SPAN, 0x00020000);
  const int X = w >> 2, wl = w & 3;

  u64 sgr = 0;
#define RES_PREP(bb) u32 xo_[4]; const char* xs_; \
    { const int row_ = 64 * (((bb) & 7) + 8 * ((bb) >> 6)) + 16 * wl; \
      _Pragma("unroll") for (int n_ = 0; n_ < 3; ++n_) { const u32 P_ = 64 * n_ + lane, r_ = P_ / 10u; xo_[n_] = (r_ < 16 ? r_ : 15) * 1280 + (P_ - 10 * r_) * 16; PINV(xo_[n_]); } \
      xo_[3] = X ? WS_STATS + (row_ + (lane & 15)) * 8 : WS_TOP2 + ((bb) & 15) * 128; PINV(xo_[3]); \
      xs_ = karg(0) + (u32)(row_ * 1280 + (((bb) >> 3) & 7) * 160); PINS(xs_); }
#define RES_ISSUE() asm volatile("s_cmp_eq_u32 %8, 0\n\tglobal_load_dwordx2 %0, %1, %2 sc1\n\ts_cbranch_scc1 1f\n\t" \
                 WO_D("%7, 0", "%3", "%6") WO_D("m0, 1024", "%4", "%6") WO_D("m0, 1024", "%5", "%6") "\n1:" \
                 : "=&v"(sgr) : "v"(xo_[3]), "s"(ws), "v"(xo_[0]), "v"(xo_[1]), "v"(xo_[2]), "s"(xs_), "s"(L_XR + wl * 3072), "s"(X) : "memory", "scc")

  if (!helper) {
    ROW_LOADS();
    const int Lx = (xc >> 1) * 30 + bi, uj = Lx >> 3, rtile = (xc & 1) * 8 + (Lx & 7);
    const int uA = (uj / 5) * 10 + uj % 5;
    const int rot = (rtile * 3 + uj) % 10;
    const u32 vx = (8 * w + (lane >> 3)) * 1280 + (((lane & 7) ^ ((4 * w + (lane >> 4)) & 7)) << 4);
    const u32 vw = (8 * w + (lane >> 3)) * 3840 + (((lane & 7) ^ ((lane >> 2) & 4)) << 4);
    const char* xbase = (const char*)x + rtile * (128 * 1280);
    const char* wbase = (const char*)wqkv + uA * 128;
    auto piece = [&](int op) {
      const int a = op / 3, p = op % 3, pg = op - 30;
      int sl = (op < 30 ? a : pg) + rot; sl = sl >= 10 ? sl - 10 : sl;
      if (op >= 30) dma16(L_BP + (pg == 9 ? 6 : pg % 6) * 8192 + w * 1024, vw, wbase + 640 + sl * (64 * 3840));
      else if (p < 2) dma16((a % 4) * SLOT + p * 8192 + w * 1024, vx, xbase + sl * 128 + p * (64 * 1280));
      else dma16((a % 4) * SLOT + 16384 + w * 1024, vw, wbase + sl * (64 * 3840));
    };
    piece(0); piece(1); piece(2); piece(3); piece(4); piece(5);
    const int tt = wl, fh = X;
    const u32 key = (c >> 1) & 7;
    u32 xad[2][4];
#pragma unroll
    for (int i = 0; i < 4; ++i) { xad[0][i] = (32 * tt + c) * 128 + (((2 * i + hh) ^ key) << 4); xad[1][i] = xad[0][i] + 2 * SLOT; PINV(xad[0][i]); PINV(xad[1][i]); }
    const u32 q4 = (lane >> 2) & 3, p4 = lane & 3, g2 = (lane >> 4) & 1;
    u32 wadA = (8 * hh + q4) * 128 + (((4 * fh + 2 * (p4 & 1) + g2) ^ ((q4 >> 1) << 2)) << 4) + (p4 >> 1) * 8, wadB = wadA + L_BP, wadC = wadA + 2 * SLOT;
    const char* pfa;
    {
      const u32 l0 = (bi * 8 + w) * 38 + (lane < 37 ? lane : 37), l = l0 < 8960 ? l0 : 8959, m = l / 1920u, g = l - 1920 * m, r = g / 3u;
      const char *wg = karg(5), *wu = karg(6), *wd = karg(7), *wo = karg(4);
      pfa = m >= 3 ? wo + (l - 5760) * 128 : m == 2 ? wd + xc * (192 * 1280) + g * 128 : (m ? wu : wg) + r * 3072 + xc * 384 + (g - 3 * r) * 128;
    }
    u32 goff = WS_STATS + (rtile * 128 + 32 * tt + c) * 8;
    u32 wadT = (32 * X + 8 * (lane >> 4) + q4) * 128 + (((2 * wl + (p4 >> 1)) ^ ((q4 >> 1) << 2)) << 4) + (p4 & 1) * 8;
    u32 wadV = wadT + L_BP; wadT += 16384;
    u32 wadU = wadT + 2 * SLOT;
    u32 qoff = WS_QKV + (8 * uA + 4 * fh + 2 * hh) * 32768 + (rtile * 128 + 32 * tt + c) * 16;
    u32 csl = L_CP + (32 * fh + c) * 4, cswr = L_CP + X * 256 + (16 * wl + (lane & 15)) * 4;
    u32 csm = L_CP + 1024 + w * 128 + c * 4, csr = L_CP + 1024 + w * 128 + hh * 64;
    u32x4 onev = {0x3F803F80u, 0x3F803F80u, 0x3F803F80u, 0x3F803F80u};
    PINV(wadA); PINV(wadB); PINV(wadC); PINV(wadT); PINV(wadU); PINV(wadV); PINV(pfa); PINV(goff); PINV(qoff); PINV(csl); PINV(cswr); PINV(csm); PINV(csr); PINV(onev);
    u32 wv[8];
    wo_offsets(wv, wl, lane);
    ROW_STATS(6);
    if ((P1X & 8) && X == 1) __builtin_amdgcn_s_setprio(1);
    SB0();
    s16x8 xf[40], wf[2][4], wt[8], cf, cg;
    const s16x8 ones = __builtin_bit_cast(s16x8, onev);
    f32x16 acc, acb;
    f32x4 acs = {0.f, 0.f, 0.f, 0.f}, act = acs;
#pragma unroll
    for (int i = 0; i < 16; ++i) { acc[i] = 0.f; acb[i] = 0.f; }
    u64 gr = 0;
    float mneg = 0.f, rsq = 0.f;
    u32 qscb = uA < 10 ? 0x3ea52b5cu : 0x3f800000u;
    PINS(qscb);
    u32 eo[8];
    f32x4 cp[4];
    float c0 = 0.f, c1 = 0.f;
    auto epi2 = [&](float a0, float a1, float k0, float k1) {
      u32 r; float t;
      asm("v_fma_f32 %0, %2, %4, %6\n\tv_fma_f32 %1, %2, %5, %7\n\tv_mul_f32 %0, %3, %0\n\tv_mul_f32 %1, %3, %1\n\tv_cvt_pk_bf16_f32 %0, %0, %1"
          : "=&v"(r), "=&v"(t) : "v"(mneg), "v"(rsq), "v"(k0), "v"(k1), "v"(a0), "v"(a1));
      return r;
    };
    auto epi_store = [&](int second, int k) {
      __builtin_amdgcn_raw_buffer_store_b128((u32x4){eo[4 * k], eo[4 * k + 1], eo[4 * k + 2], eo[4 * k + 3]}, wsr, qoff + k * 32768 + second * (40 * 32768), 0, 16);
    };
    static_for<0, 21>([&](auto uc) __attribute__((always_inline)) {
      constexpr int u = decltype(uc)::v;
      constexpr int sa = slice_of(u), sb = page_of(u), pa = slice_of(u - 1), pb = page_of(u - 1);
      constexpr int G = u < 7 ? u : u < 9 ? -1 : u < 14 ? u - 2 : u == 16 ? 12 : u == 19 ? 13 : -1;
      if constexpr (u < 20 && need_op(u) >= 0) WAITBARV(younger(need_op(u), u));
      if (u == 7) asm volatile("s_barrier" ::: "memory");
      if (u == 15) asm volatile("s_waitcnt lgkmcnt(6)\n\ts_barrier" ::: "memory");
      if (u == 19) asm volatile("s_waitcnt lgkmcnt(4)\n\ts_barrier" ::: "memory");
      if (G == 11) {
        PINV(gr);
        u32 pay = (u32)gr;
        if (!P1X && !__all((u32)(gr >> 32) == tag)) pay = poll_gran(ws, goff, tag);
        mneg = -blo(pay); rsq = __builtin_amdgcn_rcpf(bhi(pay)) * __uint_as_float(qscb); PINV(mneg); PINV(rsq);
      }
      if (u >= UOFF && u < UOFF + 16) STAMP(16 + (u >= UOFF && u < UOFF + 16 ? u - UOFF : 0));
      SB0();
#pragma unroll
      for (int i = 0; i < 4; ++i) {
        if constexpr (u > 0) {
          if constexpr (pa >= 0) acc = mfma32(wf[(u - 1) & 1][i], xf[4 * pa + i], acc);
          else acb = mfma32(u > 13 ? wt[(4 * (u - 14) + i) & 7] : wf[(u - 1) & 1][i], xf[4 * pb + i], acb);
        }
        SB0();
        if constexpr (u < 20) {
          if constexpr (sa >= 0) {
            const u32 wbs = (sa & 2 ? wadC : wadA) + (sa & 1) * SLOT + 16384 + i * 2048;
            xf[4 * sa + i] = lds128(xad[(sa >> 1) & 1][i] + (sa & 1) * SLOT); wf[u & 1][i] = ldstr(wbs, wbs + 512);
          } else if constexpr (u < 13) { const u32 pbase = wadB + sb * 8192 + i * 2048; wf[u & 1][i] = ldstr(pbase, pbase + 512); }
          else {
            auto rd = [&](int f) { const int pn = 3 + f / 4; const u32 pbase = wadB + (pn == 9 ? 6 : pn % 6) * 8192 + (f & 3) * 2048; wt[f & 7] = ldstr(pbase, pbase + 512); };
            if constexpr (u == 13) { rd(i == 0 ? 0 : i == 1 ? 2 : i == 2 ? 3 : 5); if (!(i & 1)) rd(i == 0 ? 1 : 4); }
            else if (4 * (u - 14) + i + 6 < 28) rd(4 * (u - 14) + i + 6);
          }
        }
        if (i == 3) {
          if constexpr (u > 0 && pa >= 0) acs = mfma16(ones, cf, acs);
          if constexpr ((u >= 7 && u <= 9) || (u >= 14 && u <= 17)) act = mfma16(ones, cf, act);
          if constexpr (sa >= 0) { const u32 cb = (sa & 2 ? wadU : wadT) + (sa & 1) * SLOT; cf = ldstr(cb, cb + 512); }
          if constexpr (u >= 6 && u <= 8) cf = ldstr(wadV + sb * 8192, wadV + sb * 8192 + 512);
          if constexpr (u >= 13 && u <= 16) { constexpr int pn = 3 + 2 * (u - 13); const u32 cb = wadV + (pn == 9 ? 6 : pn % 6) * 8192; cf = ldstr(cb, cb + 512); }
        }
        if (i == 1) {
          if constexpr (u >= 14 && u <= 16) act = mfma16(ones, cg, act);
          if constexpr (u >= 13 && u <= 15) { constexpr int pn = 4 + 2 * (u - 13); const u32 cb = wadV + (pn % 6) * 8192; cg = ldstr(cb, cb + 512); }
        }
        const int op = sched_op(u, i);
        if (op >= 0 && op < 40) piece(op);
        if (op == OP_GR) asm volatile("global_load_dwordx2 %0, %1, %2 sc1" : "=v"(gr) : "v"(goff), "s"(ws) : "memory");
        if (op == OP_PF) asm volatile("s_mov_b32 m0, %0\n\ts_nop 0\n\tglobal_load_lds_dword %1, off" :: "s"(L_PF), "v"(pfa) : "memory");
        if constexpr (u == 14 || u == 18) { if (i == 1) *lp<float>(cswr + (u == 14 ? 0 : 512)) = u == 14 ? acs[0] : act[0]; }
        if constexpr (u == 15 || u == 19) {
          constexpr u32 o = u == 15 ? 0 : 512;
          if (i == 0) { c0 = *lp<float>(csl + o); c1 = *lp<float>(csl + o + 256); }
          if (i == 2) *lp<float>(csm) = c0 + c1;
          if (i == 3) {
#pragma unroll
            for (int k = 0; k < 4; ++k) cp[k] = *lp<f32x4>(csr + 16 * k);
          }
        }
        if constexpr (u == 16 || u == 17) {
          constexpr int q = 2 * (u - 16);
          eo[2 * q + i] = epi2(acc[4 * q + 2 * i], acc[4 * q + 2 * i + 1], cp[q + (i >> 1)][2 * (i & 1)], cp[q + (i >> 1)][2 * (i & 1) + 1]);
          if (i == 3) epi_store(0, u - 16);
        }
        SB0();
      }
    });
    STAMP(4);
    u32 fl = WS_QFLAG + (rtile * 30 + uA) * 128; u64 tagv = tag64;
    PINV(fl); PINV(tagv);
#pragma unroll
    for (int i = 0; i < 4; ++i) {
      eo[2 * i]     = pk(fmaf(mneg, cp[i][0], acb[4 * i]) * rsq, fmaf(mneg, cp[i][1], acb[4 * i + 1]) * rsq);
      eo[2 * i + 1] = pk(fmaf(mneg, cp[i][2], acb[4 * i + 2]) * rsq, fmaf(mneg, cp[i][3], acb[4 * i + 3]) * rsq);
      if (i & 1) epi_store(1, i >> 1);
    }
    SB0();
    STAMP(3);
    WAITBAR(2);
    if (w == 0) store_flag(ws, fl, tagv, 1);
    const char* woc = karg(4) + (bi & 7) * 160; PINS(woc);
    if (X == 1) stage_wo<0>(woc, 0, wl, wv);
    else {
      const u32 l = (bi * 4 + wl) * 27 + (lane < 26 ? lane : 26);
      asm volatile("s_mov_b32 m0, %0\n\ts_nop 4\n\tglobal_load_lds_dword %1, %2" :: "s"(L_PF), "v"((3200 + (l < 3200 ? l : 3199)) * 128), "s"(woc - (bi & 7) * 160) : "memory");
    }
    {
    int b2 = blockIdx.x; PINS(b2);
    const int xc = b2 & 7, bi = b2 >> 3;
    const int odd = bi >= 15, hl = 2 * xc + odd, grp = bi - 15 * odd, head = 16 * X + hl;
    const int oc0 = 5 * (8 * X + xc);
    const int oA = oc0 + 3 * odd, oG = oc0 + 2;
    const u32 kv = X == 0 ? L_KVA : L_KVB;
    const int g0 = grp == 0;
    u32 pollo;
    {
      const int l4 = lane & 15, isq = (l4 + 4) >> 4, b1 = (l4 >> 1) & 1, ri = l4 >> 2;
      const int oct = oc0 + 2 * odd + 2 * (l4 & 1) + (80 + 80 * b1) * (1 - isq);
      const int rt = g0 ? (isq ? b1 : (ri + 1) >> 1) : grp - 1 + ri - isq;
      pollo = WS_QFLAG + (rt * 30 + (oct >> 3)) * 128;
    }
    const int t0 = g0 ? wl : 4 * (grp + 1) + wl, t1 = g0 ? 7 - wl : t0;
    u32 qo0 = WS_QKV + (oA + hh) * 32768 + c * 16 + t0 * 512, qo1 = WS_QKV + oG * 32768 + c * 16 + t0 * 512, l16 = lane * 16;
    u32 qd = (t1 - t0) * 512;
    const int tp0 = (3 - g0) * (wl >> 1);
    const char* kvs = ws + (u32)(WS_QKV + 80 * (1 + (wl & 1)) * 32768 + ((g0 ? 0 : 128 * (grp - 1)) + 64 * tp0) * 16);
    const char* sA = kvs + (u32)(oA * 32768); const char* sB = sA + 32768; const char* sG = kvs + (u32)(oG * 32768);
    u32 mA = kv + 3 * (wl & 1) * PLANE + tp0 * 1024;
    RES_PREP(b2);
    fl += 5 * 128;
    PINV(pollo); PINV(qo0); PINV(qo1); PINV(l16); PINV(fl); PINS(sA); PINS(sB); PINS(sG); PINS(mA); PINS(qd);
    asm volatile("s_waitcnt vmcnt(6) lgkmcnt(0)\n\ts_cmp_ge_u32 %0, 4\n\ts_cbranch_scc1 1f\n\ts_waitcnt vmcnt(2)\n\ts_cmp_eq_u32 %0, 0\n\ts_cbranch_scc1 1f\n\ts_waitcnt vmcnt(1)\n1:\n\ts_barrier"
                 :: "s"(w) : "memory", "scc");
    if (w == 4) store_flag(ws, fl, tagv, 1);
    STAMP(5);
    RES_ISSUE();
    if (X == 1) stage_wo<1>(woc, 0, wl, wv);
    __builtin_amdgcn_s_setprio(1);
    u32x4 qa, qb, qa2, qb2;
    {
      u64 pt; u32 pn;
      asm volatile(POLL_TXT("%4", "%5", "%6", "%7", "%8")
                   "s_memrealtime %17\n\t"
                   "s_mov_b32 m0, %9\n\tglobal_load_dwordx4 %0, %14, %7 sc1\n\tglobal_load_lds_dwordx4 %13, %10 sc1\n\tglobal_load_lds_dwordx4 %13, %10 offset:1024 sc1\n\t"
                   "s_add_u32 m0, %9, 6208\n\tglobal_load_dwordx4 %1, %15, %7 sc1\n\tglobal_load_lds_dwordx4 %13, %11 sc1\n\tglobal_load_lds_dwordx4 %13, %11 offset:1024 sc1\n\t"
                   "s_add_u32 m0, %9, 12416\n\ts_cmp_lg_u32 %16, 0\n\tglobal_load_lds_dwordx4 %13, %12 sc1\n\tglobal_load_lds_dwordx4 %13, %12 offset:1024 sc1\n\t"
                   "s_cbranch_scc1 3f\n\t"
                   "global_load_lds_dwordx4 %13, %12 offset:2048 sc1\n\ts_mov_b32 m0, %9\n\ts_nop 0\n\tglobal_load_lds_dwordx4 %13, %10 offset:2048 sc1\n\t"
                   "s_add_u32 m0, %9, 6208\n\ts_nop 0\n\tglobal_load_lds_dwordx4 %13, %11 offset:2048 sc1\n\ts_branch 4f\n"
                   "3:\n\tv_add_u32 %14, %16, %14\n\tv_add_u32 %15, %16, %15\n\tglobal_load_dwordx4 %2, %14, %7 sc1\n\tglobal_load_dwordx4 %3, %15, %7 sc1\n"
                   "4:"
                   "\n\ts_waitcnt lgkmcnt(0)"
                   : "=&v"(qa), "=&v"(qb), "=&v"(qa2), "=&v"(qb2), "=&v"(pt), "=&s"(pn), "+v"(pollo), "+s"(ws), "+s"(tag64), "+s"(mA), "+s"(sA), "+s"(sB), "+s"(sG),
                     "+v"(l16), "+v"(qo0), "+v"(qo1), "+s"(qd)
                     , "=&s"(st[6])
                   :: "memory", "scc", "vcc");
    }
    u32 mk[16], dm[8];
    {
      int d = 4 * hh - 1 - c;
#pragma unroll
      for (int i = 0; i < 16; ++i) asm("v_add_u32 %0, %2, %1\n\tv_ashrrev_i32 %0, 31, %0" : "=&v"(mk[i]) : "v"(d), "i"((i & 3) + 8 * (i >> 2)));
    }
#pragma unroll
    for (int i = 0; i < 8; ++i) dm[i] = (mk[2 * i] & 0xffffu) | (mk[2 * i + 1] & 0xffff0000u);
    u32 hmask = (u32)-hh, kml = (u32)((hh | odd) - 1), kmh = (u32)((hh | (odd ^ 1)) - 1);
    const u32 realv = (u32)(((g2 & ((p4 | (p4 >> 1)) & 1))) - 1);
    u32 vlane = hh * 64 + q4 * 16 + ((realv & (kv + (3 + 2 * g2 + (p4 & 1)) * PLANE + (g2 * odd + (p4 >> 1)) * 8)) | (~realv & L_ONE));
    u32 vstep = realv & 512u;
    const int dl0 = g0 ? 0 : 8 + wl;
    u32 vold = vlane + (realv & (u32)(wl * 512));
    vlane += realv & (u32)(dl0 * 512);
    u32 kla = kv + hh * PLANE + c * 16 + (dl0 - 8) * 512, klb = kv + 2 * PLANE + c * 16 + (dl0 - 8) * 512;
    u32 oo = WS_O + X * (1u << 20) + t0 * 16384 + (c >> 4) * 8192 + xc * 1024 + (2 * odd + hh) * 256 + (c & 15) * 16;
    u32 co4 = (WS_C4 + X * (1u << 18) + t0 * 4096 + (c >> 4) * 2048 + hl * 128 + (c & 15) * 8) | (hmask & 0x80000000u);
    u32 ofl = WS_OFLAG + (t0 * 32 + head) * 128 + (lane & 1) * (qd * 8);
    u32 cnta = L_CNT + 4 * X, one = 1, tmpv, tmps;
    asm volatile("s_waitcnt vmcnt(0)\n\ts_mov_b64 exec, 1\n\tds_add_u32 %2, %3\n\ts_mov_b64 exec, -1\n"
                 "5:\n\tds_read_b32 %0, %2\n\ts_waitcnt lgkmcnt(0)\n\tv_readfirstlane_b32 %1, %0\n\ts_cmp_ge_u32 %1, 4\n\ts_cbranch_scc0 5b"
                 : "=&v"(tmpv), "=&s"(tmps), "+v"(cnta), "+v"(one), "+v"(qa), "+v"(qb), "+v"(qa2), "+v"(qb2), "+v"(sgr),
                   "+v"(kml), "+v"(kmh), "+v"(hmask), "+v"(vlane), "+v"(vold), "+v"(vstep), "+v"(kla), "+v"(klb), "+v"(oo), "+v"(co4), "+v"(ofl),
                   "+v"(dm[0]), "+v"(dm[1]), "+v"(dm[2]), "+v"(dm[3]), "+v"(dm[4]), "+v"(dm[5]), "+v"(dm[6]), "+v"(dm[7])
                 :: "memory", "scc");
    STAMP(7);
    {
      auto in_range = [&](const f32x16& O) {
        const float l = bsel(O[12], O[8], hmask);
        const float t = ((O[0] + O[1]) + (O[2] + O[3])) + ((O[4] + O[5]) + (O[6] + O[7])) + ((O[8] + O[9]) + (O[10] + O[11]));
        return __all(l > 0x1p-40f && l < 0x1p40f && __builtin_fabsf(t) < __builtin_inff());
      };
      auto put = [&](const f32x16& O, u32 ao, u32 ac) {
        const float inv = __builtin_amdgcn_rcpf(bsel(O[12], O[8], hmask));
        __builtin_amdgcn_raw_buffer_store_b128((u32x4){pk(O[0] * inv, O[1] * inv), pk(O[2] * inv, O[3] * inv), pk(O[4] * inv, O[5] * inv), pk(O[6] * inv, O[7] * inv)}, wsr, ao, 0, 16);
        __builtin_amdgcn_raw_buffer_store_b64((u32x2){pk(O[8] * inv, O[9] * inv), pk(O[10] * inv, O[11] * inv)}, wsr, ac, 0, 16);
      };
      bool general = ALWAYS_MAX || g0;
      if (!general) {
        const f32x16 O = attn_item<true>(__builtin_bit_cast(s16x8, qa), __builtin_bit_cast(s16x8, (u32x4){qb[0] & kml, qb[1] & kml, qb[2] & kmh, qb[3] & kmh}),
                                         kla, klb, 0, vlane, vold, vstep, dm, mk, 7, 0.f, 0.f);
        STAMP(12);
        if (in_range(O)) put(O, oo, co4); else general = true;
      }
      if (general) {
        int cc = c, hc = hh; PINV(cc); PINV(hc);
        NOUNROLL for (int it = 0; it <= g0; ++it) {
          const s16x8 q0 = __builtin_bit_cast(s16x8, qa);
          const s16x8 q1 = __builtin_bit_cast(s16x8, (u32x4){qb[0] & kml, qb[1] & kml, qb[2] & kmh, qb[3] & kmh});
          const int tq = it ? t1 : t0, nfull = g0 ? tq : 7;
          const u32 kd = g0 ? tq * 512 : 0, ka = kla + kd, kb = klb + kd, va = vlane + (vstep ? kd : 0u);
          bool shift = ALWAYS_MAX || !g0;
          f32x16 O;
          NOUNROLL for (;;) {
            const float negm = shift ? attn_negmax(q0, q1, ka, kb, nfull, !g0, cc, hc) : 0.f;
            O = attn_item<false>(q0, q1, ka, kb, g0 ? 4096u : 0u, va, g0 ? va : vold, vstep, dm, mk, nfull, negm, g0 ? -__builtin_inff() : negm);
            if (shift || in_range(O)) break;
            shift = true;
          }
          put(O, oo + (tq - t0) * 16384, co4 + (tq - t0) * 4096);
          qa = qa2; qb = qb2;
        }
      }
    }
    STAMP(8);
    WAITV(0);
    store_flag(ws, ofl, tagv, g0 ? 3 : 1);
    STAMP(9);
    if (X == 0) {
      lds_arrive(L_CNT + 8);
      __builtin_amdgcn_s_setprio(0);
      lds_wait(L_CNT + 8, 4);
      stage_wo<2>(woc, 1, wl, wv);
    } else {
      lds_arrive(L_CNT + 20);
    }
    }
  } else {
    ROW_LOADS();
    const int hg = xc * 2 + (bi - 30);
    const char *wg = karg(5), *wu = karg(6), *wd = karg(7);
    u32x4 hv[40];
    const int pc = (lane & 15) - (hg & 1) * 4, rg = lane >> 4;
    const bool act = pc >= 0 && pc < 12;
    const u32 gvo = (8 * w + 2 * rg) * 3072 + hg * 192 + (act ? pc : 0) * 16;
#define GU_ISSUE(n, r) asm volatile("global_load_dwordx4 %0, %4, %5\n\tglobal_load_dwordx4 %1, %4, %5 offset:3072\n\t" \
                       "global_load_dwordx4 %2, %4, %6\n\tglobal_load_dwordx4 %3, %4, %6 offset:3072" \
                       : "=&v"(hv[4 * (r)]), "=&v"(hv[4 * (r) + 1]), "=&v"(hv[4 * (r) + 2]), "=&v"(hv[4 * (r) + 3]) \
                       : "v"(gvo), "s"((const char*)wg + (n) * (64 * 3072)), "s"((const char*)wu + (n) * (64 * 3072)) : "memory")
    GU_ISSUE(0, 0); GU_ISSUE(1, 1);
    ROW_STATS(8);
    GU_ISSUE(2, 2); GU_ISSUE(3, 3); GU_ISSUE(4, 4);
    u32x4 ra, rb;
    const u32 roff = WS_RR + tid * 32;
    asm volatile("global_load_dwordx4 %0, %2, %3 sc1\n\tglobal_load_dwordx4 %1, %2, %3 offset:16 sc1" : "=&v"(ra), "=&v"(rb) : "v"(roff), "s"(ws) : "memory");
    STAMP(2);
    if (w >= 2) { GU_ISSUE(5, 5); }
    asm volatile("s_waitcnt vmcnt(4)\n\ts_cmp_ge_u32 %2, 2\n\ts_cbranch_scc1 1f\n\ts_waitcnt vmcnt(0)\n1:" : "+v"(ra), "+v"(rb) : "s"(w) : "memory", "scc");
    NOUNROLL for (u32 sp = 0; sp < SPIN_CAP && !__all(ra[1] == tag && ra[3] == tag && rb[1] == tag && rb[3] == tag); ++sp) {
      __builtin_amdgcn_s_sleep(1);
      asm volatile("global_load_dwordx4 %0, %2, %3 sc1\n\tglobal_load_dwordx4 %1, %2, %3 offset:16 sc1\n\ts_waitcnt vmcnt(0)" : "=&v"(ra), "=&v"(rb) : "v"(roff), "s"(ws) : "memory");
    }
    auto mkey = [](u32 f, u32 row) { return ((f ^ (((int)f >> 31) | 0x80000000u)) & 0xfffff800u) | (2047u - row); };
    auto mx = [](u32 p, u32 q) { return p > q ? p : q; };
    auto mn = [](u32 p, u32 q) { return p < q ? p : q; };
    u32 k1, k2;
    {
      const u32 e0 = mkey(ra[0], 4 * tid), e1 = mkey(ra[2], 4 * tid + 1), e2 = mkey(rb[0], 4 * tid + 2), e3 = mkey(rb[2], 4 * tid + 3);
      k1 = mx(mx(e0, e1), mx(e2, e3));
      k2 = mx(mn(mx(e0, e1), mx(e2, e3)), mx(mn(e0, e1), mn(e2, e3)));
    }
#define TOP2_STEP(o1, o2) do { const u32 p_ = (o1), q_ = (o2); k2 = mx(mn(k1, p_), mx(k2, q_)); k1 = mx(k1, p_); } while (0)
#define TOP2_DPP(C) TOP2_STEP(__builtin_amdgcn_mov_dpp(k1, C, 0xf, 0xf, true), __builtin_amdgcn_mov_dpp(k2, C, 0xf, 0xf, true))
    TOP2_DPP(0xB1); TOP2_DPP(0x4E); TOP2_DPP(0x141); TOP2_DPP(0x140);
    LAS u32x2* kk = lp<u32x2>(4096);
    if ((lane & 15) == 0) kk[4 * w + rg] = (u32x2){k1, k2};
    BAR();
    { const u32x2 e = kk[lane & 15], f = kk[16 + (lane & 15)]; k1 = e[0]; k2 = e[1]; TOP2_STEP(f[0], f[1]); }
    TOP2_DPP(0xB1); TOP2_DPP(0x4E); TOP2_DPP(0x141); TOP2_DPP(0x140);
    k1 = uni(k1); k2 = uni(k2);
    if (w >= 2) { GU_ISSUE(6, 6); GU_ISSUE(7, 7); }
    const u32 i0 = 2047u - (k1 & 2047u), i1 = 2047u - (k2 & 2047u);
    if (tid == 0) { gst64(ws, WS_TOP2 + hg * 128, ((u64)tag << 32) | i0 | (i1 << 16)); if (hg == 15) gst64(ws, WS_EPOCH, tag64); }
    STAMP(3);
    LAS u32* xp = lp<u32>(8192);
    if (w < 2) {
      const u32 row = w ? i1 : i0;
      u32 xrow[5];
#pragma unroll
      for (int i = 0; i < 5; ++i) xrow[i] = ((const u32*)x)[row * 320 + i * 64 + lane];
      const u32 sg = poll_gran(ws, WS_STATS + row * 8, tag);
      const float mb = blo(sg), rs = __builtin_amdgcn_rcpf(bhi(sg));
#pragma unroll
      for (int i = 0; i < 5; ++i) {
        const u32 t = pk(blo(xrow[i]) - mb, bhi(xrow[i]) - mb);
        xp[(i * 64 + lane) * 2 + w] = pk(blo(t) * rs, bhi(t) * rs);
      }
      GU_ISSUE(5, 5); GU_ISSUE(6, 6); GU_ISSUE(7, 7);
    }
    BAR();
    float ga[32];
#pragma unroll
    for (int i = 0; i < 32; ++i) ga[i] = 0.f;
#pragma unroll
    for (int n = 0; n < 10; ++n) {
      const int r = n;
      if (n == 1) GU_ISSUE(8, 8);
      if (n == 2) GU_ISSUE(9, 9);
      if (n >= 3 && n < 8) {
#pragma unroll
        for (int k = 3 * (n - 3); k < 3 * (n - 2); ++k) dma16(24576 + (k * 8 + w) * 1024, lane * 16, (const char*)wd + hg * (96 * 1280) + (k * 8 + w) * 1024);
      }
      asm volatile("s_waitcnt vmcnt(%4)" : "+v"(hv[4 * r]), "+v"(hv[4 * r + 1]), "+v"(hv[4 * r + 2]), "+v"(hv[4 * r + 3]) : "i"(n < 5 ? 63 : 4 * (9 - n) + (n < 7 ? 3 * (n - 2) : 15)) : "memory");
      const u32x2 xv = *(const LAS u32x2*)(xp + (32 * n + 4 * w + rg) * 2);
      const bf16x2 x0 = asb(xv[0]), x1 = asb(xv[1]);
#pragma unroll
      for (int m = 0; m < 2; ++m)
#pragma unroll
        for (int i = 0; i < 4; ++i) {
          const bf16x2 lo = asb(__builtin_amdgcn_perm(hv[4 * r + 2 * m + 1][i], hv[4 * r + 2 * m][i], 0x05040100u));
          const bf16x2 hi = asb(__builtin_amdgcn_perm(hv[4 * r + 2 * m + 1][i], hv[4 * r + 2 * m][i], 0x07060302u));
          ga[16 * m + 2 * i] = __builtin_amdgcn_fdot2_f32_bf16(lo, x0, ga[16 * m + 2 * i], false);
          ga[16 * m + 8 + 2 * i] = __builtin_amdgcn_fdot2_f32_bf16(lo, x1, ga[16 * m + 8 + 2 * i], false);
          ga[16 * m + 2 * i + 1] = __builtin_amdgcn_fdot2_f32_bf16(hi, x0, ga[16 * m + 2 * i + 1], false);
          ga[16 * m + 8 + 2 * i + 1] = __builtin_amdgcn_fdot2_f32_bf16(hi, x1, ga[16 * m + 8 + 2 * i + 1], false);
        }
#pragma unroll
      for (int i = 0; i < 32; i += 8) asm volatile("" : "+v"(ga[i]), "+v"(ga[i + 1]), "+v"(ga[i + 2]), "+v"(ga[i + 3]), "+v"(ga[i + 4]), "+v"(ga[i + 5]), "+v"(ga[i + 6]), "+v"(ga[i + 7]));
    }
    STAMP(4);
    LAS float* red = lp<float>(11264);
    {
      float h16[16], h8[8];
#pragma unroll
      for (int i = 0; i < 16; ++i) { auto r = __builtin_amdgcn_permlane32_swap(__float_as_uint(ga[i]), __float_as_uint(ga[i + 16]), false, false); h16[i] = __uint_as_float(r[0]) + __uint_as_float(r[1]); }
#pragma unroll
      for (int i = 0; i < 8; ++i) { auto r = __builtin_amdgcn_permlane16_swap(__float_as_uint(h16[i]), __float_as_uint(h16[i + 8]), false, false); h8[i] = __uint_as_float(r[0]) + __uint_as_float(r[1]); }
      if (act) {
        LAS float* d = red + w * 384 + rg * 96 + pc * 8;
        *(LAS f32x4*)d = (f32x4){h8[0], h8[1], h8[2], h8[3]}; *(LAS f32x4*)(d + 4) = (f32x4){h8[4], h8[5], h8[6], h8[7]};
      }
    }
    BAR();
    STAMP(6);
    LAS u16* hs = lp<u16>(4096);
    if (tid < 192) {
      float g = 0.f, u = 0.f;
#pragma unroll
      for (int k = 0; k < 8; ++k) { g += red[k * 384 + tid]; u += red[k * 384 + 192 + tid]; }
      g = rbf(g); u = rbf(u);
      const float hval = rbf(g * __builtin_amdgcn_rcpf(1.f + __expf(-g))) * u;
      const int t = tid >= 96, f = tid - 96 * t;
      hs[(f >> 1) * 4 + t * 2 + (f & 1)] = (u16)pk(hval, hval);
    }
    STAMP(12);
    WAITBAR(0);
    STAMP(7);
    float y[2][8];
#pragma unroll
    for (int i = 0; i < 8; ++i) y[0][i] = y[1][i] = 0.f;
    const int rgp = tid / 80, cp = tid - 80 * rgp;
    if (tid < 480) {
#pragma unroll
      for (int i = 0; i < 8; ++i) {
        const u32 ad = 24576 + (12 * i + 2 * rgp) * 1280 + cp * 16;
        const u32x4 d0 = *lp<u32x4>(ad), d1 = *lp<u32x4>(ad + 1280);
        const u32x2 hp = *lp<u32x2>(4096 + (6 * i + rgp) * 8);
        const bf16x2 h0 = asb(hp[0]), h1 = asb(hp[1]);
#pragma unroll
        for (int k = 0; k < 4; ++k) {
          const bf16x2 dl_ = asb(__builtin_amdgcn_perm(d1[k], d0[k], 0x05040100u));
          const bf16x2 dh_ = asb(__builtin_amdgcn_perm(d1[k], d0[k], 0x07060302u));
          y[0][2 * k] = __builtin_amdgcn_fdot2_f32_bf16(dl_, h0, y[0][2 * k], false); y[1][2 * k] = __builtin_amdgcn_fdot2_f32_bf16(dl_, h1, y[1][2 * k], false);
          y[0][2 * k + 1] = __builtin_amdgcn_fdot2_f32_bf16(dh_, h0, y[0][2 * k + 1], false); y[1][2 * k + 1] = __builtin_amdgcn_fdot2_f32_bf16(dh_, h1, y[1][2 * k + 1], false);
        }
      }
    }
    STAMP(8);
    BAR();
    LAS float* r3 = lp<float>(24576);
    if (tid < 480) {
#pragma unroll
      for (int t = 0; t < 2; ++t) {
        LAS float* d = r3 + rgp * 1280 + t * 640 + cp * 8;
        *(LAS f32x4*)d = (f32x4){y[t][0], y[t][1], y[t][2], y[t][3]}; *(LAS f32x4*)(d + 4) = (f32x4){y[t][4], y[t][5], y[t][6], y[t][7]};
      }
    }
    BAR();
    if (tid < 320) {
      const u32 kq = (tid >= 160 ? k2 : k1) & 0xfffff800u;
      const float rsel = rbf(__uint_as_float(kq ^ ((kq >> 31) ? 0x80000000u : ~0u)));
      const float sg = rbf(__builtin_amdgcn_rcpf(1.f + __expf(-rsel)));
      f32x4 s = *(const LAS f32x4*)(r3 + tid * 4);
#pragma unroll
      for (int k = 1; k < 6; ++k) s += *(const LAS f32x4*)(r3 + k * 1280 + tid * 4);
      const u32 off = WS_Y + hg * 10240 + tid * 32;
      __builtin_amdgcn_raw_buffer_store_b128((u32x4){__float_as_uint(s[0] * sg), tag, __float_as_uint(s[1] * sg), tag}, wsr, off, 0, 16);
      __builtin_amdgcn_raw_buffer_store_b128((u32x4){__float_as_uint(s[2] * sg), tag, __float_as_uint(s[3] * sg), tag}, wsr, off + 16, 0, 16);
    }
    BAR();
    STAMP(5);
    u32 wv[8];
    wo_offsets(wv, wl, lane);
    RES_PREP(b);
    RES_ISSUE();
    stage_wo<2>(karg(4) + (bi & 7) * 160, X, wl, wv);
    asm volatile("s_waitcnt vmcnt(0) lgkmcnt(0)\n\ts_barrier" : "+v"(sgr) :: "memory");
    if (X == 1) { lds_arrive(L_CNT + 20); lds_arrive(L_CNT + 4); }
    STAMP(9);
  }

  {
    int b3 = blockIdx.x; PINS(b3);
    const int rt3 = (b3 & 7) + 8 * (b3 >> 6), ct3 = (b3 >> 3) & 7;
    const int tk = lane & 15, g = lane >> 4;
    const int t16 = 4 * rt3 + wl, row = 16 * t16 + tk;
    const u32 wfa = (8 * g + 4 * (g & 1) + ((lane >> 2) & 3)) * 160 + (lane & 3) * 8, wfb = (8 * g + 4 * ((g & 1) ^ 1) + ((lane >> 2) & 3)) * 160 + (lane & 3) * 8;
    const u32 res = L_KVB + wl * 5120 + lane * 16;
    f32x4 acc[5];
    const u32 wb0 = X == 0 ? L_WOA : L_KVA, wb7 = X == 0 ? L_WOA + 7 * 5120 : L_WOB7;
    char* out = karg(8);
    u32 ob = WS_O + X * (1u << 20) + t16 * 8192 + lane * 16, cb = WS_C4 + X * (1u << 18) + t16 * 2048 + g * 256 + tk * 8;
    u32 ofo = (row * 640 + 80 * ct3 + 16 * (g & 1) + 8 * (g >> 1)) * 2, ofo2 = (row * 640 + 80 * ct3 + 64 + 4 * g) * 2;
    u32 w8a = wb7 + 5120 + wfa, w8b = wb7 + 5120 + wfb, resv = res;
    u32 ob2 = ob + 4096, pfo = WS_OFLAG + ((t16 >> 1) * 32 + 16 * X + tk) * 128;
    u32 w0a = wb0 + wfa, w0b = wb0 + wfb;
    PINV(ofo); PINV(ofo2); PINV(w0a); PINV(w0b); PINV(w8a); PINV(w8b); PINV(resv); PINV(ob); PINV(ob2); PINV(cb); PINV(pfo); PINS(out);
    bool early = false;
    if (X == 0) {
      u32 tp = (u32)sgr;
      if (!__all((u32)(sgr >> 32) == tag)) tp = poll_gran(ws, WS_TOP2 + (b3 & 15) * 128, tag);
      tp = uni(tp);
#pragma unroll
      for (int t = 0; t < 5; ++t) acc[t] = (f32x4){0.f, 0.f, 0.f, 0.f};
      early = (int)((tp & 0xffff) >> 4) == t16 || (int)(tp >> 20) == t16;
      if (early) { WAITV(0); lds_arrive(L_CNT + 12); __builtin_amdgcn_s_setprio(1); }
#pragma unroll 1
      for (int cidx = 0; cidx < 2; ++cidx) {
        const u32 id = cidx ? tp >> 16 : tp & 0xffff;
        if ((int)(id >> 4) != t16) continue;
        LAS float* yb = lp<float>(L_YB + wl * 320);
#pragma unroll 1
        for (int p = 0; p < 2; ++p) {
          const int col = p * 64 + lane;
          const u32 off = WS_Y + cidx * 5120 + (80 * ct3 + (col < 80 ? col : 79)) * 8;
          float s = 0.f;
          NOUNROLL for (u32 sp = 0; sp < SPIN_CAP; ++sp) {
            u64 gv[16]; bool ok = true; s = 0.f;
#pragma unroll
            for (int k = 0; k < 16; ++k) gv[k] = gld64(ws, off + k * 10240);
#pragma unroll
            for (int k = 0; k < 16; ++k) { ok = ok && (u32)(gv[k] >> 32) == tag; s += __uint_as_float((u32)gv[k]); }
            if (__all(ok)) break;
            __builtin_amdgcn_s_sleep(1);
          }
          if (col < 80) yb[col] = s;
        }
        asm volatile("s_waitcnt lgkmcnt(0)" ::: "memory");
        if (tk == (int)(id & 15)) {
#pragma unroll
          for (int t = 0; t < 5; ++t) acc[t] += *(const LAS f32x4*)(yb + 16 * t + 4 * g);
        }
        asm volatile("s_waitcnt lgkmcnt(0)" ::: "memory");
      }
    } else {
      u32 sg = (u32)sgr;
      if (!__all((u32)(sgr >> 32) == tag)) sg = poll_gran(ws, WS_STATS + row * 8, tag);
      const float mb = blo(sg), rs = __builtin_amdgcn_rcpf(bhi(sg));
#pragma unroll
      for (int t = 0; t < 5; ++t) {
        const u32x2 xr = *lp<u32x2>(L_XR + wl * 3072 + tk * 160 + g * 8 + t * 32);
#pragma unroll
        for (int i = 0; i < 2; ++i) {
          const float a0 = blo(xr[i]), a1 = bhi(xr[i]);
          const u32 d = pk(a0 - mb, a1 - mb), n = pk(blo(d) * rs, bhi(d) * rs);
          acc[t][2 * i] = a0 + blo(n); acc[t][2 * i + 1] = a1 + bhi(n);
        }
        PINV(acc[t]);
      }
    }
    STAMP(1);
    lds_wait(X == 0 ? L_CNT + 4 : L_CNT + 12, 4);
    STAMP(2);
    s16x8 wq[8][5];
#pragma unroll
    for (int ks = 0; ks < 8; ++ks)
#pragma unroll
      for (int t = 0; t < 5; ++t) wq[ks][t] = ldstr((ks < 7 ? w0a + ks * 5120 : w8a - 5120) + t * 32, (ks < 7 ? w0b + ks * 5120 : w8b - 5120) + t * 32);
    if (X == 0 && !early) { WAITV(0); lds_arrive(L_CNT + 12); __builtin_amdgcn_s_setprio(1); }
    STAMP(10);
    u32x4 of[10];
    {
      u64 pt; u32 pn;
      asm volatile(POLL_TXT("%10", "%11", "%12", "%13", "%14")
                   "s_memrealtime %18\n\t"
                   "global_load_dwordx4 %0, %15, %13 sc1\n\tglobal_load_dwordx4 %1, %15, %13 offset:1024 sc1\n\tglobal_load_dwordx4 %2, %15, %13 offset:2048 sc1\n\t"
                   "global_load_dwordx4 %3, %15, %13 offset:3072 sc1\n\tglobal_load_dwordx4 %4, %16, %13 sc1\n\tglobal_load_dwordx4 %5, %16, %13 offset:1024 sc1\n\t"
                   "global_load_dwordx4 %6, %16, %13 offset:2048 sc1\n\tglobal_load_dwordx4 %7, %16, %13 offset:3072 sc1\n\t"
                   "global_load_dwordx2 v[240:241], %17, %13 sc1\n\tglobal_load_dwordx2 v[242:243], %17, %13 offset:128 sc1\n\t"
                   "global_load_dwordx2 v[244:245], %17, %13 offset:1024 sc1\n\tglobal_load_dwordx2 v[246:247], %17, %13 offset:1152 sc1\n\t"
                   "s_waitcnt lgkmcnt(0)"
                   : "=&v"(of[0]), "=&v"(of[1]), "=&v"(of[2]), "=&v"(of[3]), "=&v"(of[4]), "=&v"(of[5]), "=&v"(of[6]), "=&v"(of[7]),
                     "=&{v[240:243]}"(of[8]), "=&{v[244:247]}"(of[9]), "=&v"(pt), "=&s"(pn), "+v"(pfo), "+s"(ws), "+s"(tag64), "+v"(ob), "+v"(ob2), "+v"(cb)
                     , "=&s"(st[11])
                   :: "memory", "scc", "vcc");
    }
    if (X == 1) {
      lds_wait(L_CNT + 32 + 4 * wl, 1);
#pragma unroll
      for (int t = 0; t < 5; ++t) acc[t] += *lp<f32x4>(resv + t * 1024);
    }
    s16x8 w8[2][5];
#pragma unroll
    for (int ks = 0; ks < 10; ++ks) {
      if (ks == 0) asm volatile("s_waitcnt vmcnt(10)" : "+v"(of[0]), "+v"(of[1]) :: "memory");
      if (ks == 2) asm volatile("s_waitcnt vmcnt(7)" : "+v"(of[2]), "+v"(of[3]), "+v"(of[4]) :: "memory");
      if (ks == 5) asm volatile("s_waitcnt vmcnt(4)" : "+v"(of[5]), "+v"(of[6]), "+v"(of[7]) :: "memory");
      if (ks == 8) asm volatile("s_waitcnt vmcnt(0)" : "+v"(of[8]), "+v"(of[9]) :: "memory");
      SB0();
#pragma unroll
      for (int t = 0; t < 5; ++t) {
        acc[t] = mfma16(ks < 8 ? wq[ks < 8 ? ks : 0][t] : w8[ks & 1][t], __builtin_bit_cast(s16x8, of[ks]), acc[t]);
        if (ks == 2 || ks == 3) w8[ks & 1][t] = ldstr(w8a + (ks & 1) * 5120 + t * 32, w8b + (ks & 1) * 5120 + t * 32);
      }
      SB0();
    }
    STAMP(30);
    if (X == 0) {
      lds_wait(L_CNT + 20, 4);
#pragma unroll
      for (int t = 0; t < 5; ++t) *lp<f32x4>(resv + t * 1024) = acc[t];
      lds_arrive(L_CNT + 32 + 4 * wl);
    } else {
      u32 o[10];
#pragma unroll
      for (int t = 0; t < 5; ++t) { o[2 * t] = pk(acc[t][0], acc[t][1]); o[2 * t + 1] = pk(acc[t][2], acc[t][3]); }
#pragma unroll
      for (int t = 0; t < 4; t += 2) {
        auto r0 = __builtin_amdgcn_permlane16_swap(o[2 * t], o[2 * t + 2], false, false);
        auto r1 = __builtin_amdgcn_permlane16_swap(o[2 * t + 1], o[2 * t + 3], false, false);
        *(GAS u32x4*)(out + ofo + 32 * t) = (u32x4){r0[0], r1[0], r0[1], r1[1]};
      }
      *(GAS u32x2*)(out + ofo2) = (u32x2){o[8], o[9]};
    }
    STAMP(31);
  }
  WAITV(0);
  STAMP(14);
  { const u64 d = (!helper && st[AMPI] > st[0]) ? st[AMPI] - st[0] : 0; const u64 tgt = st[14] + 10 * d; (void)cyc0; while (__builtin_amdgcn_s_memrealtime() < tgt) __builtin_amdgcn_s_sleep(2); }
}

extern "C" void kernel_launch(void* const* d_in, const int* in_sizes, int n_in, void* d_out, int out_size, void* d_ws, size_t ws_size,
                              hipStream_t stream) {
  (void)in_sizes; (void)n_in; (void)out_size; (void)ws_size;
  (void)hipFuncSetAttribute((const void*)fused_block, hipFuncAttributeMaxDynamicSharedMemorySize, LDS_BYTES);
  fused_block<<<256, 512, LDS_BYTES, stream>>>((const u16*)d_in[0], (const u16*)d_in[1], (const u16*)d_in[4], (char*)d_ws, (const u16*)d_in[2],
                                               (const u16*)d_in[5], (const u16*)d_in[6], (const u16*)d_in[7], (u16*)d_out);
}
```

```cpp
#include <hip/hip_runtime.h>
#include <stdint.h>

typedef unsigned short u16;
typedef unsigned u32;
typedef unsigned long long u64;
typedef u32 u32x2 __attribute__((ext_vector_type(2)));
typedef u32 u32x4 __attribute__((ext_vector_type(4)));
typedef short s16x4 __attribute__((ext_vector_type(4)));
typedef short s16x8 __attribute__((ext_vector_type(8)));
typedef float f32x2 __attribute__((ext_vector_type(2)));
typedef float f32x4 __attribute__((ext_vector_type(4)));
typedef float f32x16 __attribute__((ext_vector_type(16)));
typedef __bf16 bf16x2 __attribute__((ext_vector_type(2)));
#define LAS __attribute__((address_space(3)))
#define GAS __attribute__((address_space(1)))
#define DI __device__ __forceinline__
#define RLX __ATOMIC_RELAXED, __HIP_MEMORY_SCOPE_AGENT
#define SB0() __builtin_amdgcn_sched_barrier(0)

#define ALWAYS_MAX 0
#define P1X 0
#define CODE_LINES 250

constexpr u32 WS_EPOCH = 0;
constexpr u32 WS_QFLAG = 4096;
constexpr u32 WS_OFLAG = 65536;
constexpr u32 WS_TOP2  = 327680;
constexpr u32 WS_STATS = 331776;
constexpr u32 WS_RR    = 348160;
constexpr u32 WS_Y     = 393216;
constexpr u32 WS_QKV   = 1u << 20;
constexpr u32 WS_O     = 10u << 20;
constexpr u32 WS_C4    = 12u << 20;
constexpr u32 WS_TS    = 16u << 20;
constexpr u32 WS_SPAN  = 13u << 20;

constexpr u32 SLOT = 24576;
constexpr u32 L_BP = 4 * SLOT;
constexpr u32 L_WOA = 0;
constexpr u32 L_KVA = 51200, L_KVB = 88576, PLANE = 6208;
constexpr u32 L_WOB7 = 125952;
constexpr u32 L_XR = 141312;
constexpr u32 L_MISC = 155648;
constexpr u32 L_CNT = L_MISC;
constexpr u32 L_ONE = L_MISC + 1664;
constexpr u32 L_PF  = L_MISC + 2560;
constexpr u32 L_YB  = L_MISC + 2816;
constexpr u32 L_CP  = L_MISC + 4096;
constexpr u32 LDS_BYTES = 163840;
static_assert((L_ONE & 255) == 128 && (L_KVA & 255) == 0 && (L_KVB & 255) == 0, "bank quarters");

constexpr u32 SPIN_CAP = 1u << 17;

template <int I> struct IC { static constexpr int v = I; };
template <int I, int N, class F> DI void static_for(F&& f) { if constexpr (I < N) { f(IC<I>{}); static_for<I + 1, N>(f); } }
template <class T> DI LAS T* lp(u32 a) { return (LAS T*)(uintptr_t)a; }
DI u32 pk(float a, float b) { f32x2 v = {a, b}; return __builtin_bit_cast(u32, __builtin_convertvector(v, bf16x2)); }
DI float blo(u32 v) { return __uint_as_float(v << 16); }
DI float bhi(u32 v) { return __uint_as_float(v & 0xffff0000u); }
DI float rbf(float a) { return blo(pk(a, a)); }
DI u32 uni(u32 v) { return __builtin_amdgcn_readfirstlane(v); }
template <int C> DI float dppf(float v) { return __uint_as_float(__builtin_amdgcn_mov_dpp(__float_as_uint(v), C, 0xf, 0xf, true)); }
DI float x16sum(float v) { auto r = __builtin_amdgcn_permlane16_swap(__float_as_uint(v), __float_as_uint(v), false, false); return __uint_as_float(r[0]) + __uint_as_float(r[1]); }
DI float x32sum(float v) { auto r = __builtin_amdgcn_permlane32_swap(__float_as_uint(v), __float_as_uint(v), false, false); return __uint_as_float(r[0]) + __uint_as_float(r[1]); }
DI float wsum(float v) {
  v += dppf<0xB1>(v); v += dppf<0x4E>(v); v += dppf<0x141>(v); v += dppf<0x140>(v);
  return x32sum(x16sum(v));
}
DI float sel(float a, float b, u64 m) { return __builtin_amdgcn_inverse_ballot_w64(m) ? b : a; }
DI float bsel(float a, float b, u32 m) { return __uint_as_float((__float_as_uint(b) & m) | (__float_as_uint(a) & ~m)); }
DI bf16x2 asb(u32 v) { return __builtin_bit_cast(bf16x2, v); }
DI f32x16 mfma32(s16x8 a, s16x8 b, f32x16 c) { return __builtin_amdgcn_mfma_f32_32x32x16_bf16(a, b, c, 0, 0, 0); }
DI f32x4 mfma16(s16x8 a, s16x8 b, f32x4 c) { return __builtin_amdgcn_mfma_f32_16x16x32_bf16(a, b, c, 0, 0, 0); }
DI s16x8 lds128(u32 a) { return *lp<s16x8>(a); }
DI s16x8 ldstr(u32 a, u32 b) {
  s16x4 lo = __builtin_amdgcn_ds_read_tr16_b64_v4i16(lp<s16x4>(a)), hi = __builtin_amdgcn_ds_read_tr16_b64_v4i16(lp<s16x4>(b));
  return __builtin_shufflevector(lo, hi, 0, 1, 2, 3, 4, 5, 6, 7);
}
DI void dma16(u32 lds, u32 voff, const void* sbase) {
  asm volatile("s_mov_b32 m0, %0\n\ts_nop 0\n\tglobal_load_lds_dwordx4 %1, %2" :: "s"(lds), "v"(voff), "s"(sbase) : "memory");
}
DI void dma16c(u32 lds, u32 voff, const void* sbase) {
  asm volatile("s_mov_b32 m0, %0\n\ts_nop 0\n\tglobal_load_lds_dwordx4 %1, %2 sc1" :: "s"(lds), "v"(voff), "s"(sbase) : "memory");
}
#define WAITV(n) asm volatile("s_waitcnt vmcnt(" #n ")" ::: "memory")
#define WAITBAR(n) asm volatile("s_waitcnt vmcnt(%0) lgkmcnt(0)\n\ts_barrier" :: "i"(n) : "memory")
#define WAITBAR2(n0, n1) asm volatile("s_waitcnt vmcnt(" #n1 ") lgkmcnt(0)\n\ts_cmp_lg_u32 %0, 0\n\ts_cbranch_scc1 1f\n\ts_waitcnt vmcnt(" #n0 ")\n1:\n\ts_barrier" :: "s"(X) : "memory", "scc")
#define WAITBAR1(n1) asm volatile("s_cmp_lg_u32 %0, 0\n\ts_cbranch_scc0 1f\n\ts_waitcnt vmcnt(" #n1 ")\n1:\n\ts_waitcnt lgkmcnt(0)\n\ts_barrier" :: "s"(X) : "memory", "scc")
#define WAITBARV(n) asm volatile("s_waitcnt vmcnt(%0)\n\ts_barrier" :: "i"(n) : "memory")
#define BAR() asm volatile("s_waitcnt lgkmcnt(0)\n\ts_barrier" ::: "memory")
DI u64 gld64(const char* ws, u32 off) { return __hip_atomic_load((GAS u64*)(ws + off), RLX); }
DI u64 gld64w(const char* ws, u32 off) {
  u64 r; asm volatile("global_load_dwordx2 %0, %1, %2 sc1\n\ts_waitcnt vmcnt(0)" : "=&v"(r) : "v"(off), "s"(ws) : "memory"); return r;
}
#define NOUNROLL _Pragma("unroll 1")
DI void gst64(char* ws, u32 off, u64 v) { __hip_atomic_store((GAS u64*)(ws + off), v, RLX); }
DI void lds_arrive(u32 a) {
  u32 va = a, one = 1;
  asm volatile("s_waitcnt lgkmcnt(0)\n\ts_mov_b64 exec, 1\n\tds_add_u32 %0, %1\n\ts_mov_b64 exec, -1" :: "v"(va), "v"(one) : "memory");
}
DI void lds_wait(u32 a, u32 n) {
  u32 va = a, t, c;
  asm volatile("1:\n\tds_read_b32 %0, %2\n\ts_waitcnt lgkmcnt(0)\n\tv_readfirstlane_b32 %1, %0\n\ts_cmp_ge_u32 %1, %3\n\ts_cbranch_scc1 2f\n\ts_sleep 1\n\ts_branch 1b\n2:"
               : "=&v"(t), "=&s"(c) : "v"(va), "s"(n) : "memory", "scc");
}
#define POLL_TXT(T, N, OFF, WS, TAG) "s_mov_b32 " N ", 0x20000\n1:\n\tglobal_load_dwordx2 " T ", " OFF ", " WS " sc1\n\ts_waitcnt vmcnt(0)\n\t" \
    "v_cmp_ne_u64 vcc, " TAG ", " T "\n\ts_cbranch_vccz 2f\n\ts_sleep 1\n\ts_sub_u32 " N ", " N ", 1\n\ts_cbranch_scc0 1b\n2:\n\t"
DI void poll_flags(const char* ws, u32 off, u64 tag) {
  u64 t; u32 n;
  asm volatile(POLL_TXT("%0", "%1", "%2", "%3", "%4") : "=&v"(t), "=&s"(n) : "v"(off), "s"(ws), "s"(tag) : "memory", "scc", "vcc");
}
DI u32 poll_gran(const char* ws, u32 off, u32 tag) {
  u64 g;
  NOUNROLL for (u32 s = 0; s < SPIN_CAP; ++s) { g = gld64w(ws, off); if (__all((u32)(g >> 32) == tag)) break; __builtin_amdgcn_s_sleep(1); }
  return (u32)g;
}
DI void store_flag(char* ws, u32 off, u64 val, u64 mask) {
  asm volatile("s_mov_b64 exec, %3\n\tglobal_store_dwordx2 %0, %1, %2 sc1\n\ts_mov_b64 exec, -1" :: "v"(off), "v"(val), "s"(ws), "s"(mask) : "memory");
}
#define PINV(x) asm volatile("" : "+v"(x))
#define PINS(x) asm volatile("" : "+s"(x))

#define UOFF 0
#define STAMP(i) do {} while (0)

DI void wo_offsets(u32 (&wv)[8], int wl, int lane) {
#pragma unroll
  for (int k = 0; k < 8; ++k) {
    const int n = k < 5 ? k : k < 7 ? 3 * (wl & 1) + k - 5 : (wl & 1 ? 4 : 2);
    const u32 P = 64 * n + lane, r = P / 10u, pc = P - 10 * r, g = r >> 3, j = (r & 7) ^ ((g & 1) << 2);
    wv[k] = ((k >= 5 ? 40 * g + 16 : 8 * (g + (g >> 1))) + j) * 1280 + pc * 16;
    PINV(wv[k]);
  }
}
template <int PART>
DI void stage_wo(const char* woc, int H, int wl, const u32 (&wv)[8]) {
  auto at = [&](int ks) { return H == 0 ? L_WOA + ks * 5120 : ks < 7 ? L_KVA + ks * 5120 : L_WOB7 + (ks - 7) * 5120; };
  const int n2 = 3 * (wl & 1), k2 = wl >> 1;
  const char *s0 = woc + (320 * H + 40 * wl) * 1280, *s2 = woc + (320 * H + 160 * k2) * 1280;
#define WO_D(M, V, S) "s_add_u32 m0, " M "\n\ts_nop 0\n\tglobal_load_lds_dwordx4 " V ", " S "\n\t"
#define WO_5 "s_nop 4\n\t" WO_D("%7, 0", "%0", "%5") WO_D("m0, 1024", "%1", "%5") WO_D("m0, 1024", "%2", "%5") WO_D("m0, 1024", "%3", "%5") WO_D("m0, 1024", "%4", "%5")
  if (PART != 1)
    asm volatile(WO_5 WO_D("%8, 0", "%9", "%6")
                 :: "v"(wv[0]), "v"(wv[1]), "v"(wv[2]), "v"(wv[3]), "v"(wv[4]), "s"(s0), "s"(s2), "s"(at(wl)), "s"(at(8 + k2) + (wl & 1 ? 4 : 2) * 1024), "v"(wv[7]) : "memory", "scc");
  if (PART != 0)
    asm volatile(WO_5 WO_D("%8, 0", "%9", "%6") WO_D("m0, 1024", "%10", "%6")
                 :: "v"(wv[0]), "v"(wv[1]), "v"(wv[2]), "v"(wv[3]), "v"(wv[4]), "s"(s0 + 160 * 1280), "s"(s2), "s"(at(wl + 4)), "s"(at(8 + k2) + n2 * 1024), "v"(wv[5]), "v"(wv[6]) : "memory", "scc");
}

constexpr int slice_of(int v) { return v < 6 ? v : (v >= 9 && v < 13) ? v - 3 : -1; }
constexpr int page_of(int v) { return v < 9 ? v - 6 : v - 10; }
constexpr int OP_GR = 41, OP_PF = 43;
constexpr int sched_op(int u, int i) {
  if (u == 0) return i < 3 ? 6 + i : -1;
  if (u <= 3) return i < 3 ? 3 * (u + 2) + i : 30 + u - 1;
  if (u <= 6) return i < 3 ? 3 * (u + 2) + i : -1;
  if (u == 7) return i < 3 ? 27 + i : OP_GR;
  if (u == 8) return i < 3 ? 33 + i : -1;
  if (u == 9) return i == 0 ? 39 : -1;
  if (u == 10) return i < 3 ? 36 + i : -1;
  if (u == 11) return i == 0 ? OP_PF : -1;
  return -1;
}
constexpr int younger(int op, int u) {
  int n = op < 6 ? 5 - op + 1 : 0; bool seen = op < 6;
  for (int v = 0; v < u; ++v) for (int i = 0; i < 4; ++i) { const int o = sched_op(v, i); if (o < 0) continue; if (seen) ++n; if (o == op) seen = true; }
  return n;
}
constexpr int need_op(int u) {
  return slice_of(u) >= 0 ? 3 * slice_of(u) + 2 : u == 6 ? 32 : u == 13 ? 38 : -1;
}
template <bool FULL>
DI f32x16 attn_item(s16x8 q0, s16x8 q1, u32 ka, u32 kb, u32 ko, u32 va, u32 vo, u32 vstep, const u32 (&dm)[8], const u32 (&mk)[16], int nfull, float negm, float cold) {
  f32x16 ci, co, O;
#pragma unroll
  for (int i = 0; i < 16; ++i) { ci[i] = FULL ? 0.f : negm; co[i] = FULL ? 0.f : cold; O[i] = 0.f; }
  f32x16 S[2];
  s16x8 kn0, kn1, pend, vpend;
  {
    const s16x8 kd0 = lds128(ka + 4096), ko0 = lds128(ka + ko), kd1 = lds128(kb + 4096), ko1 = lds128(kb + ko), k10 = lds128(ka + 3584), k11 = lds128(kb + 3584);
    const s16x8 v0 = ldstr(va, va + 128), vo0 = ldstr(vo, vo + 128), vo1 = ldstr(vo + 256, vo + 384), v1 = ldstr(va + 256, va + 384);
    kn0 = lds128(ka + 3072); kn1 = lds128(kb + 3072);
    SB0();
    f32x16 sd = mfma32(kd0, q0, ci), so = mfma32(ko0, q0, co);
    sd = mfma32(kd1, q1, sd); so = mfma32(ko1, q1, so);
    S[1] = mfma32(k10, q0, ci);
    SB0();
    u32 p[8], pd[8];
#pragma unroll
    for (int i = 0; i < 8; ++i) {
      p[i] = pk(__builtin_amdgcn_exp2f(bsel(so[2 * i], sd[2 * i], mk[2 * i])), __builtin_amdgcn_exp2f(bsel(so[2 * i + 1], sd[2 * i + 1], mk[2 * i + 1])));
      pd[i] = p[i] & dm[i]; p[i] ^= pd[i];
      if (i == 1) { S[1] = mfma32(k11, q1, S[1]); PINV(S[1]); SB0(); }
      if (i == 3) { O = mfma32(v0, __builtin_bit_cast(s16x8, (u32x4){pd[0], pd[1], pd[2], pd[3]}), O); PINV(O); SB0(); }
      if (i == 5) { O = mfma32(vo0, __builtin_bit_cast(s16x8, (u32x4){p[0], p[1], p[2], p[3]}), O); PINV(O); SB0(); }
    }
    O = mfma32(vo1, __builtin_bit_cast(s16x8, (u32x4){p[4], p[5], p[6], p[7]}), O);
    pend = __builtin_bit_cast(s16x8, (u32x4){pd[4], pd[5], pd[6], pd[7]}); vpend = v1;
  }
#pragma unroll
  for (int e = 1; e <= 7; ++e) {
    if (!FULL && e > nfull) break;
    const f32x16& sc = S[e & 1];
    va -= vstep;
    SB0();
    f32x16 sn = ci;
    if (e < 7) sn = mfma32(kn0, q0, ci);
    const s16x8 v0 = ldstr(va, va + 128), v1 = ldstr(va + 256, va + 384);
    u32 p[8];
    float ex[16];
#pragma unroll
    for (int i = 0; i < 4; ++i) ex[i] = __builtin_amdgcn_exp2f(sc[i]);
    SB0();
    if (e < 7) sn = mfma32(kn1, q1, sn);
    if (e < 6) { kn0 = lds128(ka + 3072 - e * 512); kn1 = lds128(kb + 3072 - e * 512); }
#pragma unroll
    for (int i = 4; i < 8; ++i) ex[i] = __builtin_amdgcn_exp2f(sc[i]);
    p[0] = pk(ex[0], ex[1]); p[1] = pk(ex[2], ex[3]);
    SB0();
    O = mfma32(vpend, pend, O); PINV(O);
    SB0();
#pragma unroll
    for (int i = 8; i < 12; ++i) ex[i] = __builtin_amdgcn_exp2f(sc[i]);
    p[2] = pk(ex[4], ex[5]); p[3] = pk(ex[6], ex[7]);
    SB0();
    O = mfma32(v0, __builtin_bit_cast(s16x8, (u32x4){p[0], p[1], p[2], p[3]}), O); PINV(O);
    SB0();
#pragma unroll
    for (int i = 12; i < 16; ++i) ex[i] = __builtin_amdgcn_exp2f(sc[i]);
    p[4] = pk(ex[8], ex[9]); p[5] = pk(ex[10], ex[11]); p[6] = pk(ex[12], ex[13]); p[7] = pk(ex[14], ex[15]);
    pend = __builtin_bit_cast(s16x8, (u32x4){p[4], p[5], p[6], p[7]}); vpend = v1;
    S[(e + 1) & 1] = sn;
    SB0();
  }
  return mfma32(vpend, pend, O);
}
DI float attn_negmax(s16x8 q0, s16x8 q1, u32 ka, u32 kb, int nfull, bool has_old, int c, int hh) {
  f32x16 z;
#pragma unroll
  for (int i = 0; i < 16; ++i) z[i] = 0.f;
  float m = -__builtin_inff();
  NOUNROLL for (int e = 0; e <= nfull + (has_old ? 1 : 0); ++e) {
    const f32x16 s = mfma32(lds128(kb + 4096 - e * 512), q1, mfma32(lds128(ka + 4096 - e * 512), q0, z));
    int lo = (e == 8 ? c + 1 : 0) - 4 * hh, n = (e == 0 ? c : 31) - 4 * hh - lo;
    PINV(lo); PINV(n);
#pragma unroll
    for (int i = 0; i < 16; ++i) { const int k = (i & 3) + 8 * (i >> 2) - lo; m = fmaxf(m, (k >= 0 && k <= n) ? s[i] : m); }
  }
  auto r = __builtin_amdgcn_permlane32_swap(__float_as_uint(m), __float_as_uint(m), false, false);
  return -fmaxf(__uint_as_float(r[0]), __uint_as_float(r[1]));
}

DI void row_stats(u32x4 xa, u32 xb, u32x4 wa, u32 wb, char* ws, int srow, u32 tag, int tid) {
  const int lane = tid & 63;
  const u32 xu[5] = {xa[0], xa[1], xa[2], xa[3], xb}, wu5[5] = {wa[0], wa[1], wa[2], wa[3], wb};
  f32x2 v[5], s2 = {0.f, 0.f};
#pragma unroll
  for (int i = 0; i < 5; ++i) { v[i] = (f32x2){blo(xu[i]), bhi(xu[i])}; s2 += v[i]; }
  const float mean = wsum(s2[0] + s2[1]) * (1.f / 640.f), mb = rbf(mean);
  f32x2 q2 = {0.f, 0.f};
  float r = 0.f;
#pragma unroll
  for (int i = 0; i < 5; ++i) {
    const f32x2 d = v[i] - mean, e = v[i] - mb;
    q2 += d * d;
    r = __builtin_amdgcn_fdot2_f32_bf16(asb(pk(e[0], e[1])), asb(wu5[i]), r, false);
  }
  float q = q2[0] + q2[1];
  q += dppf<0xB1>(q); r += dppf<0xB1>(r); q += dppf<0x4E>(q); r += dppf<0x4E>(r);
  q += dppf<0x141>(q); r += dppf<0x141>(r); q += dppf<0x140>(q); r += dppf<0x140>(r);
  { auto t = __builtin_amdgcn_permlane16_swap(__float_as_uint(q), __float_as_uint(r), false, false); q = __uint_as_float(t[0]) + __uint_as_float(t[1]); }
  q = x32sum(q);
  r = __uint_as_float(__builtin_amdgcn_readlane(__float_as_uint(q), 16));
  q = __uint_as_float(__builtin_amdgcn_readlane(__float_as_uint(q), 0));
  const float sd = rbf(__builtin_amdgcn_sqrtf(rbf(rbf(q * (1.f / 640.f)) + 1e-5f)));
  const u32 pay = lane == 0 ? ((__float_as_uint(mb) >> 16) | __float_as_uint(sd)) : __float_as_uint(r * __builtin_amdgcn_rcpf(sd));
  store_flag(ws, (lane == 0 ? WS_STATS : WS_RR) + srow * 8, ((u64)tag << 32) | pay, 3);
  if (tid < 16) *lp<u32>(L_CNT + tid * 4) = 0;
  if (tid < 144) *lp<u32>(L_ONE + tid * 4) = 0x3F803F80u;
}

__global__ void __launch_bounds__(512, 2)
fused_block(const u16* __restrict__ x, const u16* __restrict__ wqkv, const u16* __restrict__ wr, char* ws,
            const u16* wo_, const u16* wg_, const u16* wu_, const u16* wd_, u16* out_) {
  (void)wo_; (void)wg_; (void)wu_; (void)wd_; (void)out_;
  auto karg = [](int i) { const __attribute__((address_space(4))) u64* k = (const __attribute__((address_space(4))) u64*)__builtin_amdgcn_kernarg_segment_ptr(); PINS(k); return (char*)k[i]; };
  extern __shared__ __attribute__((aligned(16))) char smem[];
  (void)smem;
  STAMP(0);
  const int b = blockIdx.x, xc = b & 7, bi = b >> 3;
  const int tid = threadIdx.x, lane = tid & 63, w = uni(tid >> 6);
  const int c = lane & 31, hh = lane >> 5;
  const bool helper = bi >= 30;

  if (w == 7) {
    u64 pc; asm volatile("s_getpc_b64 %0" : "=s"(pc));
    constexpr int PER = (CODE_LINES + 31) / 32;
    u32 ln = bi * PER + (lane % PER); ln = ln < CODE_LINES ? ln : CODE_LINES - 1;
    asm volatile("s_mov_b32 m0, %0\n\ts_nop 0\n\tglobal_load_lds_dword %1, %2" :: "s"(L_PF), "v"(ln * 128), "s"(pc & ~127ull) : "memory");
  }
  const int srow = b * 8 + w;
#define ROW_LOADS() u32x4 xa, wa; u32 xb, wb; \
    asm volatile("s_nop 4\n\tglobal_load_dwordx4 %0, %5, %6\n\tglobal_load_dword %1, %5, %6 offset:16\n\t" \
                 "global_load_dwordx4 %2, %5, %7\n\tglobal_load_dword %3, %5, %7 offset:16\n\ts_load_dwordx2 %4, %8, 0x0" \
                 : "=&v"(xa), "=&v"(xb), "=&v"(wa), "=&v"(wb), "=&s"(tag64) : "v"(lane * 20), "s"(x + srow * 640), "s"(wr), "s"(ws) : "memory")
#define ROW_STATS(n) \
    asm volatile("s_waitcnt vmcnt(" #n ") lgkmcnt(0)" : "+v"(xa), "+v"(xb), "+v"(wa), "+v"(wb), "+s"(tag64) :: "memory"); \
    tag64 = ((u64)uni((u32)((tag64 + 1) >> 32)) << 32) | uni((u32)tag64 + 1); tag = (u32)tag64; \
    row_stats(xa, xb, wa, wb, ws, srow, tag, tid)
  u64 tag64; u32 tag;
  const __amdgpu_buffer_rsrc_t wsr = __builtin_amdgcn_make_buffer_rsrc(ws, 0, WS_SPAN, 0x00020000);
  const int X = w >> 2, wl = w & 3;

  u64 sgr = 0;
#define RES_PREP(bb) u32 xo_[4]; const char* xs_; \
    { const int row_ = 64 * (((bb) & 7) + 8 * ((bb) >> 6)) + 16 * wl; \
      _Pragma("unroll") for (int n_ = 0; n_ < 3; ++n_) { const u32 P_ = 64 * n_ + lane, r_ = P_ / 10u; xo_[n_] = (r_ < 16 ? r_ : 15) * 1280 + (P_ - 10 * r_) * 16; PINV(xo_[n_]); } \
      xo_[3] = X ? WS_STATS + (row_ + (lane & 15)) * 8 : WS_TOP2 + ((bb) & 15) * 128; PINV(xo_[3]); \
      xs_ = karg(0) + (u32)(row_ * 1280 + (((bb) >> 3) & 7) * 160); PINS(xs_); }
#define RES_ISSUE() asm volatile("s_cmp_eq_u32 %8, 0\n\tglobal_load_dwordx2 %0, %1, %2 sc1\n\ts_cbranch_scc1 1f\n\t" \
                 WO_D("%7, 0", "%3", "%6") WO_D("m0, 1024", "%4", "%6") WO_D("m0, 1024", "%5", "%6") "\n1:" \
                 : "=&v"(sgr) : "v"(xo_[3]), "s"(ws), "v"(xo_[0]), "v"(xo_[1]), "v"(xo_[2]), "s"(xs_), "s"(L_XR + wl * 3072), "s"(X) : "memory", "scc")

  if (!helper) {
    ROW_LOADS();
    const int Lx = (xc >> 1) * 30 + bi, uj = Lx >> 3, rtile = (xc & 1) * 8 + (Lx & 7);
    const int uA = (uj / 5) * 10 + uj % 5;
    const int rot = (rtile * 3 + uj) % 10;
    const u32 vx = (8 * w + (lane >> 3)) * 1280 + (((lane & 7) ^ ((4 * w + (lane >> 4)) & 7)) << 4);
    const u32 vw = (8 * w + (lane >> 3)) * 3840 + (((lane & 7) ^ ((lane >> 2) & 4)) << 4);
    const char* xbase = (const char*)x + rtile * (128 * 1280);
    const char* wbase = (const char*)wqkv + uA * 128;
    auto piece = [&](int op) {
      const int a = op / 3, p = op % 3, pg = op - 30;
      int sl = (op < 30 ? a : pg) + rot; sl = sl >= 10 ? sl - 10 : sl;
      if (op >= 30) dma16(L_BP + (pg == 9 ? 6 : pg % 6) * 8192 + w * 1024, vw, wbase + 640 + sl * (64 * 3840));
      else if (p < 2) dma16((a % 4) * SLOT + p * 8192 + w * 1024, vx, xbase + sl * 128 + p * (64 * 1280));
      else dma16((a % 4) * SLOT + 16384 + w * 1024, vw, wbase + sl * (64 * 3840));
    };
    piece(0); piece(1); piece(2); piece(3); piece(4); piece(5);
    const int tt = wl, fh = X;
    const u32 key = (c >> 1) & 7;
    u32 xad[2][4];
#pragma unroll
    for (int i = 0; i < 4; ++i) { xad[0][i] = (32 * tt + c) * 128 + (((2 * i + hh) ^ key) << 4); xad[1][i] = xad[0][i] + 2 * SLOT; PINV(xad[0][i]); PINV(xad[1][i]); }
    const u32 q4 = (lane >> 2) & 3, p4 = lane & 3, g2 = (lane >> 4) & 1;
    u32 wadA = (8 * hh + q4) * 128 + (((4 * fh + 2 * (p4 & 1) + g2) ^ ((q4 >> 1) << 2)) << 4) + (p4 >> 1) * 8, wadB = wadA + L_BP, wadC = wadA + 2 * SLOT;
    const char* pfa;
    {
      const u32 l0 = (bi * 8 + w) * 38 + (lane < 37 ? lane : 37), l = l0 < 8960 ? l0 : 8959, m = l / 1920u, g = l - 1920 * m, r = g / 3u;
      const char *wg = karg(5), *wu = karg(6), *wd = karg(7), *wo = karg(4);
      pfa = m >= 3 ? wo + (l - 5760) * 128 : m == 2 ? wd + xc * (192 * 1280) + g * 128 : (m ? wu : wg) + r * 3072 + xc * 384 + (g - 3 * r) * 128;
    }
    u32 goff = WS_STATS + (rtile * 128 + 32 * tt + c) * 8;
    u32 wadT = (32 * X + 8 * (lane >> 4) + q4) * 128 + (((2 * wl + (p4 >> 1)) ^ ((q4 >> 1) << 2)) << 4) + (p4 & 1) * 8;
    u32 wadV = wadT + L_BP; wadT += 16384;
    u32 wadU = wadT + 2 * SLOT;
    u32 qoff = WS_QKV + (8 * uA + 4 * fh + 2 * hh) * 32768 + (rtile * 128 + 32 * tt + c) * 16;
    u32 csl = L_CP + (32 * fh + c) * 4, cswr = L_CP + X * 256 + (16 * wl + (lane & 15)) * 4;
    u32 csm = L_CP + 1024 + w * 128 + c * 4, csr = L_CP + 1024 + w * 128 + hh * 64;
    u32x4 onev = {0x3F803F80u, 0x3F803F80u, 0x3F803F80u, 0x3F803F80u};
    PINV(wadA); PINV(wadB); PINV(wadC); PINV(wadT); PINV(wadU); PINV(wadV); PINV(pfa); PINV(goff); PINV(qoff); PINV(csl); PINV(cswr); PINV(csm); PINV(csr); PINV(onev);
    u32 wv[8];
    wo_offsets(wv, wl, lane);
    ROW_STATS(6);
    if ((P1X & 8) && X == 1) __builtin_amdgcn_s_setprio(1);
    SB0();
    s16x8 xf[40], wf[2][4], wt[8], cf, cg;
    const s16x8 ones = __builtin_bit_cast(s16x8, onev);
    f32x16 acc, acb;
    f32x4 acs = {0.f, 0.f, 0.f, 0.f}, act = acs;
#pragma unroll
    for (int i = 0; i < 16; ++i) { acc[i] = 0.f; acb[i] = 0.f; }
    u64 gr = 0;
    float mneg = 0.f, rsq = 0.f;
    u32 qscb = uA < 10 ? 0x3ea52b5cu : 0x3f800000u;
    PINS(qscb);
    u32 eo[8];
    f32x4 cp[4];
    float c0 = 0.f, c1 = 0.f;
    auto epi2 = [&](float a0, float a1, float k0, float k1) {
      u32 r; float t;
      asm("v_fma_f32 %0, %2, %4, %6\n\tv_fma_f32 %1, %2, %5, %7\n\tv_mul_f32 %0, %3, %0\n\tv_mul_f32 %1, %3, %1\n\tv_cvt_pk_bf16_f32 %0, %0, %1"
          : "=&v"(r), "=&v"(t) : "v"(mneg), "v"(rsq), "v"(k0), "v"(k1), "v"(a0), "v"(a1));
      return r;
    };
    auto epi_store = [&](int second, int k) {
      __builtin_amdgcn_raw_buffer_store_b128((u32x4){eo[4 * k], eo[4 * k + 1], eo[4 * k + 2], eo[4 * k + 3]}, wsr, qoff + k * 32768 + second * (40 * 32768), 0, 16);
    };
    static_for<0, 21>([&](auto uc) __attribute__((always_inline)) {
      constexpr int u = decltype(uc)::v;
      constexpr int sa = slice_of(u), sb = page_of(u), pa = slice_of(u - 1), pb = page_of(u - 1);
      constexpr int G = u < 7 ? u : u < 9 ? -1 : u < 14 ? u - 2 : u == 16 ? 12 : u == 19 ? 13 : -1;
      if constexpr (u < 20 && need_op(u) >= 0) WAITBARV(younger(need_op(u), u));
      if (u == 7) asm volatile("s_barrier" ::: "memory");
      if (u == 15) asm volatile("s_waitcnt lgkmcnt(6)\n\ts_barrier" ::: "memory");
      if (u == 19) asm volatile("s_waitcnt lgkmcnt(4)\n\ts_barrier" ::: "memory");
      if (G == 11) {
        PINV(gr);
        u32 pay = (u32)gr;
        if (!P1X && !__all((u32)(gr >> 32) == tag)) pay = poll_gran(ws, goff, tag);
        mneg = -blo(pay); rsq = __builtin_amdgcn_rcpf(bhi(pay)) * __uint_as_float(qscb); PINV(mneg); PINV(rsq);
      }
      if (u >= UOFF && u < UOFF + 16) STAMP(16 + (u >= UOFF && u < UOFF + 16 ? u - UOFF : 0));
      SB0();
#pragma unroll
      for (int i = 0; i < 4; ++i) {
        if constexpr (u > 0) {
          if constexpr (pa >= 0) acc = mfma32(wf[(u - 1) & 1][i], xf[4 * pa + i], acc);
          else acb = mfma32(u > 13 ? wt[(4 * (u - 14) + i) & 7] : wf[(u - 1) & 1][i], xf[4 * pb + i], acb);
        }
        SB0();
        if constexpr (u < 20) {
          if constexpr (sa >= 0) {
            const u32 wbs = (sa & 2 ? wadC : wadA) + (sa & 1) * SLOT + 16384 + i * 2048;
            xf[4 * sa + i] = lds128(xad[(sa >> 1) & 1][i] + (sa & 1) * SLOT); wf[u & 1][i] = ldstr(wbs, wbs + 512);
          } else if constexpr (u < 13) { const u32 pbase = wadB + sb * 8192 + i * 2048; wf[u & 1][i] = ldstr(pbase, pbase + 512); }
          else {
            auto rd = [&](int f) { const int pn = 3 + f / 4; const u32 pbase = wadB + (pn == 9 ? 6 : pn % 6) * 8192 + (f & 3) * 2048; wt[f & 7] = ldstr(pbase, pbase + 512); };
            if constexpr (u == 13) { rd(i == 0 ? 0 : i == 1 ? 2 : i == 2 ? 3 : 5); if (!(i & 1)) rd(i == 0 ? 1 : 4); }
            else if (4 * (u - 14) + i + 6 < 28) rd(4 * (u - 14) + i + 6);
          }
        }
        if (i == 3) {
          if constexpr (u > 0 && pa >= 0) acs = mfma16(ones, cf, acs);
          if constexpr ((u >= 7 && u <= 9) || (u >= 14 && u <= 17)) act = mfma16(ones, cf, act);
          if constexpr (sa >= 0) { const u32 cb = (sa & 2 ? wadU : wadT) + (sa & 1) * SLOT; cf = ldstr(cb, cb + 512); }
          if constexpr (u >= 6 && u <= 8) cf = ldstr(wadV + sb * 8192, wadV + sb * 8192 + 512);
          if constexpr (u >= 13 && u <= 16) { constexpr int pn = 3 + 2 * (u - 13); const u32 cb = wadV + (pn == 9 ? 6 : pn % 6) * 8192; cf = ldstr(cb, cb + 512); }
        }
        if (i == 1) {
          if constexpr (u >= 14 && u <= 16) act = mfma16(ones, cg, act);
          if constexpr (u >= 13 && u <= 15) { constexpr int pn = 4 + 2 * (u - 13); const u32 cb = wadV + (pn % 6) * 8192; cg = ldstr(cb, cb + 512); }
        }
        const int op = sched_op(u, i);
        if (op >= 0 && op < 40) piece(op);
        if (op == OP_GR) asm volatile("global_load_dwordx2 %0, %1, %2 sc1" : "=v"(gr) : "v"(goff), "s"(ws) : "memory");
        if (op == OP_PF) asm volatile("s_mov_b32 m0, %0\n\ts_nop 0\n\tglobal_load_lds_dword %1, off" :: "s"(L_PF), "v"(pfa) : "memory");
        if constexpr (u == 14 || u == 18) { if (i == 1) *lp<float>(cswr + (u == 14 ? 0 : 512)) = u == 14 ? acs[0] : act[0]; }
        if constexpr (u == 15 || u == 19) {
          constexpr u32 o = u == 15 ? 0 : 512;
          if (i == 0) { c0 = *lp<float>(csl + o); c1 = *lp<float>(csl + o + 256); }
          if (i == 2) *lp<float>(csm) = c0 + c1;
          if (i == 3) {
#pragma unroll
            for (int k = 0; k < 4; ++k) cp[k] = *lp<f32x4>(csr + 16 * k);
          }
        }
        if constexpr (u == 16 || u == 17) {
          constexpr int q = 2 * (u - 16);
          eo[2 * q + i] = epi2(acc[4 * q + 2 * i], acc[4 * q + 2 * i + 1], cp[q + (i >> 1)][2 * (i & 1)], cp[q + (i >> 1)][2 * (i & 1) + 1]);
          if (i == 3) epi_store(0, u - 16);
        }
        SB0();
      }
    });
    STAMP(4);
    u32 fl = WS_QFLAG + (rtile * 30 + uA) * 128; u64 tagv = tag64;
    PINV(fl); PINV(tagv);
#pragma unroll
    for (int i = 0; i < 4; ++i) {
      eo[2 * i]     = pk(fmaf(mneg, cp[i][0], acb[4 * i]) * rsq, fmaf(mneg, cp[i][1], acb[4 * i + 1]) * rsq);
      eo[2 * i + 1] = pk(fmaf(mneg, cp[i][2], acb[4 * i + 2]) * rsq, fmaf(mneg, cp[i][3], acb[4 * i + 3]) * rsq);
      if (i & 1) epi_store(1, i >> 1);
    }
    SB0();
    STAMP(3);
    WAITBAR(2);
    if (w == 0) store_flag(ws, fl, tagv, 1);
    const char* woc = karg(4) + (bi & 7) * 160; PINS(woc);
    if (X == 1) stage_wo<0>(woc, 0, wl, wv);
    else {
      const u32 l = (bi * 4 + wl) * 27 + (lane < 26 ? lane : 26);
      asm volatile("s_mov_b32 m0, %0\n\ts_nop 4\n\tglobal_load_lds_dword %1, %2" :: "s"(L_PF), "v"((3200 + (l < 3200 ? l : 3199)) * 128), "s"(woc - (bi & 7) * 160) : "memory");
    }
    {
    int b2 = blockIdx.x; PINS(b2);
    const int xc = b2 & 7, bi = b2 >> 3;
    const int odd = bi >= 15, hl = 2 * xc + odd, grp = bi - 15 * odd, head = 16 * X + hl;
    const int oc0 = 5 * (8 * X + xc);
    const int oA = oc0 + 3 * odd, oG = oc0 + 2;
    const u32 kv = X == 0 ? L_KVA : L_KVB;
    const int g0 = grp == 0;
    u32 pollo;
    {
      const int l4 = lane & 15, isq = (l4 + 4) >> 4, b1 = (l4 >> 1) & 1, ri = l4 >> 2;
      const int oct = oc0 + 2 * odd + 2 * (l4 & 1) + (80 + 80 * b1) * (1 - isq);
      const int rt = g0 ? (isq ? b1 : (ri + 1) >> 1) : grp - 1 + ri - isq;
      pollo = WS_QFLAG + (rt * 30 + (oct >> 3)) * 128;
    }
    const int t0 = g0 ? wl : 4 * (grp + 1) + wl, t1 = g0 ? 7 - wl : t0;
    u32 qo0 = WS_QKV + (oA + hh) * 32768 + c * 16 + t0 * 512, qo1 = WS_QKV + oG * 32768 + c * 16 + t0 * 512, l16 = lane * 16;
    u32 qd = (t1 - t0) * 512;
    const int tp0 = (3 - g0) * (wl >> 1);
    const char* kvs = ws + (u32)(WS_QKV + 80 * (1 + (wl & 1)) * 32768 + ((g0 ? 0 : 128 * (grp - 1)) + 64 * tp0) * 16);
    const char* sA = kvs + (u32)(oA * 32768); const char* sB = sA + 32768; const char* sG = kvs + (u32)(oG * 32768);
    u32 mA = kv + 3 * (wl & 1) * PLANE + tp0 * 1024;
    RES_PREP(b2);
    fl += 5 * 128;
    PINV(pollo); PINV(qo0); PINV(qo1); PINV(l16); PINV(fl); PINS(sA); PINS(sB); PINS(sG); PINS(mA); PINS(qd);
    asm volatile("s_waitcnt vmcnt(6) lgkmcnt(0)\n\ts_cmp_ge_u32 %0, 4\n\ts_cbranch_scc1 1f\n\ts_waitcnt vmcnt(2)\n\ts_cmp_eq_u32 %0, 0\n\ts_cbranch_scc1 1f\n\ts_waitcnt vmcnt(1)\n1:\n\ts_barrier"
                 :: "s"(w) : "memory", "scc");
    if (w == 4) store_flag(ws, fl, tagv, 1);
    STAMP(5);
    RES_ISSUE();
    if (X == 1) stage_wo<1>(woc, 0, wl, wv);
    __builtin_amdgcn_s_setprio(1);
    u32x4 qa, qb, qa2, qb2;
    {
      u64 pt; u32 pn;
      asm volatile(POLL_TXT("%4", "%5", "%6", "%7", "%8")
                   "s_mov_b32 m0, %9\n\tglobal_load_dwordx4 %0, %14, %7 sc1\n\tglobal_load_lds_dwordx4 %13, %10 sc1\n\tglobal_load_lds_dwordx4 %13, %10 offset:1024 sc1\n\t"
                   "s_add_u32 m0, %9, 6208\n\tglobal_load_dwordx4 %1, %15, %7 sc1\n\tglobal_load_lds_dwordx4 %13, %11 sc1\n\tglobal_load_lds_dwordx4 %13, %11 offset:1024 sc1\n\t"
                   "s_add_u32 m0, %9, 12416\n\ts_cmp_lg_u32 %16, 0\n\tglobal_load_lds_dwordx4 %13, %12 sc1\n\tglobal_load_lds_dwordx4 %13, %12 offset:1024 sc1\n\t"
                   "s_cbranch_scc1 3f\n\t"
                   "global_load_lds_dwordx4 %13, %12 offset:2048 sc1\n\ts_mov_b32 m0, %9\n\ts_nop 0\n\tglobal_load_lds_dwordx4 %13, %10 offset:2048 sc1\n\t"
                   "s_add_u32 m0, %9, 6208\n\ts_nop 0\n\tglobal_load_lds_dwordx4 %13, %11 offset:2048 sc1\n\ts_branch 4f\n"
                   "3:\n\tv_add_u32 %14, %16, %14\n\tv_add_u32 %15, %16, %15\n\tglobal_load_dwordx4 %2, %14, %7 sc1\n\tglobal_load_dwordx4 %3, %15, %7 sc1\n"
                   "4:"
                   : "=&v"(qa), "=&v"(qb), "=&v"(qa2), "=&v"(qb2), "=&v"(pt), "=&s"(pn), "+v"(pollo), "+s"(ws), "+s"(tag64), "+s"(mA), "+s"(sA), "+s"(sB), "+s"(sG),
                     "+v"(l16), "+v"(qo0), "+v"(qo1), "+s"(qd)
                   :: "memory", "scc", "vcc");
    }
    u32 mk[16], dm[8];
    {
      int d = 4 * hh - 1 - c;
#pragma unroll
      for (int i = 0; i < 16; ++i) asm("v_add_u32 %0, %2, %1\n\tv_ashrrev_i32 %0, 31, %0" : "=&v"(mk[i]) : "v"(d), "i"((i & 3) + 8 * (i >> 2)));
    }
#pragma unroll
    for (int i = 0; i < 8; ++i) dm[i] = (mk[2 * i] & 0xffffu) | (mk[2 * i + 1] & 0xffff0000u);
    u32 hmask = (u32)-hh, kml = (u32)((hh | odd) - 1), kmh = (u32)((hh | (odd ^ 1)) - 1);
    const u32 realv = (u32)(((g2 & ((p4 | (p4 >> 1)) & 1))) - 1);
    u32 vlane = hh * 64 + q4 * 16 + ((realv & (kv + (3 + 2 * g2 + (p4 & 1)) * PLANE + (g2 * odd + (p4 >> 1)) * 8)) | (~realv & L_ONE));
    u32 vstep = realv & 512u;
    const int dl0 = g0 ? 0 : 8 + wl;
    u32 vold = vlane + (realv & (u32)(wl * 512));
    vlane += realv & (u32)(dl0 * 512);
    u32 kla = kv + hh * PLANE + c * 16 + (dl0 - 8) * 512, klb = kv + 2 * PLANE + c * 16 + (dl0 - 8) * 512;
    u32 oo = WS_O + X * (1u << 20) + t0 * 16384 + (c >> 4) * 8192 + xc * 1024 + (2 * odd + hh) * 256 + (c & 15) * 16;
    u32 co4 = (WS_C4 + X * (1u << 18) + t0 * 4096 + (c >> 4) * 2048 + hl * 128 + (c & 15) * 8) | (hmask & 0x80000000u);
    u32 ofl = WS_OFLAG + (t0 * 32 + head) * 128 + (lane & 1) * (qd * 8);
    u32 cnta = L_CNT + 4 * X, one = 1, tmpv, tmps;
    asm volatile("s_waitcnt vmcnt(0)\n\ts_mov_b64 exec, 1\n\tds_add_u32 %2, %3\n\ts_mov_b64 exec, -1\n"
                 "5:\n\tds_read_b32 %0, %2\n\ts_waitcnt lgkmcnt(0)\n\tv_readfirstlane_b32 %1, %0\n\ts_cmp_ge_u32 %1, 4\n\ts_cbranch_scc0 5b"
                 : "=&v"(tmpv), "=&s"(tmps), "+v"(cnta), "+v"(one), "+v"(qa), "+v"(qb), "+v"(qa2), "+v"(qb2), "+v"(sgr),
                   "+v"(kml), "+v"(kmh), "+v"(hmask), "+v"(vlane), "+v"(vold), "+v"(vstep), "+v"(kla), "+v"(klb), "+v"(oo), "+v"(co4), "+v"(ofl),
                   "+v"(dm[0]), "+v"(dm[1]), "+v"(dm[2]), "+v"(dm[3]), "+v"(dm[4]), "+v"(dm[5]), "+v"(dm[6]), "+v"(dm[7])
                 :: "memory", "scc");
    STAMP(7);
    {
      auto in_range = [&](const f32x16& O) {
        const float l = bsel(O[12], O[8], hmask);
        const float t = ((O[0] + O[1]) + (O[2] + O[3])) + ((O[4] + O[5]) + (O[6] + O[7])) + ((O[8] + O[9]) + (O[10] + O[11]));
        return __all(l > 0x1p-40f && l < 0x1p40f && __builtin_fabsf(t) < __builtin_inff());
      };
      auto put = [&](const f32x16& O, u32 ao, u32 ac) {
        const float inv = __builtin_amdgcn_rcpf(bsel(O[12], O[8], hmask));
        __builtin_amdgcn_raw_buffer_store_b128((u32x4){pk(O[0] * inv, O[1] * inv), pk(O[2] * inv, O[3] * inv), pk(O[4] * inv, O[5] * inv), pk(O[6] * inv, O[7] * inv)}, wsr, ao, 0, 16);
        __builtin_amdgcn_raw_buffer_store_b64((u32x2){pk(O[8] * inv, O[9] * inv), pk(O[10] * inv, O[11] * inv)}, wsr, ac, 0, 16);
      };
      bool general = ALWAYS_MAX || g0;
      if (!general) {
        const f32x16 O = attn_item<true>(__builtin_bit_cast(s16x8, qa), __builtin_bit_cast(s16x8, (u32x4){qb[0] & kml, qb[1] & kml, qb[2] & kmh, qb[3] & kmh}),
                                         kla, klb, 0, vlane, vold, vstep, dm, mk, 7, 0.f, 0.f);
        STAMP(12);
        if (in_range(O)) put(O, oo, co4); else general = true;
      }
      if (general) {
        int cc = c, hc = hh; PINV(cc); PINV(hc);
        NOUNROLL for (int it = 0; it <= g0; ++it) {
          const s16x8 q0 = __builtin_bit_cast(s16x8, qa);
          const s16x8 q1 = __builtin_bit_cast(s16x8, (u32x4){qb[0] & kml, qb[1] & kml, qb[2] & kmh, qb[3] & kmh});
          const int tq = it ? t1 : t0, nfull = g0 ? tq : 7;
          const u32 kd = g0 ? tq * 512 : 0, ka = kla + kd, kb = klb + kd, va = vlane + (vstep ? kd : 0u);
          bool shift = ALWAYS_MAX || !g0;
          f32x16 O;
          NOUNROLL for (;;) {
            const float negm = shift ? attn_negmax(q0, q1, ka, kb, nfull, !g0, cc, hc) : 0.f;
            O = attn_item<false>(q0, q1, ka, kb, g0 ? 4096u : 0u, va, g0 ? va : vold, vstep, dm, mk, nfull, negm, g0 ? -__builtin_inff() : negm);
            if (shift || in_range(O)) break;
            shift = true;
          }
          put(O, oo + (tq - t0) * 16384, co4 + (tq - t0) * 4096);
          qa = qa2; qb = qb2;
        }
      }
    }
    STAMP(8);
    WAITV(0);
    store_flag(ws, ofl, tagv, g0 ? 3 : 1);
    STAMP(9);
    if (X == 0) {
      lds_arrive(L_CNT + 8);
      __builtin_amdgcn_s_setprio(0);
      lds_wait(L_CNT + 8, 4);
      stage_wo<2>(woc, 1, wl, wv);
    } else {
      lds_arrive(L_CNT + 20);
    }
    }
  } else {
    ROW_LOADS();
    const int hg = xc * 2 + (bi - 30);
    const char *wg = karg(5), *wu = karg(6), *wd = karg(7);
    u32x4 hv[40];
    const int pc = (lane & 15) - (hg & 1) * 4, rg = lane >> 4;
    const bool act = pc >= 0 && pc < 12;
    const u32 gvo = (8 * w + 2 * rg) * 3072 + hg * 192 + (act ? pc : 0) * 16;
#define GU_ISSUE(n, r) asm volatile("global_load_dwordx4 %0, %4, %5\n\tglobal_load_dwordx4 %1, %4, %5 offset:3072\n\t" \
                       "global_load_dwordx4 %2, %4, %6\n\tglobal_load_dwordx4 %3, %4, %6 offset:3072" \
                       : "=&v"(hv[4 * (r)]), "=&v"(hv[4 * (r) + 1]), "=&v"(hv[4 * (r) + 2]), "=&v"(hv[4 * (r) + 3]) \
                       : "v"(gvo), "s"((const char*)wg + (n) * (64 * 3072)), "s"((const char*)wu + (n) * (64 * 3072)) : "memory")
    GU_ISSUE(0, 0); GU_ISSUE(1, 1);
    ROW_STATS(8);
    GU_ISSUE(2, 2); GU_ISSUE(3, 3); GU_ISSUE(4, 4);
    u32x4 ra, rb;
    const u32 roff = WS_RR + tid * 32;
    asm volatile("global_load_dwordx4 %0, %2, %3 sc1\n\tglobal_load_dwordx4 %1, %2, %3 offset:16 sc1" : "=&v"(ra), "=&v"(rb) : "v"(roff), "s"(ws) : "memory");
    STAMP(2);
    if (w >= 2) { GU_ISSUE(5, 5); }
    asm volatile("s_waitcnt vmcnt(4)\n\ts_cmp_ge_u32 %2, 2\n\ts_cbranch_scc1 1f\n\ts_waitcnt vmcnt(0)\n1:" : "+v"(ra), "+v"(rb) : "s"(w) : "memory", "scc");
    NOUNROLL for (u32 sp = 0; sp < SPIN_CAP && !__all(ra[1] == tag && ra[3] == tag && rb[1] == tag && rb[3] == tag); ++sp) {
      __builtin_amdgcn_s_sleep(1);
      asm volatile("global_load_dwordx4 %0, %2, %3 sc1\n\tglobal_load_dwordx4 %1, %2, %3 offset:16 sc1\n\ts_waitcnt vmcnt(0)" : "=&v"(ra), "=&v"(rb) : "v"(roff), "s"(ws) : "memory");
    }
    auto mkey = [](u32 f, u32 row) { return ((f ^ (((int)f >> 31) | 0x80000000u)) & 0xfffff800u) | (2047u - row); };
    auto mx = [](u32 p, u32 q) { return p > q ? p : q; };
    auto mn = [](u32 p, u32 q) { return p < q ? p : q; };
    u32 k1, k2;
    {
      const u32 e0 = mkey(ra[0], 4 * tid), e1 = mkey(ra[2], 4 * tid + 1), e2 = mkey(rb[0], 4 * tid + 2), e3 = mkey(rb[2], 4 * tid + 3);
      k1 = mx(mx(e0, e1), mx(e2, e3));
      k2 = mx(mn(mx(e0, e1), mx(e2, e3)), mx(mn(e0, e1), mn(e2, e3)));
    }
#define TOP2_STEP(o1, o2) do { const u32 p_ = (o1), q_ = (o2); k2 = mx(mn(k1, p_), mx(k2, q_)); k1 = mx(k1, p_); } while (0)
#define TOP2_DPP(C) TOP2_STEP(__builtin_amdgcn_mov_dpp(k1, C, 0xf, 0xf, true), __builtin_amdgcn_mov_dpp(k2, C, 0xf, 0xf, true))
    TOP2_DPP(0xB1); TOP2_DPP(0x4E); TOP2_DPP(0x141); TOP2_DPP(0x140);
    LAS u32x2* kk = lp<u32x2>(4096);
    if ((lane & 15) == 0) kk[4 * w + rg] = (u32x2){k1, k2};
    BAR();
    { const u32x2 e = kk[lane & 15], f = kk[16 + (lane & 15)]; k1 = e[0]; k2 = e[1]; TOP2_STEP(f[0], f[1]); }
    TOP2_DPP(0xB1); TOP2_DPP(0x4E); TOP2_DPP(0x141); TOP2_DPP(0x140);
    k1 = uni(k1); k2 = uni(k2);
    if (w >= 2) { GU_ISSUE(6, 6); GU_ISSUE(7, 7); }
    const u32 i0 = 2047u - (k1 & 2047u), i1 = 2047u - (k2 & 2047u);
    if (tid == 0) { gst64(ws, WS_TOP2 + hg * 128, ((u64)tag << 32) | i0 | (i1 << 16)); if (hg == 15) gst64(ws, WS_EPOCH, tag64); }
    STAMP(3);
    LAS u32* xp = lp<u32>(8192);
    if (w < 2) {
      const u32 row = w ? i1 : i0;
      u32 xrow[5];
#pragma unroll
      for (int i = 0; i < 5; ++i) xrow[i] = ((const u32*)x)[row * 320 + i * 64 + lane];
      const u32 sg = poll_gran(ws, WS_STATS + row * 8, tag);
      const float mb = blo(sg), rs = __builtin_amdgcn_rcpf(bhi(sg));
#pragma unroll
      for (int i = 0; i < 5; ++i) {
        const u32 t = pk(blo(xrow[i]) - mb, bhi(xrow[i]) - mb);
        xp[(i * 64 + lane) * 2 + w] = pk(blo(t) * rs, bhi(t) * rs);
      }
      GU_ISSUE(5, 5); GU_ISSUE(6, 6); GU_ISSUE(7, 7);
    }
    BAR();
    float ga[32];
#pragma unroll
    for (int i = 0; i < 32; ++i) ga[i] = 0.f;
#pragma unroll
    for (int n = 0; n < 10; ++n) {
      const int r = n;
      if (n == 1) GU_ISSUE(8, 8);
      if (n == 2) GU_ISSUE(9, 9);
      if (n >= 3 && n < 8) {
#pragma unroll
        for (int k = 3 * (n - 3); k < 3 * (n - 2); ++k) dma16(24576 + (k * 8 + w) * 1024, lane * 16, (const char*)wd + hg * (96 * 1280) + (k * 8 + w) * 1024);
      }
      asm volatile("s_waitcnt vmcnt(%4)" : "+v"(hv[4 * r]), "+v"(hv[4 * r + 1]), "+v"(hv[4 * r + 2]), "+v"(hv[4 * r + 3]) : "i"(n < 5 ? 63 : 4 * (9 - n) + (n < 7 ? 3 * (n - 2) : 15)) : "memory");
      const u32x2 xv = *(const LAS u32x2*)(xp + (32 * n + 4 * w + rg) * 2);
      const bf16x2 x0 = asb(xv[0]), x1 = asb(xv[1]);
#pragma unroll
      for (int m = 0; m < 2; ++m)
#pragma unroll
        for (int i = 0; i < 4; ++i) {
          const bf16x2 lo = asb(__builtin_amdgcn_perm(hv[4 * r + 2 * m + 1][i], hv[4 * r + 2 * m][i], 0x05040100u));
          const bf16x2 hi = asb(__builtin_amdgcn_perm(hv[4 * r + 2 * m + 1][i], hv[4 * r + 2 * m][i], 0x07060302u));
          ga[16 * m + 2 * i] = __builtin_amdgcn_fdot2_f32_bf16(lo, x0, ga[16 * m + 2 * i], false);
          ga[16 * m + 8 + 2 * i] = __builtin_amdgcn_fdot2_f32_bf16(lo, x1, ga[16 * m + 8 + 2 * i], false);
          ga[16 * m + 2 * i + 1] = __builtin_amdgcn_fdot2_f32_bf16(hi, x0, ga[16 * m + 2 * i + 1], false);
          ga[16 * m + 8 + 2 * i + 1] = __builtin_amdgcn_fdot2_f32_bf16(hi, x1, ga[16 * m + 8 + 2 * i + 1], false);
        }
#pragma unroll
      for (int i = 0; i < 32; i += 8) asm volatile("" : "+v"(ga[i]), "+v"(ga[i + 1]), "+v"(ga[i + 2]), "+v"(ga[i + 3]), "+v"(ga[i + 4]), "+v"(ga[i + 5]), "+v"(ga[i + 6]), "+v"(ga[i + 7]));
    }
    STAMP(4);
    LAS float* red = lp<float>(11264);
    {
      float h16[16], h8[8];
#pragma unroll
      for (int i = 0; i < 16; ++i) { auto r = __builtin_amdgcn_permlane32_swap(__float_as_uint(ga[i]), __float_as_uint(ga[i + 16]), false, false); h16[i] = __uint_as_float(r[0]) + __uint_as_float(r[1]); }
#pragma unroll
      for (int i = 0; i < 8; ++i) { auto r = __builtin_amdgcn_permlane16_swap(__float_as_uint(h16[i]), __float_as_uint(h16[i + 8]), false, false); h8[i] = __uint_as_float(r[0]) + __uint_as_float(r[1]); }
      if (act) {
        LAS float* d = red + w * 384 + rg * 96 + pc * 8;
        *(LAS f32x4*)d = (f32x4){h8[0], h8[1], h8[2], h8[3]}; *(LAS f32x4*)(d + 4) = (f32x4){h8[4], h8[5], h8[6], h8[7]};
      }
    }
    BAR();
    STAMP(6);
    LAS u16* hs = lp<u16>(4096);
    if (tid < 192) {
      float g = 0.f, u = 0.f;
#pragma unroll
      for (int k = 0; k < 8; ++k) { g += red[k * 384 + tid]; u += red[k * 384 + 192 + tid]; }
      g = rbf(g); u = rbf(u);
      const float hval = rbf(g * __builtin_amdgcn_rcpf(1.f + __expf(-g))) * u;
      const int t = tid >= 96, f = tid - 96 * t;
      hs[(f >> 1) * 4 + t * 2 + (f & 1)] = (u16)pk(hval, hval);
    }
    STAMP(12);
    WAITBAR(0);
    STAMP(7);
    float y[2][8];
#pragma unroll
    for (int i = 0; i < 8; ++i) y[0][i] = y[1][i] = 0.f;
    const int rgp = tid / 80, cp = tid - 80 * rgp;
    if (tid < 480) {
#pragma unroll
      for (int i = 0; i < 8; ++i) {
        const u32 ad = 24576 + (12 * i + 2 * rgp) * 1280 + cp * 16;
        const u32x4 d0 = *lp<u32x4>(ad), d1 = *lp<u32x4>(ad + 1280);
        const u32x2 hp = *lp<u32x2>(4096 + (6 * i + rgp) * 8);
        const bf16x2 h0 = asb(hp[0]), h1 = asb(hp[1]);
#pragma unroll
        for (int k = 0; k < 4; ++k) {
          const bf16x2 dl_ = asb(__builtin_amdgcn_perm(d1[k], d0[k], 0x05040100u));
          const bf16x2 dh_ = asb(__builtin_amdgcn_perm(d1[k], d0[k], 0x07060302u));
          y[0][2 * k] = __builtin_amdgcn_fdot2_f32_bf16(dl_, h0, y[0][2 * k], false); y[1][2 * k] = __builtin_amdgcn_fdot2_f32_bf16(dl_, h1, y[1][2 * k], false);
          y[0][2 * k + 1] = __builtin_amdgcn_fdot2_f32_bf16(dh_, h0, y[0][2 * k + 1], false); y[1][2 * k + 1] = __builtin_amdgcn_fdot2_f32_bf16(dh_, h1, y[1][2 * k + 1], false);
        }
      }
    }
    STAMP(8);
    BAR();
    LAS float* r3 = lp<float>(24576);
    if (tid < 480) {
#pragma unroll
      for (int t = 0; t < 2; ++t) {
        LAS float* d = r3 + rgp * 1280 + t * 640 + cp * 8;
        *(LAS f32x4*)d = (f32x4){y[t][0], y[t][1], y[t][2], y[t][3]}; *(LAS f32x4*)(d + 4) = (f32x4){y[t][4], y[t][5], y[t][6], y[t][7]};
      }
    }
    BAR();
    if (tid < 320) {
      const u32 kq = (tid >= 160 ? k2 : k1) & 0xfffff800u;
      const float rsel = rbf(__uint_as_float(kq ^ ((kq >> 31) ? 0x80000000u : ~0u)));
      const float sg = rbf(__builtin_amdgcn_rcpf(1.f + __expf(-rsel)));
      f32x4 s = *(const LAS f32x4*)(r3 + tid * 4);
#pragma unroll
      for (int k = 1; k < 6; ++k) s += *(const LAS f32x4*)(r3 + k * 1280 + tid * 4);
      const u32 off = WS_Y + hg * 10240 + tid * 32;
      __builtin_amdgcn_raw_buffer_store_b128((u32x4){__float_as_uint(s[0] * sg), tag, __float_as_uint(s[1] * sg), tag}, wsr, off, 0, 16);
      __builtin_amdgcn_raw_buffer_store_b128((u32x4){__float_as_uint(s[2] * sg), tag, __float_as_uint(s[3] * sg), tag}, wsr, off + 16, 0, 16);
    }
    BAR();
    STAMP(5);
    u32 wv[8];
    wo_offsets(wv, wl, lane);
    RES_PREP(b);
    RES_ISSUE();
    stage_wo<2>(karg(4) + (bi & 7) * 160, X, wl, wv);
    asm volatile("s_waitcnt vmcnt(0) lgkmcnt(0)\n\ts_barrier" : "+v"(sgr) :: "memory");
    if (X == 1) { lds_arrive(L_CNT + 20); lds_arrive(L_CNT + 4); }
    STAMP(9);
  }

  {
    int b3 = blockIdx.x; PINS(b3);
    const int rt3 = (b3 & 7) + 8 * (b3 >> 6), ct3 = (b3 >> 3) & 7;
    const int tk = lane & 15, g = lane >> 4;
    const int t16 = 4 * rt3 + wl, row = 16 * t16 + tk;
    const u32 wfa = (8 * g + 4 * (g & 1) + ((lane >> 2) & 3)) * 160 + (lane & 3) * 8, wfb = (8 * g + 4 * ((g & 1) ^ 1) + ((lane >> 2) & 3)) * 160 + (lane & 3) * 8;
    const u32 res = L_KVB + wl * 5120 + lane * 16;
    f32x4 acc[5];
    const u32 wb0 = X == 0 ? L_WOA : L_KVA, wb7 = X == 0 ? L_WOA + 7 * 5120 : L_WOB7;
    char* out = karg(8);
    u32 ob = WS_O + X * (1u << 20) + t16 * 8192 + lane * 16, cb = WS_C4 + X * (1u << 18) + t16 * 2048 + g * 256 + tk * 8;
    u32 ofo = (row * 640 + 80 * ct3 + 16 * (g & 1) + 8 * (g >> 1)) * 2, ofo2 = (row * 640 + 80 * ct3 + 64 + 4 * g) * 2;
    u32 w8a = wb7 + 5120 + wfa, w8b = wb7 + 5120 + wfb, resv = res;
    u32 ob2 = ob + 4096, pfo = WS_OFLAG + ((t16 >> 1) * 32 + 16 * X + tk) * 128;
    u32 w0a = wb0 + wfa, w0b = wb0 + wfb;
    PINV(ofo); PINV(ofo2); PINV(w0a); PINV(w0b); PINV(w8a); PINV(w8b); PINV(resv); PINV(ob); PINV(ob2); PINV(cb); PINV(pfo); PINS(out);
    bool early = false;
    if (X == 0) {
      u32 tp = (u32)sgr;
      if (!__all((u32)(sgr >> 32) == tag)) tp = poll_gran(ws, WS_TOP2 + (b3 & 15) * 128, tag);
      tp = uni(tp);
#pragma unroll
      for (int t = 0; t < 5; ++t) acc[t] = (f32x4){0.f, 0.f, 0.f, 0.f};
      early = (int)((tp & 0xffff) >> 4) == t16 || (int)(tp >> 20) == t16;
      if (early) { WAITV(0); lds_arrive(L_CNT + 12); __builtin_amdgcn_s_setprio(1); }
#pragma unroll 1
      for (int cidx = 0; cidx < 2; ++cidx) {
        const u32 id = cidx ? tp >> 16 : tp & 0xffff;
        if ((int)(id >> 4) != t16) continue;
        LAS float* yb = lp<float>(L_YB + wl * 320);
#pragma unroll 1
        for (int p = 0; p < 2; ++p) {
          const int col = p * 64 + lane;
          const u32 off = WS_Y + cidx * 5120 + (80 * ct3 + (col < 80 ? col : 79)) * 8;
          float s = 0.f;
          NOUNROLL for (u32 sp = 0; sp < SPIN_CAP; ++sp) {
            u64 gv[16]; bool ok = true; s = 0.f;
#pragma unroll
            for (int k = 0; k < 16; ++k) gv[k] = gld64(ws, off + k * 10240);
#pragma unroll
            for (int k = 0; k < 16; ++k) { ok = ok && (u32)(gv[k] >> 32) == tag; s += __uint_as_float((u32)gv[k]); }
            if (__all(ok)) break;
            __builtin_amdgcn_s_sleep(1);
          }
          if (col < 80) yb[col] = s;
        }
        asm volatile("s_waitcnt lgkmcnt(0)" ::: "memory");
        if (tk == (int)(id & 15)) {
#pragma unroll
          for (int t = 0; t < 5; ++t) acc[t] += *(const LAS f32x4*)(yb + 16 * t + 4 * g);
        }
        asm volatile("s_waitcnt lgkmcnt(0)" ::: "memory");
      }
    } else {
      u32 sg = (u32)sgr;
      if (!__all((u32)(sgr >> 32) == tag)) sg = poll_gran(ws, WS_STATS + row * 8, tag);
      const float mb = blo(sg), rs = __builtin_amdgcn_rcpf(bhi(sg));
#pragma unroll
      for (int t = 0; t < 5; ++t) {
        const u32x2 xr = *lp<u32x2>(L_XR + wl * 3072 + tk * 160 + g * 8 + t * 32);
#pragma unroll
        for (int i = 0; i < 2; ++i) {
          const float a0 = blo(xr[i]), a1 = bhi(xr[i]);
          const u32 d = pk(a0 - mb, a1 - mb), n = pk(blo(d) * rs, bhi(d) * rs);
          acc[t][2 * i] = a0 + blo(n); acc[t][2 * i + 1] = a1 + bhi(n);
        }
        PINV(acc[t]);
      }
    }
    STAMP(1);
    lds_wait(X == 0 ? L_CNT + 4 : L_CNT + 12, 4);
    STAMP(2);
    s16x8 wq[8][5];
#pragma unroll
    for (int ks = 0; ks < 8; ++ks)
#pragma unroll
      for (int t = 0; t < 5; ++t) wq[ks][t] = ldstr((ks < 7 ? w0a + ks * 5120 : w8a - 5120) + t * 32, (ks < 7 ? w0b + ks * 5120 : w8b - 5120) + t * 32);
    if (X == 0 && !early) { WAITV(0); lds_arrive(L_CNT + 12); __builtin_amdgcn_s_setprio(1); }
    STAMP(10);
    u32x4 of[10];
    {
      u64 pt; u32 pn;
      asm volatile(POLL_TXT("%10", "%11", "%12", "%13", "%14")
                   "global_load_dwordx4 %0, %15, %13 sc1\n\tglobal_load_dwordx4 %1, %15, %13 offset:1024 sc1\n\tglobal_load_dwordx4 %2, %15, %13 offset:2048 sc1\n\t"
                   "global_load_dwordx4 %3, %15, %13 offset:3072 sc1\n\tglobal_load_dwordx4 %4, %16, %13 sc1\n\tglobal_load_dwordx4 %5, %16, %13 offset:1024 sc1\n\t"
                   "global_load_dwordx4 %6, %16, %13 offset:2048 sc1\n\tglobal_load_dwordx4 %7, %16, %13 offset:3072 sc1\n\t"
                   "global_load_dwordx2 v[240:241], %17, %13 sc1\n\tglobal_load_dwordx2 v[242:243], %17, %13 offset:128 sc1\n\t"
                   "global_load_dwordx2 v[244:245], %17, %13 offset:1024 sc1\n\tglobal_load_dwordx2 v[246:247], %17, %13 offset:1152 sc1\n\t"
                   : "=&v"(of[0]), "=&v"(of[1]), "=&v"(of[2]), "=&v"(of[3]), "=&v"(of[4]), "=&v"(of[5]), "=&v"(of[6]), "=&v"(of[7]),
                     "=&{v[240:243]}"(of[8]), "=&{v[244:247]}"(of[9]), "=&v"(pt), "=&s"(pn), "+v"(pfo), "+s"(ws), "+s"(tag64), "+v"(ob), "+v"(ob2), "+v"(cb)
                   :: "memory", "scc", "vcc");
    }
    if (X == 1) {
      lds_wait(L_CNT + 32 + 4 * wl, 1);
#pragma unroll
      for (int t = 0; t < 5; ++t) acc[t] += *lp<f32x4>(resv + t * 1024);
    }
    s16x8 w8[2][5];
#pragma unroll
    for (int ks = 0; ks < 10; ++ks) {
      if (ks == 0) asm volatile("s_waitcnt vmcnt(10)" : "+v"(of[0]), "+v"(of[1]) :: "memory");
      if (ks == 2) asm volatile("s_waitcnt vmcnt(7)" : "+v"(of[2]), "+v"(of[3]), "+v"(of[4]) :: "memory");
      if (ks == 5) asm volatile("s_waitcnt vmcnt(4)" : "+v"(of[5]), "+v"(of[6]), "+v"(of[7]) :: "memory");
      if (ks == 8) asm volatile("s_waitcnt vmcnt(0)" : "+v"(of[8]), "+v"(of[9]) :: "memory");
      SB0();
#pragma unroll
      for (int t = 0; t < 5; ++t) {
        acc[t] = mfma16(ks < 8 ? wq[ks < 8 ? ks : 0][t] : w8[ks & 1][t], __builtin_bit_cast(s16x8, of[ks]), acc[t]);
        if (ks == 2 || ks == 3) w8[ks & 1][t] = ldstr(w8a + (ks & 1) * 5120 + t * 32, w8b + (ks & 1) * 5120 + t * 32);
      }
      SB0();
    }
    STAMP(30);
    if (X == 0) {
      lds_wait(L_CNT + 20, 4);
#pragma unroll
      for (int t = 0; t < 5; ++t) *lp<f32x4>(resv + t * 1024) = acc[t];
      lds_arrive(L_CNT + 32 + 4 * wl);
    } else {
      u32 o[10];
#pragma unroll
      for (int t = 0; t < 5; ++t) { o[2 * t] = pk(acc[t][0], acc[t][1]); o[2 * t + 1] = pk(acc[t][2], acc[t][3]); }
#pragma unroll
      for (int t = 0; t < 4; t += 2) {
        auto r0 = __builtin_amdgcn_permlane16_swap(o[2 * t], o[2 * t + 2], false, false);
        auto r1 = __builtin_amdgcn_permlane16_swap(o[2 * t + 1], o[2 * t + 3], false, false);
        __builtin_amdgcn_raw_buffer_store_b128((u32x4){r0[0], r1[0], r0[1], r1[1]}, __builtin_amdgcn_make_buffer_rsrc(out, 0, 2048 * 1280, 0x00020000), ofo + 32 * t, 0, 16);
      }
      __builtin_amdgcn_raw_buffer_store_b64((u32x2){o[8], o[9]}, __builtin_amdgcn_make_buffer_rsrc(out, 0, 2048 * 1280, 0x00020000), ofo2, 0, 16);
    }
    STAMP(31);
  }
}

extern "C" void kernel_launch(void* const* d_in, const int* in_sizes, int n_in, void* d_out, int out_size, void* d_ws, size_t ws_size,
                              hipStream_t stream) {
  (void)in_sizes; (void)n_in; (void)out_size; (void)ws_size;
  (void)hipFuncSetAttribute((const void*)fused_block, hipFuncAttributeMaxDynamicSharedMemorySize, LDS_BYTES);
  fused_block<<<256, 512, LDS_BYTES, stream>>>((const u16*)d_in[0], (const u16*)d_in[1], (const u16*)d_in[4], (char*)d_ws, (const u16*)d_in[2],
                                               (const u16*)d_in[5], (const u16*)d_in[6], (const u16*)d_in[7], (u16*)d_out);
}
```

```cpp
#define STAMPS
#define AMPI 14
#define SMASK 0x4001u
#include <hip/hip_runtime.h>
#include <stdint.h>

typedef unsigned short u16;
typedef unsigned u32;
typedef unsigned long long u64;
typedef u32 u32x2 __attribute__((ext_vector_type(2)));
typedef u32 u32x4 __attribute__((ext_vector_type(4)));
typedef short s16x4 __attribute__((ext_vector_type(4)));
typedef short s16x8 __attribute__((ext_vector_type(8)));
typedef float f32x2 __attribute__((ext_vector_type(2)));
typedef float f32x4 __attribute__((ext_vector_type(4)));
typedef float f32x16 __attribute__((ext_vector_type(16)));
typedef __bf16 bf16x2 __attribute__((ext_vector_type(2)));
#define LAS __attribute__((address_space(3)))
#define GAS __attribute__((address_space(1)))
#define DI __device__ __forceinline__
#define RLX __ATOMIC_RELAXED, __HIP_MEMORY_SCOPE_AGENT
#define SB0() __builtin_amdgcn_sched_barrier(0)

#define ALWAYS_MAX 0
#define P1X 0
#define CODE_LINES 250

constexpr u32 WS_EPOCH = 0;
constexpr u32 WS_QFLAG = 4096;
constexpr u32 WS_OFLAG = 65536;
constexpr u32 WS_TOP2  = 327680;
constexpr u32 WS_STATS = 331776;
constexpr u32 WS_RR    = 348160;
constexpr u32 WS_Y     = 393216;
constexpr u32 WS_QKV   = 1u << 20;
constexpr u32 WS_O     = 10u << 20;
constexpr u32 WS_C4    = 12u << 20;
constexpr u32 WS_TS    = 16u << 20;
constexpr u32 WS_SPAN  = 13u << 20;

constexpr u32 SLOT = 24576;
constexpr u32 L_BP = 4 * SLOT;
constexpr u32 L_WOA = 0;
constexpr u32 L_KVA = 51200, L_KVB = 88576, PLANE = 6208;
constexpr u32 L_WOB7 = 125952;
constexpr u32 L_XR = 141312;
constexpr u32 L_MISC = 155648;
constexpr u32 L_CNT = L_MISC;
constexpr u32 L_ONE = L_MISC + 1664;
constexpr u32 L_PF  = L_MISC + 2560;
constexpr u32 L_YB  = L_MISC + 2816;
constexpr u32 L_CP  = L_MISC + 4096;
constexpr u32 LDS_BYTES = 163840;
static_assert((L_ONE & 255) == 128 && (L_KVA & 255) == 0 && (L_KVB & 255) == 0, "bank quarters");

constexpr u32 SPIN_CAP = 1u << 17;

template <int I> struct IC { static constexpr int v = I; };
template <int I, int N, class F> DI void static_for(F&& f) { if constexpr (I < N) { f(IC<I>{}); static_for<I + 1, N>(f); } }
template <class T> DI LAS T* lp(u32 a) { return (LAS T*)(uintptr_t)a; }
DI u32 pk(float a, float b) { f32x2 v = {a, b}; return __builtin_bit_cast(u32, __builtin_convertvector(v, bf16x2)); }
DI float blo(u32 v) { return __uint_as_float(v << 16); }
DI float bhi(u32 v) { return __uint_as_float(v & 0xffff0000u); }
DI float rbf(float a) { return blo(pk(a, a)); }
DI u32 uni(u32 v) { return __builtin_amdgcn_readfirstlane(v); }
template <int C> DI float dppf(float v) { return __uint_as_float(__builtin_amdgcn_mov_dpp(__float_as_uint(v), C, 0xf, 0xf, true)); }
DI float x16sum(float v) { auto r = __builtin_amdgcn_permlane16_swap(__float_as_uint(v), __float_as_uint(v), false, false); return __uint_as_float(r[0]) + __uint_as_float(r[1]); }
DI float x32sum(float v) { auto r = __builtin_amdgcn_permlane32_swap(__float_as_uint(v), __float_as_uint(v), false, false); return __uint_as_float(r[0]) + __uint_as_float(r[1]); }
DI float wsum(float v) {
  v += dppf<0xB1>(v); v += dppf<0x4E>(v); v += dppf<0x141>(v); v += dppf<0x140>(v);
  return x32sum(x16sum(v));
}
DI float sel(float a, float b, u64 m) { return __builtin_amdgcn_inverse_ballot_w64(m) ? b : a; }
DI float bsel(float a, float b, u32 m) { return __uint_as_float((__float_as_uint(b) & m) | (__float_as_uint(a) & ~m)); }
DI bf16x2 asb(u32 v) { return __builtin_bit_cast(bf16x2, v); }
DI f32x16 mfma32(s16x8 a, s16x8 b, f32x16 c) { return __builtin_amdgcn_mfma_f32_32x32x16_bf16(a, b, c, 0, 0, 0); }
DI f32x4 mfma16(s16x8 a, s16x8 b, f32x4 c) { return __builtin_amdgcn_mfma_f32_16x16x32_bf16(a, b, c, 0, 0, 0); }
DI s16x8 lds128(u32 a) { return *lp<s16x8>(a); }
DI s16x8 ldstr(u32 a, u32 b) {
  s16x4 lo = __builtin_amdgcn_ds_read_tr16_b64_v4i16(lp<s16x4>(a)), hi = __builtin_amdgcn_ds_read_tr16_b64_v4i16(lp<s16x4>(b));
  return __builtin_shufflevector(lo, hi, 0, 1, 2, 3, 4, 5, 6, 7);
}
DI void dma16(u32 lds, u32 voff, const void* sbase) {
  asm volatile("s_mov_b32 m0, %0\n\ts_nop 0\n\tglobal_load_lds_dwordx4 %1, %2" :: "s"(lds), "v"(voff), "s"(sbase) : "memory");
}
DI void dma16c(u32 lds, u32 voff, const void* sbase) {
  asm volatile("s_mov_b32 m0, %0\n\ts_nop 0\n\tglobal_load_lds_dwordx4 %1, %2 sc1" :: "s"(lds), "v"(voff), "s"(sbase) : "memory");
}
#define WAITV(n) asm volatile("s_waitcnt vmcnt(" #n ")" ::: "memory")
#define WAITBAR(n) asm volatile("s_waitcnt vmcnt(%0) lgkmcnt(0)\n\ts_barrier" :: "i"(n) : "memory")
#define WAITBAR2(n0, n1) asm volatile("s_waitcnt vmcnt(" #n1 ") lgkmcnt(0)\n\ts_cmp_lg_u32 %0, 0\n\ts_cbranch_scc1 1f\n\ts_waitcnt vmcnt(" #n0 ")\n1:\n\ts_barrier" :: "s"(X) : "memory", "scc")
#define WAITBAR1(n1) asm volatile("s_cmp_lg_u32 %0, 0\n\ts_cbranch_scc0 1f\n\ts_waitcnt vmcnt(" #n1 ")\n1:\n\ts_waitcnt lgkmcnt(0)\n\ts_barrier" :: "s"(X) : "memory", "scc")
#define WAITBARV(n) asm volatile("s_waitcnt vmcnt(%0)\n\ts_barrier" :: "i"(n) : "memory")
#define BAR() asm volatile("s_waitcnt lgkmcnt(0)\n\ts_barrier" ::: "memory")
DI u64 gld64(const char* ws, u32 off) { return __hip_atomic_load((GAS u64*)(ws + off), RLX); }
DI u64 gld64w(const char* ws, u32 off) {
  u64 r; asm volatile("global_load_dwordx2 %0, %1, %2 sc1\n\ts_waitcnt vmcnt(0)" : "=&v"(r) : "v"(off), "s"(ws) : "memory"); return r;
}
#define NOUNROLL _Pragma("unroll 1")
DI void gst64(char* ws, u32 off, u64 v) { __hip_atomic_store((GAS u64*)(ws + off), v, RLX); }
DI void lds_arrive(u32 a) {
  u32 va = a, one = 1;
  asm volatile("s_waitcnt lgkmcnt(0)\n\ts_mov_b64 exec, 1\n\tds_add_u32 %0, %1\n\ts_mov_b64 exec, -1" :: "v"(va), "v"(one) : "memory");
}
DI void lds_wait(u32 a, u32 n) {
  u32 va = a, t, c;
  asm volatile("1:\n\tds_read_b32 %0, %2\n\ts_waitcnt lgkmcnt(0)\n\tv_readfirstlane_b32 %1, %0\n\ts_cmp_ge_u32 %1, %3\n\ts_cbranch_scc1 2f\n\ts_sleep 1\n\ts_branch 1b\n2:"
               : "=&v"(t), "=&s"(c) : "v"(va), "s"(n) : "memory", "scc");
}
#define POLL_TXT(T, N, OFF, WS, TAG) "s_mov_b32 " N ", 0x20000\n1:\n\tglobal_load_dwordx2 " T ", " OFF ", " WS " sc1\n\ts_waitcnt vmcnt(0)\n\t" \
    "v_cmp_ne_u64 vcc, " TAG ", " T "\n\ts_cbranch_vccz 2f\n\ts_sleep 1\n\ts_sub_u32 " N ", " N ", 1\n\ts_cbranch_scc0 1b\n2:\n\t"
DI void poll_flags(const char* ws, u32 off, u64 tag) {
  u64 t; u32 n;
  asm volatile(POLL_TXT("%0", "%1", "%2", "%3", "%4") : "=&v"(t), "=&s"(n) : "v"(off), "s"(ws), "s"(tag) : "memory", "scc", "vcc");
}
DI u32 poll_gran(const char* ws, u32 off, u32 tag) {
  u64 g;
  NOUNROLL for (u32 s = 0; s < SPIN_CAP; ++s) { g = gld64w(ws, off); if (__all((u32)(g >> 32) == tag)) break; __builtin_amdgcn_s_sleep(1); }
  return (u32)g;
}
DI void store_flag(char* ws, u32 off, u64 val, u64 mask) {
  asm volatile("s_mov_b64 exec, %3\n\tglobal_store_dwordx2 %0, %1, %2 sc1\n\ts_mov_b64 exec, -1" :: "v"(off), "v"(val), "s"(ws), "s"(mask) : "memory");
}
#define PINV(x) asm volatile("" : "+v"(x))
#define PINS(x) asm volatile("" : "+s"(x))

#define UOFF 0
#define STAMP(i) do { if ((SMASK >> (i)) & 1) { SB0(); st[i] = __builtin_amdgcn_s_memrealtime(); SB0(); } } while (0)

DI void wo_offsets(u32 (&wv)[8], int wl, int lane) {
#pragma unroll
  for (int k = 0; k < 8; ++k) {
    const int n = k < 5 ? k : k < 7 ? 3 * (wl & 1) + k - 5 : (wl & 1 ? 4 : 2);
    const u32 P = 64 * n + lane, r = P / 10u, pc = P - 10 * r, g = r >> 3, j = (r & 7) ^ ((g & 1) << 2);
    wv[k] = ((k >= 5 ? 40 * g + 16 : 8 * (g + (g >> 1))) + j) * 1280 + pc * 16;
    PINV(wv[k]);
  }
}
template <int PART>
DI void stage_wo(const char* woc, int H, int wl, const u32 (&wv)[8]) {
  auto at = [&](int ks) { return H == 0 ? L_WOA + ks * 5120 : ks < 7 ? L_KVA + ks * 5120 : L_WOB7 + (ks - 7) * 5120; };
  const int n2 = 3 * (wl & 1), k2 = wl >> 1;
  const char *s0 = woc + (320 * H + 40 * wl) * 1280, *s2 = woc + (320 * H + 160 * k2) * 1280;
#define WO_D(M, V, S) "s_add_u32 m0, " M "\n\ts_nop 0\n\tglobal_load_lds_dwordx4 " V ", " S "\n\t"
#define WO_5 "s_nop 4\n\t" WO_D("%7, 0", "%0", "%5") WO_D("m0, 1024", "%1", "%5") WO_D("m0, 1024", "%2", "%5") WO_D("m0, 1024", "%3", "%5") WO_D("m0, 1024", "%4", "%5")
  if (PART != 1)
    asm volatile(WO_5 WO_D("%8, 0", "%9", "%6")
                 :: "v"(wv[0]), "v"(wv[1]), "v"(wv[2]), "v"(wv[3]), "v"(wv[4]), "s"(s0), "s"(s2), "s"(at(wl)), "s"(at(8 + k2) + (wl & 1 ? 4 : 2) * 1024), "v"(wv[7]) : "memory", "scc");
  if (PART != 0)
    asm volatile(WO_5 WO_D("%8, 0", "%9", "%6") WO_D("m0, 1024", "%10", "%6")
                 :: "v"(wv[0]), "v"(wv[1]), "v"(wv[2]), "v"(wv[3]), "v"(wv[4]), "s"(s0 + 160 * 1280), "s"(s2), "s"(at(wl + 4)), "s"(at(8 + k2) + n2 * 1024), "v"(wv[5]), "v"(wv[6]) : "memory", "scc");
}

constexpr int slice_of(int v) { return v < 6 ? v : (v >= 9 && v < 13) ? v - 3 : -1; }
constexpr int page_of(int v) { return v < 9 ? v - 6 : v - 10; }
constexpr int OP_GR = 41, OP_PF = 43;
constexpr int sched_op(int u, int i) {
  if (u == 0) return i < 3 ? 6 + i : -1;
  if (u <= 3) return i < 3 ? 3 * (u + 2) + i : 30 + u - 1;
  if (u <= 6) return i < 3 ? 3 * (u + 2) + i : -1;
  if (u == 7) return i < 3 ? 27 + i : OP_GR;
  if (u == 8) return i < 3 ? 33 + i : -1;
  if (u == 9) return i == 0 ? 39 : -1;
  if (u == 10) return i < 3 ? 36 + i : -1;
  if (u == 11) return i == 0 ? OP_PF : -1;
  return -1;
}
constexpr int younger(int op, int u) {
  int n = op < 6 ? 5 - op + 1 : 0; bool seen = op < 6;
  for (int v = 0; v < u; ++v) for (int i = 0; i < 4; ++i) { const int o = sched_op(v, i); if (o < 0) continue; if (seen) ++n; if (o == op) seen = true; }
  return n;
}
constexpr int need_op(int u) {
  return slice_of(u) >= 0 ? 3 * slice_of(u) + 2 : u == 6 ? 32 : u == 13 ? 38 : -1;
}
template <bool FULL>
DI f32x16 attn_item(s16x8 q0, s16x8 q1, u32 ka, u32 kb, u32 ko, u32 va, u32 vo, u32 vstep, const u32 (&dm)[8], const u32 (&mk)[16], int nfull, float negm, float cold) {
  f32x16 ci, co, O;
#pragma unroll
  for (int i = 0; i < 16; ++i) { ci[i] = FULL ? 0.f : negm; co[i] = FULL ? 0.f : cold; O[i] = 0.f; }
  f32x16 S[2];
  s16x8 kn0, kn1, pend, vpend;
  {
    const s16x8 kd0 = lds128(ka + 4096), ko0 = lds128(ka + ko), kd1 = lds128(kb + 4096), ko1 = lds128(kb + ko), k10 = lds128(ka + 3584), k11 = lds128(kb + 3584);
    const s16x8 v0 = ldstr(va, va + 128), vo0 = ldstr(vo, vo + 128), vo1 = ldstr(vo + 256, vo + 384), v1 = ldstr(va + 256, va + 384);
    kn0 = lds128(ka + 3072); kn1 = lds128(kb + 3072);
    SB0();
    f32x16 sd = mfma32(kd0, q0, ci), so = mfma32(ko0, q0, co);
    sd = mfma32(kd1, q1, sd); so = mfma32(ko1, q1, so);
    S[1] = mfma32(k10, q0, ci);
    SB0();
    u32 p[8], pd[8];
#pragma unroll
    for (int i = 0; i < 8; ++i) {
      p[i] = pk(__builtin_amdgcn_exp2f(bsel(so[2 * i], sd[2 * i], mk[2 * i])), __builtin_amdgcn_exp2f(bsel(so[2 * i + 1], sd[2 * i + 1], mk[2 * i + 1])));
      pd[i] = p[i] & dm[i]; p[i] ^= pd[i];
      if (i == 1) { S[1] = mfma32(k11, q1, S[1]); PINV(S[1]); SB0(); }
      if (i == 3) { O = mfma32(v0, __builtin_bit_cast(s16x8, (u32x4){pd[0], pd[1], pd[2], pd[3]}), O); PINV(O); SB0(); }
      if (i == 5) { O = mfma32(vo0, __builtin_bit_cast(s16x8, (u32x4){p[0], p[1], p[2], p[3]}), O); PINV(O); SB0(); }
    }
    O = mfma32(vo1, __builtin_bit_cast(s16x8, (u32x4){p[4], p[5], p[6], p[7]}), O);
    pend = __builtin_bit_cast(s16x8, (u32x4){pd[4], pd[5], pd[6], pd[7]}); vpend = v1;
  }
#pragma unroll
  for (int e = 1; e <= 7; ++e) {
    if (!FULL && e > nfull) break;
    const f32x16& sc = S[e & 1];
    va -= vstep;
    SB0();
    f32x16 sn = ci;
    if (e < 7) sn = mfma32(kn0, q0, ci);
    const s16x8 v0 = ldstr(va, va + 128), v1 = ldstr(va + 256, va + 384);
    u32 p[8];
    float ex[16];
#pragma unroll
    for (int i = 0; i < 4; ++i) ex[i] = __builtin_amdgcn_exp2f(sc[i]);
    SB0();
    if (e < 7) sn = mfma32(kn1, q1, sn);
    if (e < 6) { kn0 = lds128(ka + 3072 - e * 512); kn1 = lds128(kb + 3072 - e * 512); }
#pragma unroll
    for (int i = 4; i < 8; ++i) ex[i] = __builtin_amdgcn_exp2f(sc[i]);
    p[0] = pk(ex[0], ex[1]); p[1] = pk(ex[2], ex[3]);
    SB0();
    O = mfma32(vpend, pend, O); PINV(O);
    SB0();
#pragma unroll
    for (int i = 8; i < 12; ++i) ex[i] = __builtin_amdgcn_exp2f(sc[i]);
    p[2] = pk(ex[4], ex[5]); p[3] = pk(ex[6], ex[7]);
    SB0();
    O = mfma32(v0, __builtin_bit_cast(s16x8, (u32x4){p[0], p[1], p[2], p[3]}), O); PINV(O);
    SB0();
#pragma unroll
    for (int i = 12; i < 16; ++i) ex[i] = __builtin_amdgcn_exp2f(sc[i]);
    p[4] = pk(ex[8], ex[9]); p[5] = pk(ex[10], ex[11]); p[6] = pk(ex[12], ex[13]); p[7] = pk(ex[14], ex[15]);
    pend = __builtin_bit_cast(s16x8, (u32x4){p[4], p[5], p[6], p[7]}); vpend = v1;
    S[(e + 1) & 1] = sn;
    SB0();
  }
  return mfma32(vpend, pend, O);
}
DI float attn_negmax(s16x8 q0, s16x8 q1, u32 ka, u32 kb, int nfull, bool has_old, int c, int hh) {
  f32x16 z;
#pragma unroll
  for (int i = 0; i < 16; ++i) z[i] = 0.f;
  float m = -__builtin_inff();
  NOUNROLL for (int e = 0; e <= nfull + (has_old ? 1 : 0); ++e) {
    const f32x16 s = mfma32(lds128(kb + 4096 - e * 512), q1, mfma32(lds128(ka + 4096 - e * 512), q0, z));
    int lo = (e == 8 ? c + 1 : 0) - 4 * hh, n = (e == 0 ? c : 31) - 4 * hh - lo;
    PINV(lo); PINV(n);
#pragma unroll
    for (int i = 0; i < 16; ++i) { const int k = (i & 3) + 8 * (i >> 2) - lo; m = fmaxf(m, (k >= 0 && k <= n) ? s[i] : m); }
  }
  auto r = __builtin_amdgcn_permlane32_swap(__float_as_uint(m), __float_as_uint(m), false, false);
  return -fmaxf(__uint_as_float(r[0]), __uint_as_float(r[1]));
}

DI void row_stats(u32x4 xa, u32 xb, u32x4 wa, u32 wb, char* ws, int srow, u32 tag, int tid) {
  const int lane = tid & 63;
  const u32 xu[5] = {xa[0], xa[1], xa[2], xa[3], xb}, wu5[5] = {wa[0], wa[1], wa[2], wa[3], wb};
  f32x2 v[5], s2 = {0.f, 0.f};
#pragma unroll
  for (int i = 0; i < 5; ++i) { v[i] = (f32x2){blo(xu[i]), bhi(xu[i])}; s2 += v[i]; }
  const float mean = wsum(s2[0] + s2[1]) * (1.f / 640.f), mb = rbf(mean);
  f32x2 q2 = {0.f, 0.f};
  float r = 0.f;
#pragma unroll
  for (int i = 0; i < 5; ++i) {
    const f32x2 d = v[i] - mean, e = v[i] - mb;
    q2 += d * d;
    r = __builtin_amdgcn_fdot2_f32_bf16(asb(pk(e[0], e[1])), asb(wu5[i]), r, false);
  }
  float q = q2[0] + q2[1];
  q += dppf<0xB1>(q); r += dppf<0xB1>(r); q += dppf<0x4E>(q); r += dppf<0x4E>(r);
  q += dppf<0x141>(q); r += dppf<0x141>(r); q += dppf<0x140>(q); r += dppf<0x140>(r);
  { auto t = __builtin_amdgcn_permlane16_swap(__float_as_uint(q), __float_as_uint(r), false, false); q = __uint_as_float(t[0]) + __uint_as_float(t[1]); }
  q = x32sum(q);
  r = __uint_as_float(__builtin_amdgcn_readlane(__float_as_uint(q), 16));
  q = __uint_as_float(__builtin_amdgcn_readlane(__float_as_uint(q), 0));
  const float sd = rbf(__builtin_amdgcn_sqrtf(rbf(rbf(q * (1.f / 640.f)) + 1e-5f)));
  const u32 pay = lane == 0 ? ((__float_as_uint(mb) >> 16) | __float_as_uint(sd)) : __float_as_uint(r * __builtin_amdgcn_rcpf(sd));
  store_flag(ws, (lane == 0 ? WS_STATS : WS_RR) + srow * 8, ((u64)tag << 32) | pay, 3);
  if (tid < 16) *lp<u32>(L_CNT + tid * 4) = 0;
  if (tid < 144) *lp<u32>(L_ONE + tid * 4) = 0x3F803F80u;
}

__global__ void __launch_bounds__(512, 2)
fused_block(const u16* __restrict__ x, const u16* __restrict__ wqkv, const u16* __restrict__ wr, char* ws,
            const u16* wo_, const u16* wg_, const u16* wu_, const u16* wd_, u16* out_) {
  (void)wo_; (void)wg_; (void)wu_; (void)wd_; (void)out_;
  auto karg = [](int i) { const __attribute__((address_space(4))) u64* k = (const __attribute__((address_space(4))) u64*)__builtin_amdgcn_kernarg_segment_ptr(); PINS(k); return (char*)k[i]; };
  extern __shared__ __attribute__((aligned(16))) char smem[];
  (void)smem;
  u64 st[32];
  for (int i = 0; i < 32; ++i) st[i] = 0;
  STAMP(0);
  const u64 cyc0 = __builtin_amdgcn_s_memtime();
  const int b = blockIdx.x, xc = b & 7, bi = b >> 3;
  const int tid = threadIdx.x, lane = tid & 63, w = uni(tid >> 6);
  const int c = lane & 31, hh = lane >> 5;
  const bool helper = bi >= 30;

  if (w == 7) {
    u64 pc; asm volatile("s_getpc_b64 %0" : "=s"(pc));
    constexpr int PER = (CODE_LINES + 31) / 32;
    u32 ln = bi * PER + (lane % PER); ln = ln < CODE_LINES ? ln : CODE_LINES - 1;
    asm volatile("s_mov_b32 m0, %0\n\ts_nop 0\n\tglobal_load_lds_dword %1, %2" :: "s"(L_PF), "v"(ln * 128), "s"(pc & ~127ull) : "memory");
  }
  const int srow = b * 8 + w;
#define ROW_LOADS() u32x4 xa, wa; u32 xb, wb; \
    asm volatile("s_nop 4\n\tglobal_load_dwordx4 %0, %5, %6\n\tglobal_load_dword %1, %5, %6 offset:16\n\t" \
                 "global_load_dwordx4 %2, %5, %7\n\tglobal_load_dword %3, %5, %7 offset:16\n\ts_load_dwordx2 %4, %8, 0x0" \
                 : "=&v"(xa), "=&v"(xb), "=&v"(wa), "=&v"(wb), "=&s"(tag64) : "v"(lane * 20), "s"(x + srow * 640), "s"(wr), "s"(ws) : "memory")
#define ROW_STATS(n) \
    asm volatile("s_waitcnt vmcnt(" #n ") lgkmcnt(0)" : "+v"(xa), "+v"(xb), "+v"(wa), "+v"(wb), "+s"(tag64) :: "memory"); \
    tag64 = ((u64)uni((u32)((tag64 + 1) >> 32)) << 32) | uni((u32)tag64 + 1); tag = (u32)tag64; \
    row_stats(xa, xb, wa, wb, ws, srow, tag, tid)
  u64 tag64; u32 tag;
  const __amdgpu_buffer_rsrc_t wsr = __builtin_amdgcn_make_buffer_rsrc(ws, 0, WS_SPAN, 0x00020000);
  const int X = w >> 2, wl = w & 3;

  u64 sgr = 0;
#define RES_PREP(bb) u32 xo_[4]; const char* xs_; \
    { const int row_ = 64 * (((bb) & 7) + 8 * ((bb) >> 6)) + 16 * wl; \
      _Pragma("unroll") for (int n_ = 0; n_ < 3; ++n_) { const u32 P_ = 64 * n_ + lane, r_ = P_ / 10u; xo_[n_] = (r_ < 16 ? r_ : 15) * 1280 + (P_ - 10 * r_) * 16; PINV(xo_[n_]); } \
      xo_[3] = X ? WS_STATS + (row_ + (lane & 15)) * 8 : WS_TOP2 + ((bb) & 15) * 128; PINV(xo_[3]); \
      xs_ = karg(0) + (u32)(row_ * 1280 + (((bb) >> 3) & 7) * 160); PINS(xs_); }
#define RES_ISSUE() asm volatile("s_cmp_eq_u32 %8, 0\n\tglobal_load_dwordx2 %0, %1, %2 sc1\n\ts_cbranch_scc1 1f\n\t" \
                 WO_D("%7, 0", "%3", "%6") WO_D("m0, 1024", "%4", "%6") WO_D("m0, 1024", "%5", "%6") "\n1:" \
                 : "=&v"(sgr) : "v"(xo_[3]), "s"(ws), "v"(xo_[0]), "v"(xo_[1]), "v"(xo_[2]), "s"(xs_), "s"(L_XR + wl * 3072), "s"(X) : "memory", "scc")

  if (!helper) {
    ROW_LOADS();
    const int Lx = (xc >> 1) * 30 + bi, uj = Lx >> 3, rtile = (xc & 1) * 8 + (Lx & 7);
    const int uA = (uj / 5) * 10 + uj % 5;
    const int rot = (rtile * 3 + uj) % 10;
    const u32 vx = (8 * w + (lane >> 3)) * 1280 + (((lane & 7) ^ ((4 * w + (lane >> 4)) & 7)) << 4);
    const u32 vw = (8 * w + (lane >> 3)) * 3840 + (((lane & 7) ^ ((lane >> 2) & 4)) << 4);
    const char* xbase = (const char*)x + rtile * (128 * 1280);
    const char* wbase = (const char*)wqkv + uA * 128;
    auto piece = [&](int op) {
      const int a = op / 3, p = op % 3, pg = op - 30;
      int sl = (op < 30 ? a : pg) + rot; sl = sl >= 10 ? sl - 10 : sl;
      if (op >= 30) dma16(L_BP + (pg == 9 ? 6 : pg % 6) * 8192 + w * 1024, vw, wbase + 640 + sl * (64 * 3840));
      else if (p < 2) dma16((a % 4) * SLOT + p * 8192 + w * 1024, vx, xbase + sl * 128 + p * (64 * 1280));
      else dma16((a % 4) * SLOT + 16384 + w * 1024, vw, wbase + sl * (64 * 3840));
    };
    piece(0); piece(1); piece(2); piece(3); piece(4); piece(5);
    const int tt = wl, fh = X;
    const u32 key = (c >> 1) & 7;
    u32 xad[2][4];
#pragma unroll
    for (int i = 0; i < 4; ++i) { xad[0][i] = (32 * tt + c) * 128 + (((2 * i + hh) ^ key) << 4); xad[1][i] = xad[0][i] + 2 * SLOT; PINV(xad[0][i]); PINV(xad[1][i]); }
    const u32 q4 = (lane >> 2) & 3, p4 = lane & 3, g2 = (lane >> 4) & 1;
    u32 wadA = (8 * hh + q4) * 128 + (((4 * fh + 2 * (p4 & 1) + g2) ^ ((q4 >> 1) << 2)) << 4) + (p4 >> 1) * 8, wadB = wadA + L_BP, wadC = wadA + 2 * SLOT;
    const char* pfa;
    {
      const u32 l0 = (bi * 8 + w) * 38 + (lane < 37 ? lane : 37), l = l0 < 8960 ? l0 : 8959, m = l / 1920u, g = l - 1920 * m, r = g / 3u;
      const char *wg = karg(5), *wu = karg(6), *wd = karg(7), *wo = karg(4);
      pfa = m >= 3 ? wo + (l - 5760) * 128 : m == 2 ? wd + xc * (192 * 1280) + g * 128 : (m ? wu : wg) + r * 3072 + xc * 384 + (g - 3 * r) * 128;
    }
    u32 goff = WS_STATS + (rtile * 128 + 32 * tt + c) * 8;
    u32 wadT = (32 * X + 8 * (lane >> 4) + q4) * 128 + (((2 * wl + (p4 >> 1)) ^ ((q4 >> 1) << 2)) << 4) + (p4 & 1) * 8;
    u32 wadV = wadT + L_BP; wadT += 16384;
    u32 wadU = wadT + 2 * SLOT;
    u32 qoff = WS_QKV + (8 * uA + 4 * fh + 2 * hh) * 32768 + (rtile * 128 + 32 * tt + c) * 16;
    u32 csl = L_CP + (32 * fh + c) * 4, cswr = L_CP + X * 256 + (16 * wl + (lane & 15)) * 4;
    u32 csm = L_CP + 1024 + w * 128 + c * 4, csr = L_CP + 1024 + w * 128 + hh * 64;
    u32x4 onev = {0x3F803F80u, 0x3F803F80u, 0x3F803F80u, 0x3F803F80u};
    PINV(wadA); PINV(wadB); PINV(wadC); PINV(wadT); PINV(wadU); PINV(wadV); PINV(pfa); PINV(goff); PINV(qoff); PINV(csl); PINV(cswr); PINV(csm); PINV(csr); PINV(onev);
    u32 wv[8];
    wo_offsets(wv, wl, lane);
    ROW_STATS(6);
    if ((P1X & 8) && X == 1) __builtin_amdgcn_s_setprio(1);
    SB0();
    s16x8 xf[40], wf[2][4], wt[8], cf, cg;
    const s16x8 ones = __builtin_bit_cast(s16x8, onev);
    f32x16 acc, acb;
    f32x4 acs = {0.f, 0.f, 0.f, 0.f}, act = acs;
#pragma unroll
    for (int i = 0; i < 16; ++i) { acc[i] = 0.f; acb[i] = 0.f; }
    u64 gr = 0;
    float mneg = 0.f, rsq = 0.f;
    u32 qscb = uA < 10 ? 0x3ea52b5cu : 0x3f800000u;
    PINS(qscb);
    u32 eo[8];
    f32x4 cp[4];
    float c0 = 0.f, c1 = 0.f;
    auto epi2 = [&](float a0, float a1, float k0, float k1) {
      u32 r; float t;
      asm("v_fma_f32 %0, %2, %4, %6\n\tv_fma_f32 %1, %2, %5, %7\n\tv_mul_f32 %0, %3, %0\n\tv_mul_f32 %1, %3, %1\n\tv_cvt_pk_bf16_f32 %0, %0, %1"
          : "=&v"(r), "=&v"(t) : "v"(mneg), "v"(rsq), "v"(k0), "v"(k1), "v"(a0), "v"(a1));
      return r;
    };
    auto epi_store = [&](int second, int k) {
      __builtin_amdgcn_raw_buffer_store_b128((u32x4){eo[4 * k], eo[4 * k + 1], eo[4 * k + 2], eo[4 * k + 3]}, wsr, qoff + k * 32768 + second * (40 * 32768), 0, 16);
    };
    static_for<0, 21>([&](auto uc) __attribute__((always_inline)) {
      constexpr int u = decltype(uc)::v;
      constexpr int sa = slice_of(u), sb = page_of(u), pa = slice_of(u - 1), pb = page_of(u - 1);
      constexpr int G = u < 7 ? u : u < 9 ? -1 : u < 14 ? u - 2 : u == 16 ? 12 : u == 19 ? 13 : -1;
      if constexpr (u < 20 && need_op(u) >= 0) WAITBARV(younger(need_op(u), u));
      if (u == 7) asm volatile("s_barrier" ::: "memory");
      if (u == 15) asm volatile("s_waitcnt lgkmcnt(6)\n\ts_barrier" ::: "memory");
      if (u == 19) asm volatile("s_waitcnt lgkmcnt(4)\n\ts_barrier" ::: "memory");
      if (G == 11) {
        PINV(gr);
        u32 pay = (u32)gr;
        if (!P1X && !__all((u32)(gr >> 32) == tag)) pay = poll_gran(ws, goff, tag);
        mneg = -blo(pay); rsq = __builtin_amdgcn_rcpf(bhi(pay)) * __uint_as_float(qscb); PINV(mneg); PINV(rsq);
      }
      if (u >= UOFF && u < UOFF + 16) STAMP(16 + (u >= UOFF && u < UOFF + 16 ? u - UOFF : 0));
      SB0();
#pragma unroll
      for (int i = 0; i < 4; ++i) {
        if constexpr (u > 0) {
          if constexpr (pa >= 0) acc = mfma32(wf[(u - 1) & 1][i], xf[4 * pa + i], acc);
          else acb = mfma32(u > 13 ? wt[(4 * (u - 14) + i) & 7] : wf[(u - 1) & 1][i], xf[4 * pb + i], acb);
        }
        SB0();
        if constexpr (u < 20) {
          if constexpr (sa >= 0) {
            const u32 wbs = (sa & 2 ? wadC : wadA) + (sa & 1) * SLOT + 16384 + i * 2048;
            xf[4 * sa + i] = lds128(xad[(sa >> 1) & 1][i] + (sa & 1) * SLOT); wf[u & 1][i] = ldstr(wbs, wbs + 512);
          } else if constexpr (u < 13) { const u32 pbase = wadB + sb * 8192 + i * 2048; wf[u & 1][i] = ldstr(pbase, pbase + 512); }
          else {
            auto rd = [&](int f) { const int pn = 3 + f / 4; const u32 pbase = wadB + (pn == 9 ? 6 : pn % 6) * 8192 + (f & 3) * 2048; wt[f & 7] = ldstr(pbase, pbase + 512); };
            if constexpr (u == 13) { rd(i == 0 ? 0 : i == 1 ? 2 : i == 2 ? 3 : 5); if (!(i & 1)) rd(i == 0 ? 1 : 4); }
            else if (4 * (u - 14) + i + 6 < 28) rd(4 * (u - 14) + i + 6);
          }
        }
        if (i == 3) {
          if constexpr (u > 0 && pa >= 0) acs = mfma16(ones, cf, acs);
          if constexpr ((u >= 7 && u <= 9) || (u >= 14 && u <= 17)) act = mfma16(ones, cf, act);
          if constexpr (sa >= 0) { const u32 cb = (sa & 2 ? wadU : wadT) + (sa & 1) * SLOT; cf = ldstr(cb, cb + 512); }
          if constexpr (u >= 6 && u <= 8) cf = ldstr(wadV + sb * 8192, wadV + sb * 8192 + 512);
          if constexpr (u >= 13 && u <= 16) { constexpr int pn = 3 + 2 * (u - 13); const u32 cb = wadV + (pn == 9 ? 6 : pn % 6) * 8192; cf = ldstr(cb, cb + 512); }
        }
        if (i == 1) {
          if constexpr (u >= 14 && u <= 16) act = mfma16(ones, cg, act);
          if constexpr (u >= 13 && u <= 15) { constexpr int pn = 4 + 2 * (u - 13); const u32 cb = wadV + (pn % 6) * 8192; cg = ldstr(cb, cb + 512); }
        }
        const int op = sched_op(u, i);
        if (op >= 0 && op < 40) piece(op);
        if (op == OP_GR) asm volatile("global_load_dwordx2 %0, %1, %2 sc1" : "=v"(gr) : "v"(goff), "s"(ws) : "memory");
        if (op == OP_PF) asm volatile("s_mov_b32 m0, %0\n\ts_nop 0\n\tglobal_load_lds_dword %1, off" :: "s"(L_PF), "v"(pfa) : "memory");
        if constexpr (u == 14 || u == 18) { if (i == 1) *lp<float>(cswr + (u == 14 ? 0 : 512)) = u == 14 ? acs[0] : act[0]; }
        if constexpr (u == 15 || u == 19) {
          constexpr u32 o = u == 15 ? 0 : 512;
          if (i == 0) { c0 = *lp<float>(csl + o); c1 = *lp<float>(csl + o + 256); }
          if (i == 2) *lp<float>(csm) = c0 + c1;
          if (i == 3) {
#pragma unroll
            for (int k = 0; k < 4; ++k) cp[k] = *lp<f32x4>(csr + 16 * k);
          }
        }
        if constexpr (u == 16 || u == 17) {
          constexpr int q = 2 * (u - 16);
          eo[2 * q + i] = epi2(acc[4 * q + 2 * i], acc[4 * q + 2 * i + 1], cp[q + (i >> 1)][2 * (i & 1)], cp[q + (i >> 1)][2 * (i & 1) + 1]);
          if (i == 3) epi_store(0, u - 16);
        }
        SB0();
      }
    });
    STAMP(4);
    u32 fl = WS_QFLAG + (rtile * 30 + uA) * 128; u64 tagv = tag64;
    PINV(fl); PINV(tagv);
#pragma unroll
    for (int i = 0; i < 4; ++i) {
      eo[2 * i]     = pk(fmaf(mneg, cp[i][0], acb[4 * i]) * rsq, fmaf(mneg, cp[i][1], acb[4 * i + 1]) * rsq);
      eo[2 * i + 1] = pk(fmaf(mneg, cp[i][2], acb[4 * i + 2]) * rsq, fmaf(mneg, cp[i][3], acb[4 * i + 3]) * rsq);
      if (i & 1) epi_store(1, i >> 1);
    }
    SB0();
    STAMP(3);
    WAITBAR(2);
    if (w == 0) store_flag(ws, fl, tagv, 1);
    const char* woc = karg(4) + (bi & 7) * 160; PINS(woc);
    if (X == 1) stage_wo<0>(woc, 0, wl, wv);
    else {
      const u32 l = (bi * 4 + wl) * 27 + (lane < 26 ? lane : 26);
      asm volatile("s_mov_b32 m0, %0\n\ts_nop 4\n\tglobal_load_lds_dword %1, %2" :: "s"(L_PF), "v"((3200 + (l < 3200 ? l : 3199)) * 128), "s"(woc - (bi & 7) * 160) : "memory");
    }
    {
    int b2 = blockIdx.x; PINS(b2);
    const int xc = b2 & 7, bi = b2 >> 3;
    const int odd = bi >= 15, hl = 2 * xc + odd, grp = bi - 15 * odd, head = 16 * X + hl;
    const int oc0 = 5 * (8 * X + xc);
    const int oA = oc0 + 3 * odd, oG = oc0 + 2;
    const u32 kv = X == 0 ? L_KVA : L_KVB;
    const int g0 = grp == 0;
    u32 pollo;
    {
      const int l4 = lane & 15, isq = (l4 + 4) >> 4, b1 = (l4 >> 1) & 1, ri = l4 >> 2;
      const int oct = oc0 + 2 * odd + 2 * (l4 & 1) + (80 + 80 * b1) * (1 - isq);
      const int rt = g0 ? (isq ? b1 : (ri + 1) >> 1) : grp - 1 + ri - isq;
      pollo = WS_QFLAG + (rt * 30 + (oct >> 3)) * 128;
    }
    const int t0 = g0 ? wl : 4 * (grp + 1) + wl, t1 = g0 ? 7 - wl : t0;
    u32 qo0 = WS_QKV + (oA + hh) * 32768 + c * 16 + t0 * 512, qo1 = WS_QKV + oG * 32768 + c * 16 + t0 * 512, l16 = lane * 16;
    u32 qd = (t1 - t0) * 512;
    const int tp0 = (3 - g0) * (wl >> 1);
    const char* kvs = ws + (u32)(WS_QKV + 80 * (1 + (wl & 1)) * 32768 + ((g0 ? 0 : 128 * (grp - 1)) + 64 * tp0) * 16);
    const char* sA = kvs + (u32)(oA * 32768); const char* sB = sA + 32768; const char* sG = kvs + (u32)(oG * 32768);
    u32 mA = kv + 3 * (wl & 1) * PLANE + tp0 * 1024;
    RES_PREP(b2);
    fl += 5 * 128;
    PINV(pollo); PINV(qo0); PINV(qo1); PINV(l16); PINV(fl); PINS(sA); PINS(sB); PINS(sG); PINS(mA); PINS(qd);
    asm volatile("s_waitcnt vmcnt(6) lgkmcnt(0)\n\ts_cmp_ge_u32 %0, 4\n\ts_cbranch_scc1 1f\n\ts_waitcnt vmcnt(2)\n\ts_cmp_eq_u32 %0, 0\n\ts_cbranch_scc1 1f\n\ts_waitcnt vmcnt(1)\n1:\n\ts_barrier"
                 :: "s"(w) : "memory", "scc");
    if (w == 4) store_flag(ws, fl, tagv, 1);
    STAMP(5);
    RES_ISSUE();
    if (X == 1) stage_wo<1>(woc, 0, wl, wv);
    __builtin_amdgcn_s_setprio(1);
    u32x4 qa, qb, qa2, qb2;
    {
      u64 pt; u32 pn;
      asm volatile(POLL_TXT("%4", "%5", "%6", "%7", "%8")
                   "s_memrealtime %17\n\t"
                   "s_mov_b32 m0, %9\n\tglobal_load_dwordx4 %0, %14, %7 sc1\n\tglobal_load_lds_dwordx4 %13, %10 sc1\n\tglobal_load_lds_dwordx4 %13, %10 offset:1024 sc1\n\t"
                   "s_add_u32 m0, %9, 6208\n\tglobal_load_dwordx4 %1, %15, %7 sc1\n\tglobal_load_lds_dwordx4 %13, %11 sc1\n\tglobal_load_lds_dwordx4 %13, %11 offset:1024 sc1\n\t"
                   "s_add_u32 m0, %9, 12416\n\ts_cmp_lg_u32 %16, 0\n\tglobal_load_lds_dwordx4 %13, %12 sc1\n\tglobal_load_lds_dwordx4 %13, %12 offset:1024 sc1\n\t"
                   "s_cbranch_scc1 3f\n\t"
                   "global_load_lds_dwordx4 %13, %12 offset:2048 sc1\n\ts_mov_b32 m0, %9\n\ts_nop 0\n\tglobal_load_lds_dwordx4 %13, %10 offset:2048 sc1\n\t"
                   "s_add_u32 m0, %9, 6208\n\ts_nop 0\n\tglobal_load_lds_dwordx4 %13, %11 offset:2048 sc1\n\ts_branch 4f\n"
                   "3:\n\tv_add_u32 %14, %16, %14\n\tv_add_u32 %15, %16, %15\n\tglobal_load_dwordx4 %2, %14, %7 sc1\n\tglobal_load_dwordx4 %3, %15, %7 sc1\n"
                   "4:"
                   "\n\ts_waitcnt lgkmcnt(0)"
                   : "=&v"(qa), "=&v"(qb), "=&v"(qa2), "=&v"(qb2), "=&v"(pt), "=&s"(pn), "+v"(pollo), "+s"(ws), "+s"(tag64), "+s"(mA), "+s"(sA), "+s"(sB), "+s"(sG),
                     "+v"(l16), "+v"(qo0), "+v"(qo1), "+s"(qd)
                     , "=&s"(st[6])
                   :: "memory", "scc", "vcc");
    }
    u32 mk[16], dm[8];
    {
      int d = 4 * hh - 1 - c;
#pragma unroll
      for (int i = 0; i < 16; ++i) asm("v_add_u32 %0, %2, %1\n\tv_ashrrev_i32 %0, 31, %0" : "=&v"(mk[i]) : "v"(d), "i"((i & 3) + 8 * (i >> 2)));
    }
#pragma unroll
    for (int i = 0; i < 8; ++i) dm[i] = (mk[2 * i] & 0xffffu) | (mk[2 * i + 1] & 0xffff0000u);
    u32 hmask = (u32)-hh, kml = (u32)((hh | odd) - 1), kmh = (u32)((hh | (odd ^ 1)) - 1);
    const u32 realv = (u32)(((g2 & ((p4 | (p4 >> 1)) & 1))) - 1);
    u32 vlane = hh * 64 + q4 * 16 + ((realv & (kv + (3 + 2 * g2 + (p4 & 1)) * PLANE + (g2 * odd + (p4 >> 1)) * 8)) | (~realv & L_ONE));
    u32 vstep = realv & 512u;
    const int dl0 = g0 ? 0 : 8 + wl;
    u32 vold = vlane + (realv & (u32)(wl * 512));
    vlane += realv & (u32)(dl0 * 512);
    u32 kla = kv + hh * PLANE + c * 16 + (dl0 - 8) * 512, klb = kv + 2 * PLANE + c * 16 + (dl0 - 8) * 512;
    u32 oo = WS_O + X * (1u << 20) + t0 * 16384 + (c >> 4) * 8192 + xc * 1024 + (2 * odd + hh) * 256 + (c & 15) * 16;
    u32 co4 = (WS_C4 + X * (1u << 18) + t0 * 4096 + (c >> 4) * 2048 + hl * 128 + (c & 15) * 8) | (hmask & 0x80000000u);
    u32 ofl = WS_OFLAG + (t0 * 32 + head) * 128 + (lane & 1) * (qd * 8);
    u32 cnta = L_CNT + 4 * X, one = 1, tmpv, tmps;
    asm volatile("s_waitcnt vmcnt(0)\n\ts_mov_b64 exec, 1\n\tds_add_u32 %2, %3\n\ts_mov_b64 exec, -1\n"
                 "5:\n\tds_read_b32 %0, %2\n\ts_waitcnt lgkmcnt(0)\n\tv_readfirstlane_b32 %1, %0\n\ts_cmp_ge_u32 %1, 4\n\ts_cbranch_scc0 5b"
                 : "=&v"(tmpv), "=&s"(tmps), "+v"(cnta), "+v"(one), "+v"(qa), "+v"(qb), "+v"(qa2), "+v"(qb2), "+v"(sgr),
                   "+v"(kml), "+v"(kmh), "+v"(hmask), "+v"(vlane), "+v"(vold), "+v"(vstep), "+v"(kla), "+v"(klb), "+v"(oo), "+v"(co4), "+v"(ofl),
                   "+v"(dm[0]), "+v"(dm[1]), "+v"(dm[2]), "+v"(dm[3]), "+v"(dm[4]), "+v"(dm[5]), "+v"(dm[6]), "+v"(dm[7])
                 :: "memory", "scc");
    STAMP(7);
    {
      auto in_range = [&](const f32x16& O) {
        const float l = bsel(O[12], O[8], hmask);
        const float t = ((O[0] + O[1]) + (O[2] + O[3])) + ((O[4] + O[5]) + (O[6] + O[7])) + ((O[8] + O[9]) + (O[10] + O[11]));
        return __all(l > 0x1p-40f && l < 0x1p40f && __builtin_fabsf(t) < __builtin_inff());
      };
      auto put = [&](const f32x16& O, u32 ao, u32 ac) {
        const float inv = __builtin_amdgcn_rcpf(bsel(O[12], O[8], hmask));
        __builtin_amdgcn_raw_buffer_store_b128((u32x4){pk(O[0] * inv, O[1] * inv), pk(O[2] * inv, O[3] * inv), pk(O[4] * inv, O[5] * inv), pk(O[6] * inv, O[7] * inv)}, wsr, ao, 0, 16);
        __builtin_amdgcn_raw_buffer_store_b64((u32x2){pk(O[8] * inv, O[9] * inv), pk(O[10] * inv, O[11] * inv)}, wsr, ac, 0, 16);
      };
      bool general = ALWAYS_MAX || g0;
      if (!general) {
        const f32x16 O = attn_item<true>(__builtin_bit_cast(s16x8, qa), __builtin_bit_cast(s16x8, (u32x4){qb[0] & kml, qb[1] & kml, qb[2] & kmh, qb[3] & kmh}),
                                         kla, klb, 0, vlane, vold, vstep, dm, mk, 7, 0.f, 0.f);
        STAMP(12);
        if (in_range(O)) put(O, oo, co4); else general = true;
      }
      if (general) {
        int cc = c, hc = hh; PINV(cc); PINV(hc);
        NOUNROLL for (int it = 0; it <= g0; ++it) {
          const s16x8 q0 = __builtin_bit_cast(s16x8, qa);
          const s16x8 q1 = __builtin_bit_cast(s16x8, (u32x4){qb[0] & kml, qb[1] & kml, qb[2] & kmh, qb[3] & kmh});
          const int tq = it ? t1 : t0, nfull = g0 ? tq : 7;
          const u32 kd = g0 ? tq * 512 : 0, ka = kla + kd, kb = klb + kd, va = vlane + (vstep ? kd : 0u);
          bool shift = ALWAYS_MAX || !g0;
          f32x16 O;
          NOUNROLL for (;;) {
            const float negm = shift ? attn_negmax(q0, q1, ka, kb, nfull, !g0, cc, hc) : 0.f;
            O = attn_item<false>(q0, q1, ka, kb, g0 ? 4096u : 0u, va, g0 ? va : vold, vstep, dm, mk, nfull, negm, g0 ? -__builtin_inff() : negm);
            if (shift || in_range(O)) break;
            shift = true;
          }
          put(O, oo + (tq - t0) * 16384, co4 + (tq - t0) * 4096);
          qa = qa2; qb = qb2;
        }
      }
    }
    STAMP(8);
    WAITV(0);
    store_flag(ws, ofl, tagv, g0 ? 3 : 1);
    STAMP(9);
    if (X == 0) {
      lds_arrive(L_CNT + 8);
      __builtin_amdgcn_s_setprio(0);
      lds_wait(L_CNT + 8, 4);
      stage_wo<2>(woc, 1, wl, wv);
    } else {
      lds_arrive(L_CNT + 20);
    }
    }
  } else {
    ROW_LOADS();
    const int hg = xc * 2 + (bi - 30);
    const char *wg = karg(5), *wu = karg(6), *wd = karg(7);
    u32x4 hv[40];
    const int pc = (lane & 15) - (hg & 1) * 4, rg = lane >> 4;
    const bool act = pc >= 0 && pc < 12;
    const u32 gvo = (8 * w + 2 * rg) * 3072 + hg * 192 + (act ? pc : 0) * 16;
#define GU_ISSUE(n, r) asm volatile("global_load_dwordx4 %0, %4, %5\n\tglobal_load_dwordx4 %1, %4, %5 offset:3072\n\t" \
                       "global_load_dwordx4 %2, %4, %6\n\tglobal_load_dwordx4 %3, %4, %6 offset:3072" \
                       : "=&v"(hv[4 * (r)]), "=&v"(hv[4 * (r) + 1]), "=&v"(hv[4 * (r) + 2]), "=&v"(hv[4 * (r) + 3]) \
                       : "v"(gvo), "s"((const char*)wg + (n) * (64 * 3072)), "s"((const char*)wu + (n) * (64 * 3072)) : "memory")
    GU_ISSUE(0, 0); GU_ISSUE(1, 1);
    ROW_STATS(8);
    GU_ISSUE(2, 2); GU_ISSUE(3, 3); GU_ISSUE(4, 4);
    u32x4 ra, rb;
    const u32 roff = WS_RR + tid * 32;
    asm volatile("global_load_dwordx4 %0, %2, %3 sc1\n\tglobal_load_dwordx4 %1, %2, %3 offset:16 sc1" : "=&v"(ra), "=&v"(rb) : "v"(roff), "s"(ws) : "memory");
    STAMP(2);
    if (w >= 2) { GU_ISSUE(5, 5); }
    asm volatile("s_waitcnt vmcnt(4)\n\ts_cmp_ge_u32 %2, 2\n\ts_cbranch_scc1 1f\n\ts_waitcnt vmcnt(0)\n1:" : "+v"(ra), "+v"(rb) : "s"(w) : "memory", "scc");
    NOUNROLL for (u32 sp = 0; sp < SPIN_CAP && !__all(ra[1] == tag && ra[3] == tag && rb[1] == tag && rb[3] == tag); ++sp) {
      __builtin_amdgcn_s_sleep(1);
      asm volatile("global_load_dwordx4 %0, %2, %3 sc1\n\tglobal_load_dwordx4 %1, %2, %3 offset:16 sc1\n\ts_waitcnt vmcnt(0)" : "=&v"(ra), "=&v"(rb) : "v"(roff), "s"(ws) : "memory");
    }
    auto mkey = [](u32 f, u32 row) { return ((f ^ (((int)f >> 31) | 0x80000000u)) & 0xfffff800u) | (2047u - row); };
    auto mx = [](u32 p, u32 q) { return p > q ? p : q; };
    auto mn = [](u32 p, u32 q) { return p < q ? p : q; };
    u32 k1, k2;
    {
      const u32 e0 = mkey(ra[0], 4 * tid), e1 = mkey(ra[2], 4 * tid + 1), e2 = mkey(rb[0], 4 * tid + 2), e3 = mkey(rb[2], 4 * tid + 3);
      k1 = mx(mx(e0, e1), mx(e2, e3));
      k2 = mx(mn(mx(e0, e1), mx(e2, e3)), mx(mn(e0, e1), mn(e2, e3)));
    }
#define TOP2_STEP(o1, o2) do { const u32 p_ = (o1), q_ = (o2); k2 = mx(mn(k1, p_), mx(k2, q_)); k1 = mx(k1, p_); } while (0)
#define TOP2_DPP(C) TOP2_STEP(__builtin_amdgcn_mov_dpp(k1, C, 0xf, 0xf, true), __builtin_amdgcn_mov_dpp(k2, C, 0xf, 0xf, true))
    TOP2_DPP(0xB1); TOP2_DPP(0x4E); TOP2_DPP(0x141); TOP2_DPP(0x140);
    LAS u32x2* kk = lp<u32x2>(4096);
    if ((lane & 15) == 0) kk[4 * w + rg] = (u32x2){k1, k2};
    BAR();
    { const u32x2 e = kk[lane & 15], f = kk[16 + (lane & 15)]; k1 = e[0]; k2 = e[1]; TOP2_STEP(f[0], f[1]); }
    TOP2_DPP(0xB1); TOP2_DPP(0x4E); TOP2_DPP(0x141); TOP2_DPP(0x140);
    k1 = uni(k1); k2 = uni(k2);
    if (w >= 2) { GU_ISSUE(6, 6); GU_ISSUE(7, 7); }
    const u32 i0 = 2047u - (k1 & 2047u), i1 = 2047u - (k2 & 2047u);
    if (tid == 0) { gst64(ws, WS_TOP2 + hg * 128, ((u64)tag << 32) | i0 | (i1 << 16)); if (hg == 15) gst64(ws, WS_EPOCH, tag64); }
    STAMP(3);
    LAS u32* xp = lp<u32>(8192);
    if (w < 2) {
      const u32 row = w ? i1 : i0;
      u32 xrow[5];
#pragma unroll
      for (int i = 0; i < 5; ++i) xrow[i] = ((const u32*)x)[row * 320 + i * 64 + lane];
      const u32 sg = poll_gran(ws, WS_STATS + row * 8, tag);
      const float mb = blo(sg), rs = __builtin_amdgcn_rcpf(bhi(sg));
#pragma unroll
      for (int i = 0; i < 5; ++i) {
        const u32 t = pk(blo(xrow[i]) - mb, bhi(xrow[i]) - mb);
        xp[(i * 64 + lane) * 2 + w] = pk(blo(t) * rs, bhi(t) * rs);
      }
      GU_ISSUE(5, 5); GU_ISSUE(6, 6); GU_ISSUE(7, 7);
    }
    BAR();
    float ga[32];
#pragma unroll
    for (int i = 0; i < 32; ++i) ga[i] = 0.f;
#pragma unroll
    for (int n = 0; n < 10; ++n) {
      const int r = n;
      if (n == 1) GU_ISSUE(8, 8);
      if (n == 2) GU_ISSUE(9, 9);
      if (n >= 3 && n < 8) {
#pragma unroll
        for (int k = 3 * (n - 3); k < 3 * (n - 2); ++k) dma16(24576 + (k * 8 + w) * 1024, lane * 16, (const char*)wd + hg * (96 * 1280) + (k * 8 + w) * 1024);
      }
      asm volatile("s_waitcnt vmcnt(%4)" : "+v"(hv[4 * r]), "+v"(hv[4 * r + 1]), "+v"(hv[4 * r + 2]), "+v"(hv[4 * r + 3]) : "i"(n < 5 ? 63 : 4 * (9 - n) + (n < 7 ? 3 * (n - 2) : 15)) : "memory");
      const u32x2 xv = *(const LAS u32x2*)(xp + (32 * n + 4 * w + rg) * 2);
      const bf16x2 x0 = asb(xv[0]), x1 = asb(xv[1]);
#pragma unroll
      for (int m = 0; m < 2; ++m)
#pragma unroll
        for (int i = 0; i < 4; ++i) {
          const bf16x2 lo = asb(__builtin_amdgcn_perm(hv[4 * r + 2 * m + 1][i], hv[4 * r + 2 * m][i], 0x05040100u));
          const bf16x2 hi = asb(__builtin_amdgcn_perm(hv[4 * r + 2 * m + 1][i], hv[4 * r + 2 * m][i], 0x07060302u));
          ga[16 * m + 2 * i] = __builtin_amdgcn_fdot2_f32_bf16(lo, x0, ga[16 * m + 2 * i], false);
          ga[16 * m + 8 + 2 * i] = __builtin_amdgcn_fdot2_f32_bf16(lo, x1, ga[16 * m + 8 + 2 * i], false);
          ga[16 * m + 2 * i + 1] = __builtin_amdgcn_fdot2_f32_bf16(hi, x0, ga[16 * m + 2 * i + 1], false);
          ga[16 * m + 8 + 2 * i + 1] = __builtin_amdgcn_fdot2_f32_bf16(hi, x1, ga[16 * m + 8 + 2 * i + 1], false);
        }
#pragma unroll
      for (int i = 0; i < 32; i += 8) asm volatile("" : "+v"(ga[i]), "+v"(ga[i + 1]), "+v"(ga[i + 2]), "+v"(ga[i + 3]), "+v"(ga[i + 4]), "+v"(ga[i + 5]), "+v"(ga[i + 6]), "+v"(ga[i + 7]));
    }
    STAMP(4);
    LAS float* red = lp<float>(11264);
    {
      float h16[16], h8[8];
#pragma unroll
      for (int i = 0; i < 16; ++i) { auto r = __builtin_amdgcn_permlane32_swap(__float_as_uint(ga[i]), __float_as_uint(ga[i + 16]), false, false); h16[i] = __uint_as_float(r[0]) + __uint_as_float(r[1]); }
#pragma unroll
      for (int i = 0; i < 8; ++i) { auto r = __builtin_amdgcn_permlane16_swap(__float_as_uint(h16[i]), __float_as_uint(h16[i + 8]), false, false); h8[i] = __uint_as_float(r[0]) + __uint_as_float(r[1]); }
      if (act) {
        LAS float* d = red + w * 384 + rg * 96 + pc * 8;
        *(LAS f32x4*)d = (f32x4){h8[0], h8[1], h8[2], h8[3]}; *(LAS f32x4*)(d + 4) = (f32x4){h8[4], h8[5], h8[6], h8[7]};
      }
    }
    BAR();
    STAMP(6);
    LAS u16* hs = lp<u16>(4096);
    if (tid < 192) {
      float g = 0.f, u = 0.f;
#pragma unroll
      for (int k = 0; k < 8; ++k) { g += red[k * 384 + tid]; u += red[k * 384 + 192 + tid]; }
      g = rbf(g); u = rbf(u);
      const float hval = rbf(g * __builtin_amdgcn_rcpf(1.f + __expf(-g))) * u;
      const int t = tid >= 96, f = tid - 96 * t;
      hs[(f >> 1) * 4 + t * 2 + (f & 1)] = (u16)pk(hval, hval);
    }
    STAMP(12);
    WAITBAR(0);
    STAMP(7);
    float y[2][8];
#pragma unroll
    for (int i = 0; i < 8; ++i) y[0][i] = y[1][i] = 0.f;
    const int rgp = tid / 80, cp = tid - 80 * rgp;
    if (tid < 480) {
#pragma unroll
      for (int i = 0; i < 8; ++i) {
        const u32 ad = 24576 + (12 * i + 2 * rgp) * 1280 + cp * 16;
        const u32x4 d0 = *lp<u32x4>(ad), d1 = *lp<u32x4>(ad + 1280);
        const u32x2 hp = *lp<u32x2>(4096 + (6 * i + rgp) * 8);
        const bf16x2 h0 = asb(hp[0]), h1 = asb(hp[1]);
#pragma unroll
        for (int k = 0; k < 4; ++k) {
          const bf16x2 dl_ = asb(__builtin_amdgcn_perm(d1[k], d0[k], 0x05040100u));
          const bf16x2 dh_ = asb(__builtin_amdgcn_perm(d1[k], d0[k], 0x07060302u));
          y[0][2 * k] = __builtin_amdgcn_fdot2_f32_bf16(dl_, h0, y[0][2 * k], false); y[1][2 * k] = __builtin_amdgcn_fdot2_f32_bf16(dl_, h1, y[1][2 * k], false);
          y[0][2 * k + 1] = __builtin_amdgcn_fdot2_f32_bf16(dh_, h0, y[0][2 * k + 1], false); y[1][2 * k + 1] = __builtin_amdgcn_fdot2_f32_bf16(dh_, h1, y[1][2 * k + 1], false);
        }
      }
    }
    STAMP(8);
    BAR();
    LAS float* r3 = lp<float>(24576);
    if (tid < 480) {
#pragma unroll
      for (int t = 0; t < 2; ++t) {
        LAS float* d = r3 + rgp * 1280 + t * 640 + cp * 8;
        *(LAS f32x4*)d = (f32x4){y[t][0], y[t][1], y[t][2], y[t][3]}; *(LAS f32x4*)(d + 4) = (f32x4){y[t][4], y[t][5], y[t][6], y[t][7]};
      }
    }
    BAR();
    if (tid < 320) {
      const u32 kq = (tid >= 160 ? k2 : k1) & 0xfffff800u;
      const float rsel = rbf(__uint_as_float(kq ^ ((kq >> 31) ? 0x80000000u : ~0u)));
      const float sg = rbf(__builtin_amdgcn_rcpf(1.f + __expf(-rsel)));
      f32x4 s = *(const LAS f32x4*)(r3 + tid * 4);
#pragma unroll
      for (int k = 1; k < 6; ++k) s += *(const LAS f32x4*)(r3 + k * 1280 + tid * 4);
      const u32 off = WS_Y + hg * 10240 + tid * 32;
      __builtin_amdgcn_raw_buffer_store_b128((u32x4){__float_as_uint(s[0] * sg), tag, __float_as_uint(s[1] * sg), tag}, wsr, off, 0, 16);
      __builtin_amdgcn_raw_buffer_store_b128((u32x4){__float_as_uint(s[2] * sg), tag, __float_as_uint(s[3] * sg), tag}, wsr, off + 16, 0, 16);
    }
    BAR();
    STAMP(5);
    u32 wv[8];
    wo_offsets(wv, wl, lane);
    RES_PREP(b);
    RES_ISSUE();
    stage_wo<2>(karg(4) + (bi & 7) * 160, X, wl, wv);
    asm volatile("s_waitcnt vmcnt(0) lgkmcnt(0)\n\ts_barrier" : "+v"(sgr) :: "memory");
    if (X == 1) { lds_arrive(L_CNT + 20); lds_arrive(L_CNT + 4); }
    STAMP(9);
  }

  {
    int b3 = blockIdx.x; PINS(b3);
    const int rt3 = (b3 & 7) + 8 * (b3 >> 6), ct3 = (b3 >> 3) & 7;
    const int tk = lane & 15, g = lane >> 4;
    const int t16 = 4 * rt3 + wl, row = 16 * t16 + tk;
    const u32 wfa = (8 * g + 4 * (g & 1) + ((lane >> 2) & 3)) * 160 + (lane & 3) * 8, wfb = (8 * g + 4 * ((g & 1) ^ 1) + ((lane >> 2) & 3)) * 160 + (lane & 3) * 8;
    const u32 res = L_KVB + wl * 5120 + lane * 16;
    f32x4 acc[5];
    const u32 wb0 = X == 0 ? L_WOA : L_KVA, wb7 = X == 0 ? L_WOA + 7 * 5120 : L_WOB7;
    char* out = karg(8);
    u32 ob = WS_O + X * (1u << 20) + t16 * 8192 + lane * 16, cb = WS_C4 + X * (1u << 18) + t16 * 2048 + g * 256 + tk * 8;
    u32 ofo = (row * 640 + 80 * ct3 + 16 * (g & 1) + 8 * (g >> 1)) * 2, ofo2 = (row * 640 + 80 * ct3 + 64 + 4 * g) * 2;
    u32 w8a = wb7 + 5120 + wfa, w8b = wb7 + 5120 + wfb, resv = res;
    u32 ob2 = ob + 4096, pfo = WS_OFLAG + ((t16 >> 1) * 32 + 16 * X + tk) * 128;
    u32 w0a = wb0 + wfa, w0b = wb0 + wfb;
    PINV(ofo); PINV(ofo2); PINV(w0a); PINV(w0b); PINV(w8a); PINV(w8b); PINV(resv); PINV(ob); PINV(ob2); PINV(cb); PINV(pfo); PINS(out);
    bool early = false;
    if (X == 0) {
      u32 tp = (u32)sgr;
      if (!__all((u32)(sgr >> 32) == tag)) tp = poll_gran(ws, WS_TOP2 + (b3 & 15) * 128, tag);
      tp = uni(tp);
#pragma unroll
      for (int t = 0; t < 5; ++t) acc[t] = (f32x4){0.f, 0.f, 0.f, 0.f};
      early = (int)((tp & 0xffff) >> 4) == t16 || (int)(tp >> 20) == t16;
      if (early) { WAITV(0); lds_arrive(L_CNT + 12); __builtin_amdgcn_s_setprio(1); }
#pragma unroll 1
      for (int cidx = 0; cidx < 2; ++cidx) {
        const u32 id = cidx ? tp >> 16 : tp & 0xffff;
        if ((int)(id >> 4) != t16) continue;
        LAS float* yb = lp<float>(L_YB + wl * 320);
#pragma unroll 1
        for (int p = 0; p < 2; ++p) {
          const int col = p * 64 + lane;
          const u32 off = WS_Y + cidx * 5120 + (80 * ct3 + (col < 80 ? col : 79)) * 8;
          float s = 0.f;
          NOUNROLL for (u32 sp = 0; sp < SPIN_CAP; ++sp) {
            u64 gv[16]; bool ok = true; s = 0.f;
#pragma unroll
            for (int k = 0; k < 16; ++k) gv[k] = gld64(ws, off + k * 10240);
#pragma unroll
            for (int k = 0; k < 16; ++k) { ok = ok && (u32)(gv[k] >> 32) == tag; s += __uint_as_float((u32)gv[k]); }
            if (__all(ok)) break;
            __builtin_amdgcn_s_sleep(1);
          }
          if (col < 80) yb[col] = s;
        }
        asm volatile("s_waitcnt lgkmcnt(0)" ::: "memory");
        if (tk == (int)(id & 15)) {
#pragma unroll
          for (int t = 0; t < 5; ++t) acc[t] += *(const LAS f32x4*)(yb + 16 * t + 4 * g);
        }
        asm volatile("s_waitcnt lgkmcnt(0)" ::: "memory");
      }
    } else {
      u32 sg = (u32)sgr;
      if (!__all((u32)(sgr >> 32) == tag)) sg = poll_gran(ws, WS_STATS + row * 8, tag);
      const float mb = blo(sg), rs = __builtin_amdgcn_rcpf(bhi(sg));
#pragma unroll
      for (int t = 0; t < 5; ++t) {
        const u32x2 xr = *lp<u32x2>(L_XR + wl * 3072 + tk * 160 + g * 8 + t * 32);
#pragma unroll
        for (int i = 0; i < 2; ++i) {
          const float a0 = blo(xr[i]), a1 = bhi(xr[i]);
          const u32 d = pk(a0 - mb, a1 - mb), n = pk(blo(d) * rs, bhi(d) * rs);
          acc[t][2 * i] = a0 + blo(n); acc[t][2 * i + 1] = a1 + bhi(n);
        }
        PINV(acc[t]);
      }
    }
    STAMP(1);
    lds_wait(X == 0 ? L_CNT + 4 : L_CNT + 12, 4);
    STAMP(2);
    s16x8 wq[8][5];
#pragma unroll
    for (int ks = 0; ks < 8; ++ks)
#pragma unroll
      for (int t = 0; t < 5; ++t) wq[ks][t] = ldstr((ks < 7 ? w0a + ks * 5120 : w8a - 5120) + t * 32, (ks < 7 ? w0b + ks * 5120 : w8b - 5120) + t * 32);
    if (X == 0 && !early) { WAITV(0); lds_arrive(L_CNT + 12); __builtin_amdgcn_s_setprio(1); }
    STAMP(10);
    u32x4 of[10];
    {
      u64 pt; u32 pn;
      asm volatile(POLL_TXT("%10", "%11", "%12", "%13", "%14")
                   "s_memrealtime %18\n\t"
                   "global_load_dwordx4 %0, %15, %13 sc1\n\tglobal_load_dwordx4 %1, %15, %13 offset:1024 sc1\n\tglobal_load_dwordx4 %2, %15, %13 offset:2048 sc1\n\t"
                   "global_load_dwordx4 %3, %15, %13 offset:3072 sc1\n\tglobal_load_dwordx4 %4, %16, %13 sc1\n\tglobal_load_dwordx4 %5, %16, %13 offset:1024 sc1\n\t"
                   "global_load_dwordx4 %6, %16, %13 offset:2048 sc1\n\tglobal_load_dwordx4 %7, %16, %13 offset:3072 sc1\n\t"
                   "global_load_dwordx2 v[240:241], %17, %13 sc1\n\tglobal_load_dwordx2 v[242:243], %17, %13 offset:128 sc1\n\t"
                   "global_load_dwordx2 v[244:245], %17, %13 offset:1024 sc1\n\tglobal_load_dwordx2 v[246:247], %17, %13 offset:1152 sc1\n\t"
                   "s_waitcnt lgkmcnt(0)"
                   : "=&v"(of[0]), "=&v"(of[1]), "=&v"(of[2]), "=&v"(of[3]), "=&v"(of[4]), "=&v"(of[5]), "=&v"(of[6]), "=&v"(of[7]),
                     "=&{v[240:243]}"(of[8]), "=&{v[244:247]}"(of[9]), "=&v"(pt), "=&s"(pn), "+v"(pfo), "+s"(ws), "+s"(tag64), "+v"(ob), "+v"(ob2), "+v"(cb)
                     , "=&s"(st[11])
                   :: "memory", "scc", "vcc");
    }
    if (X == 1) {
      lds_wait(L_CNT + 32 + 4 * wl, 1);
#pragma unroll
      for (int t = 0; t < 5; ++t) acc[t] += *lp<f32x4>(resv + t * 1024);
    }
    s16x8 w8[2][5];
#pragma unroll
    for (int ks = 0; ks < 10; ++ks) {
      if (ks == 0) asm volatile("s_waitcnt vmcnt(10)" : "+v"(of[0]), "+v"(of[1]) :: "memory");
      if (ks == 2) asm volatile("s_waitcnt vmcnt(7)" : "+v"(of[2]), "+v"(of[3]), "+v"(of[4]) :: "memory");
      if (ks == 5) asm volatile("s_waitcnt vmcnt(4)" : "+v"(of[5]), "+v"(of[6]), "+v"(of[7]) :: "memory");
      if (ks == 8) asm volatile("s_waitcnt vmcnt(0)" : "+v"(of[8]), "+v"(of[9]) :: "memory");
      SB0();
#pragma unroll
      for (int t = 0; t < 5; ++t) {
        acc[t] = mfma16(ks < 8 ? wq[ks < 8 ? ks : 0][t] : w8[ks & 1][t], __builtin_bit_cast(s16x8, of[ks]), acc[t]);
        if (ks == 2 || ks == 3) w8[ks & 1][t] = ldstr(w8a + (ks & 1) * 5120 + t * 32, w8b + (ks & 1) * 5120 + t * 32);
      }
      SB0();
    }
    STAMP(30);
    if (X == 0) {
      lds_wait(L_CNT + 20, 4);
#pragma unroll
      for (int t = 0; t < 5; ++t) *lp<f32x4>(resv + t * 1024) = acc[t];
      lds_arrive(L_CNT + 32 + 4 * wl);
    } else {
      u32 o[10];
#pragma unroll
      for (int t = 0; t < 5; ++t) { o[2 * t] = pk(acc[t][0], acc[t][1]); o[2 * t + 1] = pk(acc[t][2], acc[t][3]); }
#pragma unroll
      for (int t = 0; t < 4; t += 2) {
        auto r0 = __builtin_amdgcn_permlane16_swap(o[2 * t], o[2 * t + 2], false, false);
        auto r1 = __builtin_amdgcn_permlane16_swap(o[2 * t + 1], o[2 * t + 3], false, false);
        *(GAS u32x4*)(out + ofo + 32 * t) = (u32x4){r0[0], r1[0], r0[1], r1[1]};
      }
      *(GAS u32x2*)(out + ofo2) = (u32x2){o[8], o[9]};
    }
    STAMP(31);
  }
  WAITV(0);
  STAMP(14);
  { const u64 tt_ = *(volatile GAS u64*)(ws + (15u << 20)); u64 d = st[0] - tt_; d = d < 2000 ? d : 0; const u64 tgt = st[14] + 10 * d; (void)cyc0; while (__builtin_amdgcn_s_memrealtime() < tgt) __builtin_amdgcn_s_sleep(2); }
}

__global__ void tiny(u64* p) { if (threadIdx.x == 0) *p = __builtin_amdgcn_s_memrealtime(); }
extern "C" void kernel_launch(void* const* d_in, const int* in_sizes, int n_in, void* d_out, int out_size, void* d_ws, size_t ws_size,
                              hipStream_t stream) {
  (void)in_sizes; (void)n_in; (void)out_size; (void)ws_size;
  (void)hipFuncSetAttribute((const void*)fused_block, hipFuncAttributeMaxDynamicSharedMemorySize, LDS_BYTES);
  tiny<<<1, 64, 0, stream>>>((u64*)((char*)d_ws + (15u << 20)));
  fused_block<<<256, 512, LDS_BYTES, stream>>>((const u16*)d_in[0], (const u16*)d_in[1], (const u16*)d_in[4], (char*)d_ws, (const u16*)d_in[2],
                                               (const u16*)d_in[5], (const u16*)d_in[6], (const u16*)d_in[7], (u16*)d_out);
}
```

```cpp
#include <hip/hip_runtime.h>
#include <stdint.h>

typedef unsigned short u16;
typedef unsigned u32;
typedef unsigned long long u64;
typedef u32 u32x2 __attribute__((ext_vector_type(2)));
typedef u32 u32x4 __attribute__((ext_vector_type(4)));
typedef short s16x4 __attribute__((ext_vector_type(4)));
typedef short s16x8 __attribute__((ext_vector_type(8)));
typedef float f32x2 __attribute__((ext_vector_type(2)));
typedef float f32x4 __attribute__((ext_vector_type(4)));
typedef float f32x16 __attribute__((ext_vector_type(16)));
typedef __bf16 bf16x2 __attribute__((ext_vector_type(2)));
#define LAS __attribute__((address_space(3)))
#define GAS __attribute__((address_space(1)))
#define DI __device__ __forceinline__
#define RLX __ATOMIC_RELAXED, __HIP_MEMORY_SCOPE_AGENT
#define SB0() __builtin_amdgcn_sched_barrier(0)

#define ALWAYS_MAX 0
#define P1X 0
#define RR_DELAY 340
#define CODE_LINES 230

constexpr u32 WS_EPOCH = 0;
constexpr u32 WS_QFLAG = 4096;
constexpr u32 WS_OFLAG = 65536;
constexpr u32 WS_TOP2  = 327680;
constexpr u32 WS_STATS = 331776;
constexpr u32 WS_RR    = 348160;
constexpr u32 WS_Y     = 393216;
constexpr u32 WS_XN    = 557056;
constexpr u32 WS_GF    = 655360;
constexpr u32 WS_GP    = 13u << 20;
constexpr u32 WS_QKV   = 1u << 20;
constexpr u32 WS_O     = 10u << 20;
constexpr u32 WS_C4    = 12u << 20;
constexpr u32 WS_TS    = 16u << 20;
constexpr u32 WS_SPAN  = 14u << 20;

constexpr u32 SLOT = 24576;
constexpr u32 L_BP = 4 * SLOT;
constexpr u32 L_WOB = 0;
constexpr u32 L_KVA = 51200, L_KVB = 88576, PLANE = 6208;
constexpr u32 L_WOA7 = 125952;
constexpr u32 L_G = 125952;
constexpr u32 L_XR = 142336;
constexpr u32 L_MISC = 155648;
constexpr u32 L_CNT = L_MISC;
constexpr u32 L_ONE = L_MISC + 1664;
constexpr u32 L_PF  = L_MISC + 2560;
constexpr u32 L_YB  = L_MISC + 5376;
constexpr u32 L_CP  = L_MISC + 2816;
constexpr u32 LDS_BYTES = 163840;
static_assert((L_ONE & 255) == 128 && (L_KVA & 255) == 0 && (L_KVB & 255) == 0, "bank quarters");

constexpr u32 SPIN_CAP = 1u << 17;

template <int I> struct IC { static constexpr int v = I; };
template <int I, int N, class F> DI void static_for(F&& f) { if constexpr (I < N) { f(IC<I>{}); static_for<I + 1, N>(f); } }
template <class T> DI LAS T* lp(u32 a) { return (LAS T*)(uintptr_t)a; }
DI u32 pk(float a, float b) { f32x2 v = {a, b}; return __builtin_bit_cast(u32, __builtin_convertvector(v, bf16x2)); }
DI float blo(u32 v) { return __uint_as_float(v << 16); }
DI float bhi(u32 v) { return __uint_as_float(v & 0xffff0000u); }
DI float rbf(float a) { return blo(pk(a, a)); }
DI u32 uni(u32 v) { return __builtin_amdgcn_readfirstlane(v); }
template <int C> DI float dppf(float v) { return __uint_as_float(__builtin_amdgcn_mov_dpp(__float_as_uint(v), C, 0xf, 0xf, true)); }
DI float x16sum(float v) { auto r = __builtin_amdgcn_permlane16_swap(__float_as_uint(v), __float_as_uint(v), false, false); return __uint_as_float(r[0]) + __uint_as_float(r[1]); }
DI float x32sum(float v) { auto r = __builtin_amdgcn_permlane32_swap(__float_as_uint(v), __float_as_uint(v), false, false); return __uint_as_float(r[0]) + __uint_as_float(r[1]); }
DI float wsum(float v) {
  v += dppf<0xB1>(v); v += dppf<0x4E>(v); v += dppf<0x141>(v); v += dppf<0x140>(v);
  return x32sum(x16sum(v));
}
DI float sel(float a, float b, u64 m) { return __builtin_amdgcn_inverse_ballot_w64(m) ? b : a; }
DI float bsel(float a, float b, u32 m) { return __uint_as_float((__float_as_uint(b) & m) | (__float_as_uint(a) & ~m)); }
DI bf16x2 asb(u32 v) { return __builtin_bit_cast(bf16x2, v); }
DI f32x16 mfma32(s16x8 a, s16x8 b, f32x16 c) { return __builtin_amdgcn_mfma_f32_32x32x16_bf16(a, b, c, 0, 0, 0); }
DI f32x4 mfma16(s16x8 a, s16x8 b, f32x4 c) { return __builtin_amdgcn_mfma_f32_16x16x32_bf16(a, b, c, 0, 0, 0); }
DI s16x8 lds128(u32 a) { return *lp<s16x8>(a); }
DI s16x8 ldstr(u32 a, u32 b) {
  s16x4 lo = __builtin_amdgcn_ds_read_tr16_b64_v4i16(lp<s16x4>(a)), hi = __builtin_amdgcn_ds_read_tr16_b64_v4i16(lp<s16x4>(b));
  return __builtin_shufflevector(lo, hi, 0, 1, 2, 3, 4, 5, 6, 7);
}
DI void dma16(u32 lds, u32 voff, const void* sbase) {
  asm volatile("s_mov_b32 m0, %0\n\ts_nop 0\n\tglobal_load_lds_dwordx4 %1, %2" :: "s"(lds), "v"(voff), "s"(sbase) : "memory");
}
DI void dma16c(u32 lds, u32 voff, const void* sbase) {
  asm volatile("s_mov_b32 m0, %0\n\ts_nop 0\n\tglobal_load_lds_dwordx4 %1, %2 sc1" :: "s"(lds), "v"(voff), "s"(sbase) : "memory");
}
#define WAITV(n) asm volatile("s_waitcnt vmcnt(" #n ")" ::: "memory")
#define WAITBAR(n) asm volatile("s_waitcnt vmcnt(%0) lgkmcnt(0)\n\ts_barrier" :: "i"(n) : "memory")
#define WAITBAR2(n0, n1) asm volatile("s_waitcnt vmcnt(" #n1 ") lgkmcnt(0)\n\ts_cmp_lg_u32 %0, 0\n\ts_cbranch_scc1 1f\n\ts_waitcnt vmcnt(" #n0 ")\n1:\n\ts_barrier" :: "s"(X) : "memory", "scc")
#define WAITBAR1(n1) asm volatile("s_cmp_lg_u32 %0, 0\n\ts_cbranch_scc0 1f\n\ts_waitcnt vmcnt(" #n1 ")\n1:\n\ts_waitcnt lgkmcnt(0)\n\ts_barrier" :: "s"(X) : "memory", "scc")
#define WAITBARV(n) asm volatile("s_waitcnt vmcnt(%0)\n\ts_barrier" :: "i"(n) : "memory")
#define BAR() asm volatile("s_waitcnt lgkmcnt(0)\n\ts_barrier" ::: "memory")
DI u64 gld64(const char* ws, u32 off) { return __hip_atomic_load((GAS u64*)(ws + off), RLX); }
DI u64 gld64w(const char* ws, u32 off) {
  u64 r; asm volatile("global_load_dwordx2 %0, %1, %2 sc1\n\ts_waitcnt vmcnt(0)" : "=&v"(r) : "v"(off), "s"(ws) : "memory"); return r;
}
#define NOUNROLL _Pragma("unroll 1")
DI void gst64(char* ws, u32 off, u64 v) { __hip_atomic_store((GAS u64*)(ws + off), v, RLX); }
DI void lds_arrive(u32 a) {
  u32 va = a, one = 1;
  asm volatile("s_waitcnt lgkmcnt(0)\n\ts_mov_b64 exec, 1\n\tds_add_u32 %0, %1\n\ts_mov_b64 exec, -1" :: "v"(va), "v"(one) : "memory");
}
DI void lds_wait(u32 a, u32 n) {
  u32 va = a, t, c;
  asm volatile("1:\n\tds_read_b32 %0, %2\n\ts_waitcnt lgkmcnt(0)\n\tv_readfirstlane_b32 %1, %0\n\ts_cmp_ge_u32 %1, %3\n\ts_cbranch_scc1 2f\n\ts_sleep 1\n\ts_branch 1b\n2:"
               : "=&v"(t), "=&s"(c) : "v"(va), "s"(n) : "memory", "scc");
}
#define POLL_TXT(T, N, OFF, WS, TAG) "s_mov_b32 " N ", 0x20000\n1:\n\tglobal_load_dwordx2 " T ", " OFF ", " WS " sc1\n\ts_waitcnt vmcnt(0)\n\t" \
    "v_cmp_ne_u64 vcc, " TAG ", " T "\n\ts_cbranch_vccz 2f\n\ts_sleep 1\n\ts_sub_u32 " N ", " N ", 1\n\ts_cbranch_scc0 1b\n2:\n\t"
DI void poll_flags(const char* ws, u32 off, u64 tag) {
  u64 t; u32 n;
  asm volatile(POLL_TXT("%0", "%1", "%2", "%3", "%4") : "=&v"(t), "=&s"(n) : "v"(off), "s"(ws), "s"(tag) : "memory", "scc", "vcc");
}
DI u32 poll_gran(const char* ws, u32 off, u32 tag) {
  u64 g;
  NOUNROLL for (u32 s = 0; s < SPIN_CAP; ++s) { g = gld64w(ws, off); if (__all((u32)(g >> 32) == tag)) break; __builtin_amdgcn_s_sleep(1); }
  return (u32)g;
}
DI void store_flag(char* ws, u32 off, u64 val, u64 mask) {
  asm volatile("s_mov_b64 exec, %3\n\tglobal_store_dwordx2 %0, %1, %2 sc1\n\ts_mov_b64 exec, -1" :: "v"(off), "v"(val), "s"(ws), "s"(mask) : "memory");
}
#define PINV(x) asm volatile("" : "+v"(x))
#define PINS(x) asm volatile("" : "+s"(x))

#define UOFF 0
#define STAMP(i) do {} while (0)

DI void wo_offsets(u32 (&wv)[8], int wl, int lane) {
#pragma unroll
  for (int k = 0; k < 8; ++k) {
    const int n = k < 5 ? k : k < 7 ? 3 * (wl & 1) + k - 5 : (wl & 1 ? 4 : 2);
    const u32 P = 64 * n + lane, r = P / 10u, pc = P - 10 * r, g = r >> 3, j = (r & 7) ^ ((g & 1) << 2);
    wv[k] = ((k >= 5 ? 40 * g + 16 : 8 * (g + (g >> 1))) + j) * 1280 + pc * 16;
    PINV(wv[k]);
  }
}
template <int PART>
DI void stage_wo(const char* woc, int H, int wl, const u32 (&wv)[8]) {
  auto at = [&](int ks) { return H == 1 ? L_WOB + ks * 5120 : ks < 7 ? L_KVA + ks * 5120 : L_WOA7 + (ks - 7) * 5120; };
  const int n2 = 3 * (wl & 1), k2 = wl >> 1;
  const char *s0 = woc + (320 * H + 40 * wl) * 1280, *s2 = woc + (320 * H + 160 * k2) * 1280;
#define WO_D(M, V, S) "s_add_u32 m0, " M "\n\ts_nop 0\n\tglobal_load_lds_dwordx4 " V ", " S "\n\t"
#define WO_5 "s_nop 4\n\t" WO_D("%7, 0", "%0", "%5") WO_D("m0, 1024", "%1", "%5") WO_D("m0, 1024", "%2", "%5") WO_D("m0, 1024", "%3", "%5") WO_D("m0, 1024", "%4", "%5")
  if (PART != 1)
    asm volatile(WO_5 WO_D("%8, 0", "%9", "%6")
                 :: "v"(wv[0]), "v"(wv[1]), "v"(wv[2]), "v"(wv[3]), "v"(wv[4]), "s"(s0), "s"(s2), "s"(at(wl)), "s"(at(8 + k2) + (wl & 1 ? 4 : 2) * 1024), "v"(wv[7]) : "memory", "scc");
  if (PART != 0)
    asm volatile(WO_5 WO_D("%8, 0", "%9", "%6") WO_D("m0, 1024", "%10", "%6")
                 :: "v"(wv[0]), "v"(wv[1]), "v"(wv[2]), "v"(wv[3]), "v"(wv[4]), "s"(s0 + 160 * 1280), "s"(s2), "s"(at(wl + 4)), "s"(at(8 + k2) + n2 * 1024), "v"(wv[5]), "v"(wv[6]) : "memory", "scc");
}

constexpr int slice_of(int v) { return v < 6 ? v : (v >= 9 && v < 13) ? v - 3 : -1; }
constexpr int page_of(int v) { return v < 9 ? v - 6 : v - 10; }
constexpr int OP_GR = 41, OP_PF = 43;
constexpr int sched_op(int u, int i) {
  if (u == 0) return i < 3 ? 6 + i : -1;
  if (u <= 3) return i < 3 ? 3 * (u + 2) + i : 30 + u - 1;
  if (u <= 6) return i < 3 ? 3 * (u + 2) + i : -1;
  if (u == 7) return i < 3 ? 27 + i : OP_GR;
  if (u == 8) return i < 3 ? 33 + i : -1;
  if (u == 9) return i == 0 ? 39 : -1;
  if (u == 10) return i < 3 ? 36 + i : -1;
  if (u == 11) return i == 0 ? OP_PF : -1;
  return -1;
}
constexpr int younger(int op, int u) {
  int n = op < 6 ? 5 - op + 1 : 0; bool seen = op < 6;
  for (int v = 0; v < u; ++v) for (int i = 0; i < 4; ++i) { const int o = sched_op(v, i); if (o < 0) continue; if (seen) ++n; if (o == op) seen = true; }
  return n;
}
constexpr int need_op(int u) {
  return slice_of(u) >= 0 ? 3 * slice_of(u) + 2 : u == 6 ? 32 : u == 13 ? 38 : -1;
}
template <bool NEXT, bool LOAD>
DI void attn_block(const f32x16& sc, f32x16& sn, f32x16& O, s16x8 q0, s16x8 q1, s16x8& kn0, s16x8& kn1, u32 nka, u32 nkb, u32 va, s16x8& pend, s16x8& vpend) {
  f32x16 z;
#pragma unroll
  for (int i = 0; i < 16; ++i) z[i] = 0.f;
  SB0();
  if (NEXT) sn = mfma32(kn0, q0, z);
  const s16x8 v0 = ldstr(va, va + 128), v1 = ldstr(va + 256, va + 384);
  u32 p[8];
  float ex[16];
#pragma unroll
  for (int i = 0; i < 4; ++i) ex[i] = __builtin_amdgcn_exp2f(sc[i]);
  SB0();
  if (NEXT) sn = mfma32(kn1, q1, sn);
  if (LOAD) { kn0 = lds128(nka); kn1 = lds128(nkb); }
#pragma unroll
  for (int i = 4; i < 8; ++i) ex[i] = __builtin_amdgcn_exp2f(sc[i]);
  p[0] = pk(ex[0], ex[1]); p[1] = pk(ex[2], ex[3]);
  SB0();
  O = mfma32(vpend, pend, O); PINV(O);
  SB0();
#pragma unroll
  for (int i = 8; i < 12; ++i) ex[i] = __builtin_amdgcn_exp2f(sc[i]);
  p[2] = pk(ex[4], ex[5]); p[3] = pk(ex[6], ex[7]);
  SB0();
  O = mfma32(v0, __builtin_bit_cast(s16x8, (u32x4){p[0], p[1], p[2], p[3]}), O); PINV(O);
  SB0();
#pragma unroll
  for (int i = 12; i < 16; ++i) ex[i] = __builtin_amdgcn_exp2f(sc[i]);
  p[4] = pk(ex[8], ex[9]); p[5] = pk(ex[10], ex[11]); p[6] = pk(ex[12], ex[13]); p[7] = pk(ex[14], ex[15]);
  pend = __builtin_bit_cast(s16x8, (u32x4){p[4], p[5], p[6], p[7]}); vpend = v1;
  SB0();
}
DI f32x16 attn_full(s16x8 q0, s16x8 q1, u32 ka, u32 kb, u32 va, u32 vo, u32 vstep, const u32 (&dm)[8], const u32 (&mk)[16]) {
  f32x16 z, O, S[2];
#pragma unroll
  for (int i = 0; i < 16; ++i) z[i] = 0.f;
  s16x8 kn0, kn1, pend, vpend;
  {
    const s16x8 kd0 = lds128(ka + 4096), ko0 = lds128(ka), kd1 = lds128(kb + 4096), ko1 = lds128(kb), k10 = lds128(ka + 3584), k11 = lds128(kb + 3584);
    const s16x8 v0 = ldstr(va, va + 128), vo0 = ldstr(vo, vo + 128), vo1 = ldstr(vo + 256, vo + 384), v1 = ldstr(va + 256, va + 384);
    kn0 = lds128(ka + 3072); kn1 = lds128(kb + 3072);
    SB0();
    f32x16 sd = mfma32(kd0, q0, z), so = mfma32(ko0, q0, z);
    sd = mfma32(kd1, q1, sd); so = mfma32(ko1, q1, so);
    S[1] = mfma32(k10, q0, z);
    SB0();
    u32 p[8], pd[8];
#pragma unroll
    for (int i = 0; i < 8; ++i) {
      p[i] = pk(__builtin_amdgcn_exp2f(bsel(so[2 * i], sd[2 * i], mk[2 * i])), __builtin_amdgcn_exp2f(bsel(so[2 * i + 1], sd[2 * i + 1], mk[2 * i + 1])));
      pd[i] = p[i] & dm[i]; p[i] ^= pd[i];
      if (i == 1) { S[1] = mfma32(k11, q1, S[1]); PINV(S[1]); SB0(); }
      if (i == 3) { O = mfma32(v0, __builtin_bit_cast(s16x8, (u32x4){pd[0], pd[1], pd[2], pd[3]}), z); PINV(O); SB0(); }
      if (i == 5) { O = mfma32(vo0, __builtin_bit_cast(s16x8, (u32x4){p[0], p[1], p[2], p[3]}), O); PINV(O); SB0(); }
    }
    O = mfma32(vo1, __builtin_bit_cast(s16x8, (u32x4){p[4], p[5], p[6], p[7]}), O);
    pend = __builtin_bit_cast(s16x8, (u32x4){pd[4], pd[5], pd[6], pd[7]}); vpend = v1;
  }
  static_for<1, 8>([&](auto ec) __attribute__((always_inline)) {
    constexpr int e = decltype(ec)::v;
    va -= vstep;
    attn_block<(e < 7), (e < 6)>(S[e & 1], S[(e + 1) & 1], O, q0, q1, kn0, kn1, ka + 3072 - e * 512, kb + 3072 - e * 512, va, pend, vpend);
  });
  return mfma32(vpend, pend, O);
}
DI f32x16 attn_short(s16x8 q0, s16x8 q1, u32 ka, u32 kb, u32 va, u32 vstep, const u32 (&dm)[8], int nfull) {
  f32x16 z, O, S0, S1;
#pragma unroll
  for (int i = 0; i < 16; ++i) z[i] = 0.f;
  s16x8 kn0, kn1, pend, vpend;
  {
    const s16x8 kd0 = lds128(ka + 4096), kd1 = lds128(kb + 4096), k10 = lds128(ka + 3584), k11 = lds128(kb + 3584);
    const s16x8 v0 = ldstr(va, va + 128), v1 = ldstr(va + 256, va + 384);
    kn0 = lds128(ka + 3072); kn1 = lds128(kb + 3072);
    SB0();
    const f32x16 sd = mfma32(kd1, q1, mfma32(kd0, q0, z));
    S1 = mfma32(k11, q1, mfma32(k10, q0, z));
    SB0();
    u32 p[8];
#pragma unroll
    for (int i = 0; i < 8; ++i) p[i] = pk(__builtin_amdgcn_exp2f(sd[2 * i]), __builtin_amdgcn_exp2f(sd[2 * i + 1])) & dm[i];
    O = mfma32(v0, __builtin_bit_cast(s16x8, (u32x4){p[0], p[1], p[2], p[3]}), z);
    pend = __builtin_bit_cast(s16x8, (u32x4){p[4], p[5], p[6], p[7]}); vpend = v1;
  }
  NOUNROLL for (int n = nfull; n > 0; n -= 2) {
    va -= vstep;
    attn_block<true, true>(S1, S0, O, q0, q1, kn0, kn1, ka + 2560, kb + 2560, va, pend, vpend);
    if (n == 1) break;
    va -= vstep;
    attn_block<true, true>(S0, S1, O, q0, q1, kn0, kn1, ka + 2048, kb + 2048, va, pend, vpend);
    ka -= 1024; kb -= 1024;
  }
  return mfma32(vpend, pend, O);
}
DI f32x16 attn_shifted(s16x8 q0, s16x8 q1, u32 ka, u32 kb, u32 va, u32 vstep, int nblk, int c, int hh) {
  f32x16 z, O;
#pragma unroll
  for (int i = 0; i < 16; ++i) { z[i] = 0.f; O[i] = 0.f; }
  float m = -__builtin_inff();
  NOUNROLL for (int pass = 0; pass < 2; ++pass) {
    NOUNROLL for (int e = 0; e < nblk; ++e) {
      f32x16 s = mfma32(lds128(kb + 4096 - e * 512), q1, mfma32(lds128(ka + 4096 - e * 512), q0, z));
      int lo = (e == 8 ? c + 1 : 0) - 4 * hh, n = (e == 0 ? c : 31) - 4 * hh - lo;
      PINV(lo); PINV(n);
#pragma unroll
      for (int i = 0; i < 16; ++i) { const int k = (i & 3) + 8 * (i >> 2) - lo; s[i] = (k >= 0 && k <= n) ? s[i] : -__builtin_inff(); }
      if (pass == 0) {
#pragma unroll
        for (int i = 0; i < 16; ++i) m = fmaxf(m, s[i]);
      } else {
        u32 p[8];
#pragma unroll
        for (int i = 0; i < 8; ++i) p[i] = pk(__builtin_amdgcn_exp2f(s[2 * i] - m), __builtin_amdgcn_exp2f(s[2 * i + 1] - m));
        const u32 v = va - e * vstep;
        O = mfma32(ldstr(v, v + 128), __builtin_bit_cast(s16x8, (u32x4){p[0], p[1], p[2], p[3]}), O);
        O = mfma32(ldstr(v + 256, v + 384), __builtin_bit_cast(s16x8, (u32x4){p[4], p[5], p[6], p[7]}), O);
      }
    }
    auto r = __builtin_amdgcn_permlane32_swap(__float_as_uint(m), __float_as_uint(m), false, false);
    m = fmaxf(__uint_as_float(r[0]), __uint_as_float(r[1]));
  }
  return O;
}

DI void row_stats(u32x4 xa, u32 xb, u32x4 wa, u32 wb, char* ws, int srow, u32 tag, int tid) {
  const int lane = tid & 63;
  const u32 xu[5] = {xa[0], xa[1], xa[2], xa[3], xb}, wu5[5] = {wa[0], wa[1], wa[2], wa[3], wb};
  f32x2 v[5], s2 = {0.f, 0.f};
#pragma unroll
  for (int i = 0; i < 5; ++i) { v[i] = (f32x2){blo(xu[i]), bhi(xu[i])}; s2 += v[i]; }
  const float mean = wsum(s2[0] + s2[1]) * (1.f / 640.f), mb = rbf(mean);
  f32x2 q2 = {0.f, 0.f};
  float r = 0.f;
#pragma unroll
  for (int i = 0; i < 5; ++i) {
    const f32x2 d = v[i] - mean, e = v[i] - mb;
    q2 += d * d;
    r = __builtin_amdgcn_fdot2_f32_bf16(asb(pk(e[0], e[1])), asb(wu5[i]), r, false);
  }
  float q = q2[0] + q2[1];
  q += dppf<0xB1>(q); r += dppf<0xB1>(r); q += dppf<0x4E>(q); r += dppf<0x4E>(r);
  q += dppf<0x141>(q); r += dppf<0x141>(r); q += dppf<0x140>(q); r += dppf<0x140>(r);
  { auto t = __builtin_amdgcn_permlane16_swap(__float_as_uint(q), __float_as_uint(r), false, false); q = __uint_as_float(t[0]) + __uint_as_float(t[1]); }
  q = x32sum(q);
  r = __uint_as_float(__builtin_amdgcn_readlane(__float_as_uint(q), 16));
  q = __uint_as_float(__builtin_amdgcn_readlane(__float_as_uint(q), 0));
  const float sd = rbf(__builtin_amdgcn_sqrtf(rbf(rbf(q * (1.f / 640.f)) + 1e-5f)));
  const u32 pay = lane == 0 ? ((__float_as_uint(mb) >> 16) | __float_as_uint(sd)) : __float_as_uint(r * __builtin_amdgcn_rcpf(sd));
  store_flag(ws, (lane == 0 ? WS_STATS : WS_RR) + srow * 8, ((u64)tag << 32) | pay, 3);
  if (tid < 16) *lp<u32>(L_CNT + tid * 4) = 0;
  if (tid < 144) *lp<u32>(L_ONE + tid * 4) = 0x3F803F80u;
}

__global__ void __launch_bounds__(512, 2)
fused_block(const u16* __restrict__ x, const u16* __restrict__ wqkv, const u16* __restrict__ wr, char* ws,
            const u16* wo_, const u16* wg_, const u16* wu_, const u16* wd_, u16* out_) {
  (void)wo_; (void)wg_; (void)wu_; (void)wd_; (void)out_;
  auto karg = [](int i) { const __attribute__((address_space(4))) u64* k = (const __attribute__((address_space(4))) u64*)__builtin_amdgcn_kernarg_segment_ptr(); PINS(k); return (char*)k[i]; };
  extern __shared__ __attribute__((aligned(16))) char smem[];
  (void)smem;
  STAMP(0);
  const int b = blockIdx.x, xc = b & 7, bi = b >> 3;
  const int tid = threadIdx.x, lane = tid & 63, w = uni(tid >> 6);
  const int c = lane & 31, hh = lane >> 5;
  const bool helper = bi >= 30;

  if (w == 7) {
    u64 pc; asm volatile("s_getpc_b64 %0" : "=s"(pc));
    constexpr int PER = (CODE_LINES + 31) / 32;
    u32 ln = bi * PER + (lane % PER); ln = ln < CODE_LINES ? ln : CODE_LINES - 1;
    asm volatile("s_mov_b32 m0, %0\n\ts_nop 0\n\tglobal_load_lds_dword %1, %2" :: "s"(L_PF), "v"(ln * 128), "s"(pc & ~127ull) : "memory");
  }
  const int srow = b * 8 + w;
#define ROW_LOADS() u32x4 xa, wa; u32 xb, wb; \
    asm volatile("s_nop 4\n\tglobal_load_dwordx4 %0, %5, %6\n\tglobal_load_dword %1, %5, %6 offset:16\n\t" \
                 "global_load_dwordx4 %2, %5, %7\n\tglobal_load_dword %3, %5, %7 offset:16\n\ts_load_dwordx2 %4, %8, 0x0" \
                 : "=&v"(xa), "=&v"(xb), "=&v"(wa), "=&v"(wb), "=&s"(tag64) : "v"(lane * 20), "s"(x + srow * 640), "s"(wr), "s"(ws) : "memory")
#define ROW_STATS(n) \
    asm volatile("s_waitcnt vmcnt(" #n ") lgkmcnt(0)" : "+v"(xa), "+v"(xb), "+v"(wa), "+v"(wb), "+s"(tag64) :: "memory"); \
    tag64 = ((u64)uni((u32)((tag64 + 1) >> 32)) << 32) | uni((u32)tag64 + 1); tag = (u32)tag64; \
    row_stats(xa, xb, wa, wb, ws, srow, tag, tid)
  u64 tag64; u32 tag;
  const __amdgpu_buffer_rsrc_t wsr = __builtin_amdgcn_make_buffer_rsrc(ws, 0, WS_SPAN, 0x00020000);
  const int X = w >> 2, wl = w & 3;

  u64 sgr = 0;
#define RES_PREP(bb) u32 xo_[4]; const char* xs_; \
    { const int row_ = 64 * (((bb) & 7) + 8 * ((bb) >> 6)) + 16 * wl; \
      _Pragma("unroll") for (int n_ = 0; n_ < 3; ++n_) { const u32 P_ = 64 * n_ + lane, r_ = P_ / 10u; xo_[n_] = (r_ < 16 ? r_ : 15) * 1280 + (P_ - 10 * r_) * 16; PINV(xo_[n_]); } \
      xo_[3] = X ? WS_STATS + (row_ + (lane & 15)) * 8 : WS_TOP2 + ((bb) & 15) * 128; PINV(xo_[3]); \
      xs_ = karg(0) + (u32)(row_ * 1280 + (((bb) >> 3) & 7) * 160); PINS(xs_); }
#define RES_ISSUE() asm volatile("s_cmp_eq_u32 %8, 0\n\tglobal_load_dwordx2 %0, %1, %2 sc1\n\ts_cbranch_scc1 1f\n\t" \
                 WO_D("%7, 0", "%3", "%6") WO_D("m0, 1024", "%4", "%6") WO_D("m0, 1024", "%5", "%6") "\n1:" \
                 : "=&v"(sgr) : "v"(xo_[3]), "s"(ws), "v"(xo_[0]), "v"(xo_[1]), "v"(xo_[2]), "s"(xs_), "s"(L_XR + wl * 3072), "s"(X) : "memory", "scc")

  u32 ob, cb, pfo, ofo, ofo2, w0a, w0b, w8a, w8b, resv;
  int t16, ct3;
  char* out;
  f32x4 acc[5];
  auto p3_setup = [&]() __attribute__((always_inline)) {
    int b3 = blockIdx.x; PINS(b3);
    const int tk = lane & 15, g = lane >> 4;
    ct3 = (b3 >> 3) & 7; t16 = 4 * ((b3 & 7) + 8 * (b3 >> 6)) + wl;
    const u32 wf_ = (8 * g + ((lane >> 2) & 3)) * 160 + (lane & 3) * 8, wfa = wf_ + (g & 1) * 640, wfb = wf_ + ((g & 1) ^ 1) * 640;
    const u32 wb0 = X == 1 ? L_WOB : L_KVA, wb8 = X == 1 ? L_WOB + 8 * 5120 : L_WOA7 + 5120;
    w0a = wb0 + wfa; w0b = wb0 + wfb; w8a = wb8 + wfa; w8b = wb8 + wfb;
    resv = L_KVB + wl * 5120 + lane * 16;
    ob = WS_O + X * (1u << 20) + t16 * 8192 + lane * 16; cb = WS_C4 + X * (1u << 18) + t16 * 2048 + g * 256 + tk * 8;
    pfo = WS_OFLAG + ((t16 >> 1) * 32 + 16 * X + tk) * 128;
    const u32 ro = (16 * t16 + tk) * 1280 + 160 * ct3;
    ofo = ro + 32 * (g & 1) + 16 * (g >> 1); ofo2 = ro + 128 + 8 * g;
    out = karg(8);
    PINV(ofo); PINV(ofo2); PINV(w0a); PINV(w0b); PINV(w8a); PINV(w8b); PINV(resv); PINV(ob); PINV(cb); PINV(pfo); PINS(out); PINS(t16); PINS(ct3);
    if (X == 1) {
      u32 sg = (u32)sgr;
      if (!__all((u32)(sgr >> 32) == tag)) sg = poll_gran(ws, WS_STATS + (16 * t16 + tk) * 8, tag);
      const float rs = __builtin_amdgcn_rcpf(bhi(sg)), k1 = 1.f + rs, k0 = -blo(sg) * rs;
#pragma unroll
      for (int t = 0; t < 5; ++t) {
        const u32x2 xr = *lp<u32x2>(L_XR + wl * 3072 + tk * 160 + g * 8 + t * 32);
        acc[t] = (f32x4){fmaf(blo(xr[0]), k1, k0), fmaf(bhi(xr[0]), k1, k0), fmaf(blo(xr[1]), k1, k0), fmaf(bhi(xr[1]), k1, k0)};
      }
    } else {
#pragma unroll
      for (int t = 0; t < 5; ++t) acc[t] = (f32x4){0.f, 0.f, 0.f, 0.f};
    }
#pragma unroll
    for (int t = 0; t < 5; ++t) PINV(acc[t]);
  };

  if (!helper) {
    ROW_LOADS();
    const int Lx = (xc >> 1) * 30 + bi, uj = Lx >> 3, rtile = (xc & 1) * 8 + (Lx & 7);
    const int uA = (uj / 5) * 10 + uj % 5;
    const int rot = (rtile * 3 + uj) % 10;
    const u32 vx = (8 * w + (lane >> 3)) * 1280 + (((lane & 7) ^ ((4 * w + (lane >> 4)) & 7)) << 4);
    const u32 vw = (8 * w + (lane >> 3)) * 3840 + (((lane & 7) ^ ((lane >> 2) & 4)) << 4);
    const char* xbase = (const char*)x + rtile * (128 * 1280);
    const char* wbase = (const char*)wqkv + uA * 128;
    auto piece = [&](int op) {
      const int a = op / 3, p = op % 3, pg = op - 30;
      int sl = (op < 30 ? a : pg) + rot; sl = sl >= 10 ? sl - 10 : sl;
      if (op >= 30) dma16(L_BP + (pg == 9 ? 6 : pg % 6) * 8192 + w * 1024, vw, wbase + 640 + sl * (64 * 3840));
      else if (p < 2) dma16((a % 4) * SLOT + p * 8192 + w * 1024, vx, xbase + sl * 128 + p * (64 * 1280));
      else dma16((a % 4) * SLOT + 16384 + w * 1024, vw, wbase + sl * (64 * 3840));
    };
    piece(0); piece(1); piece(2); piece(3); piece(4); piece(5);
    const int tt = wl, fh = X;
    const u32 key = (c >> 1) & 7;
    u32 xad[2][4];
#pragma unroll
    for (int i = 0; i < 4; ++i) { xad[0][i] = (32 * tt + c) * 128 + (((2 * i + hh) ^ key) << 4); xad[1][i] = xad[0][i] + 2 * SLOT; PINV(xad[0][i]); PINV(xad[1][i]); }
    const u32 q4 = (lane >> 2) & 3, p4 = lane & 3, g2 = (lane >> 4) & 1;
    u32 wadA = (8 * hh + q4) * 128 + (((4 * fh + 2 * (p4 & 1) + g2) ^ ((q4 >> 1) << 2)) << 4) + (p4 >> 1) * 8, wadB = wadA + L_BP, wadC = wadA + 2 * SLOT;
    const int gm = bi * 8 + xc, gk = gm / 24, gc = gm - 24 * gk;
    const char* pfa;
    {
      const u32 k0 = (bi * 8 + w) * 14 + (lane < 13 ? lane : 13), k = k0 < 3200 ? k0 : 3199;
      const char *wo = karg(4), *wgu = karg(5 + X);
      pfa = lane < 48 ? wo + (3200 + k) * 128 : wgu + (u32)((64 * gk + 16 * wl + (lane & 15)) * 3072 + gc * 128);
    }
    u32 vg = (8 * w + (lane >> 3)) * 3072 + (((lane & 7) ^ ((lane >> 2) & 4)) << 4);
    u32 goff = WS_STATS + (rtile * 128 + 32 * tt + c) * 8;
    u32 wadT = (32 * X + 8 * (lane >> 4) + q4) * 128 + (((2 * wl + (p4 >> 1)) ^ ((q4 >> 1) << 2)) << 4) + (p4 & 1) * 8;
    u32 wadV = wadT + L_BP; wadT += 16384;
    u32 wadU = wadT + 2 * SLOT;
    u32 qoff = WS_QKV + (8 * uA + 4 * fh + 2 * hh) * 32768 + (rtile * 128 + 32 * tt + c) * 16;
    u32 csl = L_CP + (32 * fh + c) * 4, cswr = L_CP + X * 256 + (16 * wl + (lane & 15)) * 4;
    u32 csm = L_CP + 1024 + w * 128 + c * 4, csr = L_CP + 1024 + w * 128 + hh * 64;
    u32x4 onev = {0x3F803F80u, 0x3F803F80u, 0x3F803F80u, 0x3F803F80u};
    PINV(wadA); PINV(wadB); PINV(wadC); PINV(wadT); PINV(wadU); PINV(wadV); PINV(pfa); PINV(vg); PINV(goff); PINV(qoff); PINV(csl); PINV(cswr); PINV(csm); PINV(csr); PINV(onev);
    u32 wv[8];
    wo_offsets(wv, wl, lane);
    ROW_STATS(6);
    if ((P1X & 8) && X == 1) __builtin_amdgcn_s_setprio(1);
    SB0();
    s16x8 xf[40], wf[2][4], wt[8], cf, cg;
    const s16x8 ones = __builtin_bit_cast(s16x8, onev);
    f32x16 aca, acb;
    f32x4 acs = {0.f, 0.f, 0.f, 0.f}, act = acs;
#pragma unroll
    for (int i = 0; i < 16; ++i) { aca[i] = 0.f; acb[i] = 0.f; }
    u64 gr = 0;
    u32x4 xn[4];
    u32 xno = WS_XN + (b & 15) * 5120 + (lane & 1) * 2560 + (32 * gk + 4 * (lane >> 4)) * 8; PINV(xno);
#define XN_TXT "global_load_dwordx4 %0, %4, %5 sc1\n\tglobal_load_dwordx4 %1, %4, %5 offset:16 sc1\n\tglobal_load_dwordx4 %2, %4, %5 offset:128 sc1\n\tglobal_load_dwordx4 %3, %4, %5 offset:144 sc1"
    const char* gsrc[2] = {karg(5) + (u32)(64 * gk * 3072 + gc * 128), karg(6) + (u32)(64 * gk * 3072 + gc * 128)};
    float mneg = 0.f, rsq = 0.f;
    u32 qscb = uA < 10 ? 0x3ea52b5cu : 0x3f800000u;
    PINS(qscb);
    u32 eo[8];
    f32x4 cp[4];
    float c0 = 0.f, c1 = 0.f;
    auto epi2 = [&](float a0, float a1, float k0, float k1) {
      u32 r; float t;
      asm("v_fma_f32 %0, %2, %4, %6\n\tv_fma_f32 %1, %2, %5, %7\n\tv_mul_f32 %0, %3, %0\n\tv_mul_f32 %1, %3, %1\n\tv_cvt_pk_bf16_f32 %0, %0, %1"
          : "=&v"(r), "=&v"(t) : "v"(mneg), "v"(rsq), "v"(k0), "v"(k1), "v"(a0), "v"(a1));
      return r;
    };
    auto epi_store = [&](int second, int k) {
      __builtin_amdgcn_raw_buffer_store_b128((u32x4){eo[4 * k], eo[4 * k + 1], eo[4 * k + 2], eo[4 * k + 3]}, wsr, qoff + k * 32768 + second * (40 * 32768), 0, 16);
    };
    static_for<0, 21>([&](auto uc) __attribute__((always_inline)) {
      constexpr int u = decltype(uc)::v;
      constexpr int sa = slice_of(u), sb = page_of(u), pa = slice_of(u - 1), pb = page_of(u - 1);
      constexpr int G = u < 7 ? u : u < 9 ? -1 : u < 14 ? u - 2 : u == 16 ? 12 : u == 19 ? 13 : -1;
      if constexpr (u < 20 && need_op(u) >= 0) WAITBARV(younger(need_op(u), u));
      if (u == 7) asm volatile("s_barrier" ::: "memory");
      if (u == 15) asm volatile("s_waitcnt lgkmcnt(6)\n\ts_barrier" ::: "memory");
      if (u == 19) asm volatile("s_waitcnt lgkmcnt(4)\n\ts_barrier" ::: "memory");
      if (G == 11) {
        PINV(gr);
        u32 pay = (u32)gr;
        if (!P1X && !__all((u32)(gr >> 32) == tag)) pay = poll_gran(ws, goff, tag);
        mneg = -blo(pay); rsq = __builtin_amdgcn_rcpf(bhi(pay)) * __uint_as_float(qscb); PINV(mneg); PINV(rsq);
      }
      if (u >= UOFF && u < UOFF + 16) STAMP(16 + (u >= UOFF && u < UOFF + 16 ? u - UOFF : 0));
      SB0();
#pragma unroll
      for (int i = 0; i < 4; ++i) {
        if constexpr (u > 0) {
          if constexpr (pa >= 0) aca = mfma32(wf[(u - 1) & 1][i], xf[4 * pa + i], aca);
          else acb = mfma32(u > 13 ? wt[(4 * (u - 14) + i) & 7] : wf[(u - 1) & 1][i], xf[4 * pb + i], acb);
        }
        SB0();
        if constexpr (u < 20) {
          if constexpr (sa >= 0) {
            const u32 wbs = (sa & 2 ? wadC : wadA) + (sa & 1) * SLOT + 16384 + i * 2048;
            xf[4 * sa + i] = lds128(xad[(sa >> 1) & 1][i] + (sa & 1) * SLOT); wf[u & 1][i] = ldstr(wbs, wbs + 512);
          } else if constexpr (u < 13) { const u32 pbase = wadB + sb * 8192 + i * 2048; wf[u & 1][i] = ldstr(pbase, pbase + 512); }
          else {
            auto rd = [&](int f) { const int pn = 3 + f / 4; const u32 pbase = wadB + (pn == 9 ? 6 : pn % 6) * 8192 + (f & 3) * 2048; wt[f & 7] = ldstr(pbase, pbase + 512); };
            if constexpr (u == 13) { rd(i == 0 ? 0 : i == 1 ? 2 : i == 2 ? 3 : 5); if (!(i & 1)) rd(i == 0 ? 1 : 4); }
            else if (4 * (u - 14) + i + 6 < 28) rd(4 * (u - 14) + i + 6);
          }
        }
        if (i == 3) {
          if constexpr (u > 0 && pa >= 0) acs = mfma16(ones, cf, acs);
          if constexpr ((u >= 7 && u <= 9) || (u >= 14 && u <= 17)) act = mfma16(ones, cf, act);
          if constexpr (sa >= 0) { const u32 cb = (sa & 2 ? wadU : wadT) + (sa & 1) * SLOT; cf = ldstr(cb, cb + 512); }
          if constexpr (u >= 6 && u <= 8) cf = ldstr(wadV + sb * 8192, wadV + sb * 8192 + 512);
          if constexpr (u >= 13 && u <= 16) { constexpr int pn = 3 + 2 * (u - 13); const u32 cb = wadV + (pn == 9 ? 6 : pn % 6) * 8192; cf = ldstr(cb, cb + 512); }
        }
        if (i == 1) {
          if constexpr (u >= 14 && u <= 16) act = mfma16(ones, cg, act);
          if constexpr (u >= 13 && u <= 15) { constexpr int pn = 4 + 2 * (u - 13); const u32 cb = wadV + (pn % 6) * 8192; cg = ldstr(cb, cb + 512); }
        }
        const int op = sched_op(u, i);
        if (op >= 0 && op < 40) piece(op);
        if (op == OP_GR) asm volatile("global_load_dwordx2 %0, %1, %2 sc1" : "=v"(gr) : "v"(goff), "s"(ws) : "memory");
        if (op == OP_PF) asm volatile("s_mov_b32 m0, %0\n\ts_nop 0\n\tglobal_load_lds_dword %1, off" :: "s"(L_PF), "v"(pfa) : "memory");
        if (u == 19 && i < 2) dma16(L_G + i * 8192 + w * 1024, vg, gsrc[i]);
        if (u == 18 && i == 0) asm volatile("s_cmp_lg_u32 %6, 0\n\ts_cbranch_scc1 1f\n\t" XN_TXT "\n1:" : "=&v"(xn[0]), "=&v"(xn[1]), "=&v"(xn[2]), "=&v"(xn[3]) : "v"(xno), "s"(ws), "s"(X) : "memory", "scc");
        if constexpr (u == 14 || u == 18) { if (i == 1) *lp<float>(cswr + (u == 14 ? 0 : 512)) = u == 14 ? acs[0] : act[0]; }
        if constexpr (u == 15 || u == 19) {
          constexpr u32 o = u == 15 ? 0 : 512;
          if (i == 0) { c0 = *lp<float>(csl + o); c1 = *lp<float>(csl + o + 256); }
          if (i == 2) *lp<float>(csm) = c0 + c1;
          if (i == 3) {
#pragma unroll
            for (int k = 0; k < 4; ++k) cp[k] = *lp<f32x4>(csr + 16 * k);
          }
        }
        if constexpr (u == 16 || u == 17) {
          constexpr int q = 2 * (u - 16);
          eo[2 * q + i] = epi2(aca[4 * q + 2 * i], aca[4 * q + 2 * i + 1], cp[q + (i >> 1)][2 * (i & 1)], cp[q + (i >> 1)][2 * (i & 1) + 1]);
          if (i == 3) epi_store(0, u - 16);
        }
        SB0();
      }
    });
    STAMP(4);
    u32 fl = WS_QFLAG + (rtile * 30 + uA) * 128; u64 tagv = tag64;
    PINV(fl); PINV(tagv);
#pragma unroll
    for (int i = 0; i < 4; ++i) {
      eo[2 * i]     = pk(fmaf(mneg, cp[i][0], acb[4 * i]) * rsq, fmaf(mneg, cp[i][1], acb[4 * i + 1]) * rsq);
      eo[2 * i + 1] = pk(fmaf(mneg, cp[i][2], acb[4 * i + 2]) * rsq, fmaf(mneg, cp[i][3], acb[4 * i + 3]) * rsq);
      if (i & 1) epi_store(1, i >> 1);
    }
    SB0();
    STAMP(3);
    asm volatile("s_waitcnt vmcnt(2) lgkmcnt(0)\n\ts_barrier" : "+v"(xn[0]), "+v"(xn[1]), "+v"(xn[2]), "+v"(xn[3]) :: "memory");
    if (w == 4) store_flag(ws, fl, tagv, 1);
    const char* woc = karg(4) + (bi & 7) * 160; PINS(woc);
    if (X == 1) stage_wo<0>(woc, 1, wl, wv);
    else {
      NOUNROLL for (u32 sp = 0; sp < SPIN_CAP; ++sp) {
        u32 bad = 0;
#pragma unroll
        for (int i = 0; i < 4; ++i) bad |= (xn[i][1] ^ tag) | (xn[i][3] ^ tag);
        if (__all(bad == 0)) break;
        asm volatile("s_sleep 2\n\t" XN_TXT "\n\ts_waitcnt vmcnt(0)" : "=&v"(xn[0]), "=&v"(xn[1]), "=&v"(xn[2]), "=&v"(xn[3]) : "v"(xno), "s"(ws) : "memory");
      }
      const u32 ga = L_G + (8 * (lane >> 4) + q4) * 128 + (((2 * wl + (p4 >> 1)) ^ ((q4 >> 1) << 2)) << 4) + (p4 & 1) * 8;
      const u32 go = (lane & 14) ? 0x80000000u : WS_GP + gk * 49152 + (lane & 1) * 12288 + (64 * gc + 16 * wl + 4 * (lane >> 4)) * 8;
#pragma unroll
      for (int m = 0; m < 2; ++m) {
        f32x4 y = {0.f, 0.f, 0.f, 0.f};
#pragma unroll
        for (int ks = 0; ks < 2; ++ks)
          y = mfma16(ldstr(ga + m * 8192 + ks * 4096, ga + m * 8192 + ks * 4096 + 512), __builtin_bit_cast(s16x8, (u32x4){xn[2 * ks][0], xn[2 * ks][2], xn[2 * ks + 1][0], xn[2 * ks + 1][2]}), y);
        __builtin_amdgcn_raw_buffer_store_b128((u32x4){__float_as_uint(y[0]), tag, __float_as_uint(y[1]), tag}, wsr, go + m * 24576, 0, 16);
        __builtin_amdgcn_raw_buffer_store_b128((u32x4){__float_as_uint(y[2]), tag, __float_as_uint(y[3]), tag}, wsr, go + m * 24576 + 16, 0, 16);
      }
      store_flag(ws, WS_GF + (gk * 96 + 4 * gc + wl) * 128, tagv, 1);
      const u32 l = (bi * 4 + wl) * 27 + (lane < 26 ? lane : 26);
      asm volatile("s_mov_b32 m0, %0\n\ts_nop 4\n\tglobal_load_lds_dword %1, %2" :: "s"(L_PF), "v"((l < 3200 ? l : 3199) * 128), "s"(woc - (bi & 7) * 160) : "memory");
    }
    {
    int b2 = blockIdx.x; PINS(b2);
    const int xc = b2 & 7, bi = b2 >> 3;
    const int odd = bi >= 15, hl = 2 * xc + odd, grp = bi - 15 * odd, head = 16 * X + hl;
    const int oc0 = 5 * (8 * X + xc);
    const int oA = oc0 + 3 * odd, oG = oc0 + 2;
    const u32 kv = X == 0 ? L_KVA : L_KVB;
    const int g0 = grp == 0;
    u32 pollo;
    {
      const int l4 = lane & 15, isq = (l4 + 4) >> 4, b1 = (l4 >> 1) & 1, ri = l4 >> 2;
      const int oct = oc0 + 2 * odd + 2 * (l4 & 1) + (80 + 80 * b1) * (1 - isq);
      const int rt = g0 ? (isq ? b1 : (ri + 1) >> 1) : grp - 1 + ri - isq;
      pollo = WS_QFLAG + (rt * 30 + (oct >> 3)) * 128;
    }
    const int t0 = g0 ? wl : 4 * (grp + 1) + wl, t1 = g0 ? 7 - wl : t0;
    u32 qo0 = WS_QKV + (oA + hh) * 32768 + c * 16 + t0 * 512, qo1 = WS_QKV + oG * 32768 + c * 16 + t0 * 512, l16 = lane * 16;
    u32 qd = (t1 - t0) * 512;
    const int tp0 = (3 - g0) * (wl >> 1);
    const char* kvs = ws + (u32)(WS_QKV + 80 * (1 + (wl & 1)) * 32768 + ((g0 ? 0 : 128 * (grp - 1)) + 64 * tp0) * 16);
    const char* sA = kvs + (u32)(oA * 32768); const char* sB = sA + 32768; const char* sG = kvs + (u32)(oG * 32768);
    u32 mA = kv + 3 * (wl & 1) * PLANE + tp0 * 1024;
    RES_PREP(b2);
    fl += 5 * 128;
    PINV(pollo); PINV(qo0); PINV(qo1); PINV(l16); PINV(fl); PINS(sA); PINS(sB); PINS(sG); PINS(mA); PINS(qd);
    WAITBAR(6);
    if (w == 1) store_flag(ws, fl, tagv, 1);
    STAMP(5);
    RES_ISSUE();
    u32 mk[16], dm[8], hmask, kml, kmh, vlane, vstep, vold, kla, klb, oo, co4, ofl;
    auto att_setup = [&]() __attribute__((always_inline)) {
      int d = 4 * hh - 1 - c;
#pragma unroll
      for (int i = 0; i < 16; ++i) asm("v_add_u32 %0, %2, %1\n\tv_ashrrev_i32 %0, 31, %0" : "=&v"(mk[i]) : "v"(d), "i"((i & 3) + 8 * (i >> 2)));
#pragma unroll
      for (int i = 0; i < 8; ++i) { dm[i] = (mk[2 * i] & 0xffffu) | (mk[2 * i + 1] & 0xffff0000u); PINV(dm[i]); }
      hmask = (u32)-hh; kml = (u32)((hh | odd) - 1); kmh = (u32)((hh | (odd ^ 1)) - 1);
      const u32 realv = (u32)(((g2 & ((p4 | (p4 >> 1)) & 1))) - 1);
      vlane = hh * 64 + q4 * 16 + ((realv & (kv + (3 + 2 * g2 + (p4 & 1)) * PLANE + (g2 * odd + (p4 >> 1)) * 8)) | (~realv & L_ONE));
      vstep = realv & 512u;
      const int dl0 = g0 ? 0 : 8 + wl;
      vold = vlane + (realv & (u32)(wl * 512));
      vlane += realv & (u32)(dl0 * 512);
      kla = kv + hh * PLANE + c * 16 + (dl0 - 8) * 512; klb = kv + 2 * PLANE + c * 16 + (dl0 - 8) * 512;
      oo = WS_O + X * (1u << 20) + t0 * 16384 + (c >> 4) * 8192 + xc * 1024 + (2 * odd + hh) * 256 + (c & 15) * 16;
      co4 = (WS_C4 + X * (1u << 18) + t0 * 4096 + (c >> 4) * 2048 + hl * 128 + (c & 15) * 8) | (hmask & 0x80000000u);
      ofl = WS_OFLAG + (t0 * 32 + head) * 128 + (lane & 1) * (qd * 8);
      PINV(hmask); PINV(kml); PINV(kmh); PINV(vlane); PINV(vstep); PINV(vold); PINV(kla); PINV(klb); PINV(oo); PINV(co4); PINV(ofl);
    };
    if (X == 1) { att_setup(); asm volatile("s_waitcnt vmcnt(0)" : "+v"(sgr) :: "memory"); p3_setup(); }
    __builtin_amdgcn_s_setprio(1);
    u32x4 qa, qb, qa2, qb2;
    {
      u64 pt; u32 pn;
      asm volatile(POLL_TXT("%4", "%5", "%6", "%7", "%8")
                   "s_mov_b32 m0, %9\n\tglobal_load_dwordx4 %0, %14, %7 sc1\n\tglobal_load_lds_dwordx4 %13, %10 sc1\n\tglobal_load_lds_dwordx4 %13, %10 offset:1024 sc1\n\t"
                   "s_add_u32 m0, %9, 6208\n\tglobal_load_dwordx4 %1, %15, %7 sc1\n\tglobal_load_lds_dwordx4 %13, %11 sc1\n\tglobal_load_lds_dwordx4 %13, %11 offset:1024 sc1\n\t"
                   "s_add_u32 m0, %9, 12416\n\ts_cmp_lg_u32 %16, 0\n\tglobal_load_lds_dwordx4 %13, %12 sc1\n\tglobal_load_lds_dwordx4 %13, %12 offset:1024 sc1\n\t"
                   "s_cbranch_scc1 3f\n\t"
                   "global_load_lds_dwordx4 %13, %12 offset:2048 sc1\n\ts_mov_b32 m0, %9\n\ts_nop 0\n\tglobal_load_lds_dwordx4 %13, %10 offset:2048 sc1\n\t"
                   "s_add_u32 m0, %9, 6208\n\ts_nop 0\n\tglobal_load_lds_dwordx4 %13, %11 offset:2048 sc1\n\ts_branch 4f\n"
                   "3:\n\tv_add_u32 %14, %16, %14\n\tv_add_u32 %15, %16, %15\n\tglobal_load_dwordx4 %2, %14, %7 sc1\n\tglobal_load_dwordx4 %3, %15, %7 sc1\n"
                   "4:"
                   : "=&v"(qa), "=&v"(qb), "=&v"(qa2), "=&v"(qb2), "=&v"(pt), "=&s"(pn), "+v"(pollo), "+s"(ws), "+s"(tag64), "+s"(mA), "+s"(sA), "+s"(sB), "+s"(sG),
                     "+v"(l16), "+v"(qo0), "+v"(qo1), "+s"(qd)
                   :: "memory", "scc", "vcc");
    }
    if (X == 0) { stage_wo<1>(woc, 1, wl, wv); att_setup(); PINV(sgr); p3_setup(); }
    u32 cnta = L_CNT + 4 * X, one = 1, tmpv, tmps;
    asm volatile("s_waitcnt vmcnt(7)\n\ts_cmp_eq_u32 %8, 0\n\ts_cbranch_scc1 6f\n\ts_waitcnt vmcnt(0)\n6:\n\ts_mov_b64 exec, 1\n\tds_add_u32 %2, %3\n\ts_mov_b64 exec, -1\n"
                 "5:\n\tds_read_b32 %0, %2\n\ts_waitcnt lgkmcnt(0)\n\tv_readfirstlane_b32 %1, %0\n\ts_cmp_ge_u32 %1, 4\n\ts_cbranch_scc0 5b"
                 : "=&v"(tmpv), "=&s"(tmps), "+v"(cnta), "+v"(one), "+v"(qa), "+v"(qb), "+v"(qa2), "+v"(qb2)
                 : "s"(X) : "memory", "scc");
    STAMP(7);
    {
      auto in_range = [&](const f32x16& O) {
        const float l = bsel(O[12], O[8], hmask);
        const float t = ((O[0] + O[1]) + (O[2] + O[3])) + ((O[4] + O[5]) + (O[6] + O[7])) + ((O[8] + O[9]) + (O[10] + O[11]));
        return __all(l > 0x1p-40f && l < 0x1p40f && __builtin_fabsf(t) < __builtin_inff());
      };
      auto put = [&](const f32x16& O, u32 ao, u32 ac) {
        const float inv = __builtin_amdgcn_rcpf(bsel(O[12], O[8], hmask));
        __builtin_amdgcn_raw_buffer_store_b128((u32x4){pk(O[0] * inv, O[1] * inv), pk(O[2] * inv, O[3] * inv), pk(O[4] * inv, O[5] * inv), pk(O[6] * inv, O[7] * inv)}, wsr, ao, 0, 16);
        __builtin_amdgcn_raw_buffer_store_b64((u32x2){pk(O[8] * inv, O[9] * inv), pk(O[10] * inv, O[11] * inv)}, wsr, ac, 0, 16);
      };
      u32 todo = ALWAYS_MAX ? (g0 ? 3u : 1u) : 0u;
      const s16x8 q0 = __builtin_bit_cast(s16x8, qa), q1 = __builtin_bit_cast(s16x8, (u32x4){qb[0] & kml, qb[1] & kml, qb[2] & kmh, qb[3] & kmh});
      if (!ALWAYS_MAX) {
        if (!g0) {
          const f32x16 O = attn_full(q0, q1, kla, klb, vlane, vold, vstep, dm, mk);
          STAMP(12);
          if (in_range(O)) put(O, oo, co4); else todo = 1;
        } else {
          s16x8 r0 = q0, r1 = q1;
          NOUNROLL for (int it = 0; it < 2; ++it) {
            const int tq = it ? t1 : t0;
            const f32x16 O = attn_short(r0, r1, kla + tq * 512, klb + tq * 512, vlane + vstep * tq, vstep, dm, tq);
            if (in_range(O)) put(O, oo + it * (qd * 32), co4 + it * (qd * 8)); else todo |= 1u << it;
            r0 = __builtin_bit_cast(s16x8, qa2); r1 = __builtin_bit_cast(s16x8, (u32x4){qb2[0] & kml, qb2[1] & kml, qb2[2] & kmh, qb2[3] & kmh});
          }
        }
      }
      if (todo) {
        int cc = c, hc = hh; PINV(cc); PINV(hc);
        NOUNROLL for (int it = 0; it < 2; ++it) {
          if (!((todo >> it) & 1)) continue;
          const int tq = g0 ? (it ? t1 : t0) : 0;
          const u32x4 ra = it ? qa2 : qa, rb = it ? qb2 : qb;
          const f32x16 O = attn_shifted(__builtin_bit_cast(s16x8, ra), __builtin_bit_cast(s16x8, (u32x4){rb[0] & kml, rb[1] & kml, rb[2] & kmh, rb[3] & kmh}),
                                        kla + tq * 512, klb + tq * 512, vlane + vstep * tq, vstep, g0 ? tq + 1 : 9, cc, hc);
          put(O, oo + it * (qd * 32), co4 + it * (qd * 8));
        }
      }
    }
    STAMP(8);
    WAITV(0);
    store_flag(ws, ofl, tagv, g0 ? 3 : 1);
    lds_arrive(X == 0 ? L_CNT + 8 : L_CNT + 20);
    STAMP(9);
    if (X == 0) {
      __builtin_amdgcn_s_setprio(0);
      lds_wait(L_CNT + 8, 4);
      stage_wo<2>(woc, 0, wl, wv);
      __builtin_amdgcn_s_setprio(1);
    }
    }
  } else {
    ROW_LOADS();
    const u64 hstart = __builtin_amdgcn_s_memrealtime();
    const int hg = xc * 2 + (bi - 30);
    const char* wd = karg(7) + hg * (96 * 1280);
    const int rg = lane >> 4;
    {
      u32 r = (w * 64 + lane) / 80u, p = w * 64 + lane - 80 * r;
#pragma unroll
      for (int k = 0; k < 15; ++k) {
        dma16((k * 8 + w) * 1024, r * 1280 + ((p ^ (2 * (r & 3) + (r & 8))) << 4), wd);
        p += 32; const u32 cy = p >= 80; p -= 80 * cy; r += 6 + cy;
        if (k == 3) { ROW_STATS(4); }
      }
    }
    auto mkey = [](u32 f, u32 row) { return ((f ^ (((int)f >> 31) | 0x80000000u)) & 0xfffff800u) | (2047u - row); };
    auto mx = [](u32 p, u32 q) { return p > q ? p : q; };
    auto mn = [](u32 p, u32 q) { return p < q ? p : q; };
    u32 k1, k2;
#define TOP2_STEP(o1, o2) do { const u32 p_ = (o1), q_ = (o2); k2 = mx(mn(k1, p_), mx(k2, q_)); k1 = mx(k1, p_); } while (0)
#define TOP2_DPP(C) TOP2_STEP(__builtin_amdgcn_mov_dpp(k1, C, 0xf, 0xf, true), __builtin_amdgcn_mov_dpp(k2, C, 0xf, 0xf, true))
    NOUNROLL for (int sp = 0; sp < 64 && __builtin_amdgcn_s_memrealtime() - hstart < RR_DELAY; ++sp) __builtin_amdgcn_s_sleep(4);
    {
      u32x4 ra, rb;
      const u32 roff = WS_RR + tid * 32;
      NOUNROLL for (u32 sp = 0; sp < SPIN_CAP; ++sp) {
        asm volatile("s_nop 4\n\tglobal_load_dwordx4 %0, %2, %3 sc1\n\tglobal_load_dwordx4 %1, %2, %3 offset:16 sc1\n\ts_waitcnt vmcnt(0)" : "=&v"(ra), "=&v"(rb) : "v"(roff), "s"(ws) : "memory");
        if (__all(ra[1] == tag && ra[3] == tag && rb[1] == tag && rb[3] == tag)) break;
      }
      const u32 e0 = mkey(ra[0], 4 * tid), e1 = mkey(ra[2], 4 * tid + 1), e2 = mkey(rb[0], 4 * tid + 2), e3 = mkey(rb[2], 4 * tid + 3);
      k1 = mx(e0, e1); k2 = mn(e0, e1);
      TOP2_STEP(mx(e2, e3), mn(e2, e3));
    }
    TOP2_DPP(0xB1); TOP2_DPP(0x4E); TOP2_DPP(0x141); TOP2_DPP(0x140);
    LAS u32x2* kk = lp<u32x2>(L_CP);
    if ((lane & 15) == 0) kk[4 * w + rg] = (u32x2){k1, k2};
    BAR();
    { const u32x2 e = kk[lane & 15], f = kk[16 + (lane & 15)]; k1 = e[0]; k2 = e[1]; TOP2_STEP(f[0], f[1]); }
    TOP2_DPP(0xB1); TOP2_DPP(0x4E); TOP2_DPP(0x141); TOP2_DPP(0x140);
    k1 = uni(k1); k2 = uni(k2);
    STAMP(3);
    const u32 i0 = 2047u - (k1 & 2047u), i1 = 2047u - (k2 & 2047u);
    if (w < 2) {
      const u32 row = w ? i1 : i0;
      u32 xrow[5];
#pragma unroll
      for (int i = 0; i < 5; ++i) xrow[i] = ((const u32*)x)[row * 320 + i * 64 + lane];
      const u32 sg = poll_gran(ws, WS_STATS + row * 8, tag);
      const float mb = blo(sg), rs = __builtin_amdgcn_rcpf(bhi(sg));
#pragma unroll
      for (int i = 0; i < 5; ++i) {
        const u32 t = pk(blo(xrow[i]) - mb, bhi(xrow[i]) - mb);
        __builtin_amdgcn_raw_buffer_store_b64((u32x2){pk(blo(t) * rs, bhi(t) * rs), tag}, wsr, WS_XN + hg * 5120 + w * 2560 + (i * 64 + lane) * 8, 0, 16);
      }
      if (tid == 0) { gst64(ws, WS_TOP2 + hg * 128, ((u64)tag << 32) | i0 | (i1 << 16)); if (hg == 15) gst64(ws, WS_EPOCH, tag64); }
    }
    STAMP(4);
    WAITBAR(0);
    s16x8 df[5][3];
    {
      const u32 q = (lane >> 2) & 3, key = 2 * q + 8 * (rg & 1);
#pragma unroll
      for (int t = 0; t < 5; ++t) {
        const u32 ad = (8 * rg + q) * 1280 + (((10 * w + 2 * t + ((lane >> 1) & 1)) ^ key) << 4) + (lane & 1) * 8;
#pragma unroll
        for (int ks = 0; ks < 3; ++ks) { df[t][ks] = ldstr(ad + ks * 40960, ad + ks * 40960 + 5120); PINV(df[t][ks]); }
      }
    }
    BAR();
    STAMP(2);
    u32 wv[8];
    wo_offsets(wv, wl, lane);
    RES_PREP(b);
    RES_ISSUE();
    stage_wo<2>(karg(4) + (bi & 7) * 160, X, wl, wv);
    asm volatile("s_waitcnt vmcnt(13)" : "+v"(sgr) :: "memory");
    p3_setup();
    sgr = ((u64)tag << 32) | i0 | (i1 << 16);
    STAMP(5);
    const u32 kq = ((lane & 1) ? k2 : k1) & 0xfffff800u;
    float gate = rbf(__builtin_amdgcn_rcpf(1.f + __expf(-rbf(__uint_as_float(kq ^ ((kq >> 31) ? 0x80000000u : ~0u))))));
    u32 yo = ((lane & 14) ? 0x80000000u : WS_Y + hg * 10240 + (lane & 1) * 5120 + (80 * w + 4 * rg) * 8);
    const u32 tc = 32 * (w < 6 ? w : 5) + (lane >> 1), gtok = tc >= 96;
    u32 gpo = WS_GP + (lane & 1) * 24576 + gtok * 12288 + (96 * hg + tc - 96 * gtok) * 8;
    const int gi = lane < 60 ? lane : 59;
    u32 gfo = WS_GF + ((gi / 6) * 96 + 6 * hg + gi % 6) * 128;
    PINV(gate); PINV(yo); PINV(gpo); PINV(gfo);
    WAITV(0);
    poll_flags(ws, gfo, tag64);
    STAMP(6);
    if (w < 6) {
      float sum;
      NOUNROLL for (u32 sp = 0; sp < SPIN_CAP; ++sp) {
        u64 gv[10];
#define GP_LD(i) "global_load_dwordx2 %" #i ", %10, %11 sc1\n\tv_add_u32 %10, 0xc000, %10\n\t"
        u32 o = gpo;
        asm volatile(GP_LD(0) GP_LD(1) GP_LD(2) GP_LD(3) GP_LD(4) GP_LD(5) GP_LD(6) GP_LD(7) GP_LD(8) GP_LD(9) "s_waitcnt vmcnt(0)"
                     : "=&v"(gv[0]), "=&v"(gv[1]), "=&v"(gv[2]), "=&v"(gv[3]), "=&v"(gv[4]), "=&v"(gv[5]), "=&v"(gv[6]), "=&v"(gv[7]), "=&v"(gv[8]), "=&v"(gv[9]), "+v"(o) : "s"(ws) : "memory");
        u32 bad = 0; sum = 0.f;
#pragma unroll
        for (int k = 0; k < 10; ++k) { bad |= (u32)(gv[k] >> 32) ^ tag; sum += __uint_as_float((u32)gv[k]); }
        if (__all(bad == 0)) break;
      }
      const float g = rbf(sum), u = dppf<0xB1>(g);
      const float hval = rbf(g * __builtin_amdgcn_rcpf(1.f + __expf(-g))) * u;
      if (!(lane & 1)) *lp<u16>(L_CP + 1536 + tc * 2) = (u16)pk(hval, hval);
    }
    BAR();
    STAMP(7);
    {
      s16x8 hf[3];
#pragma unroll
      for (int ks = 0; ks < 3; ++ks) hf[ks] = lds128(L_CP + 1536 + (lane & 1) * 192 + rg * 16 + ks * 64);
      f32x4 y[5];
#pragma unroll
      for (int t = 0; t < 5; ++t) y[t] = (f32x4){0.f, 0.f, 0.f, 0.f};
#pragma unroll
      for (int ks = 0; ks < 3; ++ks)
#pragma unroll
        for (int t = 0; t < 5; ++t) y[t] = mfma16(df[t][ks], hf[ks], y[t]);
#pragma unroll
      for (int t = 0; t < 5; ++t) {
        __builtin_amdgcn_raw_buffer_store_b128((u32x4){__float_as_uint(y[t][0] * gate), tag, __float_as_uint(y[t][1] * gate), tag}, wsr, yo + t * 128, 0, 16);
        __builtin_amdgcn_raw_buffer_store_b128((u32x4){__float_as_uint(y[t][2] * gate), tag, __float_as_uint(y[t][3] * gate), tag}, wsr, yo + t * 128 + 16, 0, 16);
      }
    }
    STAMP(8);
    lds_arrive(X == 0 ? L_CNT + 8 : L_CNT + 20);
    STAMP(9);
  }

  {
    const int tk = lane & 15, g = lane >> 4;
    STAMP(1);
    if (X == 0) { WAITV(0); lds_arrive(L_CNT + 12); }
    lds_wait(X == 0 ? L_CNT + 12 : L_CNT + 8, 4);
    STAMP(2);
    s16x8 wq[8][5];
#pragma unroll
    for (int ks = 0; ks < 8; ++ks)
#pragma unroll
      for (int t = 0; t < 5; ++t) wq[ks][t] = ldstr((ks < 7 ? w0a + ks * 5120 : w8a - 5120) + t * 32, (ks < 7 ? w0b + ks * 5120 : w8b - 5120) + t * 32);
    u32 ob2 = ob + 4096;
    STAMP(10);
    u32x4 of[10];
    {
      u64 pt; u32 pn;
      asm volatile(POLL_TXT("%10", "%11", "%12", "%13", "%14")
                   "global_load_dwordx4 %0, %15, %13 sc1\n\tglobal_load_dwordx4 %1, %15, %13 offset:1024 sc1\n\tglobal_load_dwordx4 %2, %15, %13 offset:2048 sc1\n\t"
                   "global_load_dwordx4 %3, %15, %13 offset:3072 sc1\n\tglobal_load_dwordx4 %4, %16, %13 sc1\n\tglobal_load_dwordx4 %5, %16, %13 offset:1024 sc1\n\t"
                   "global_load_dwordx4 %6, %16, %13 offset:2048 sc1\n\tglobal_load_dwordx4 %7, %16, %13 offset:3072 sc1\n\t"
                   "global_load_dwordx2 v[240:241], %17, %13 sc1\n\tglobal_load_dwordx2 v[242:243], %17, %13 offset:128 sc1\n\t"
                   "global_load_dwordx2 v[244:245], %17, %13 offset:1024 sc1\n\tglobal_load_dwordx2 v[246:247], %17, %13 offset:1152 sc1\n\t"
                   : "=&v"(of[0]), "=&v"(of[1]), "=&v"(of[2]), "=&v"(of[3]), "=&v"(of[4]), "=&v"(of[5]), "=&v"(of[6]), "=&v"(of[7]),
                     "=&{v[240:243]}"(of[8]), "=&{v[244:247]}"(of[9]), "=&v"(pt), "=&s"(pn), "+v"(pfo), "+s"(ws), "+s"(tag64), "+v"(ob), "+v"(ob2), "+v"(cb)
                   :: "memory", "scc", "vcc");
    }
    s16x8 w8[2][5];
#pragma unroll
    for (int ks = 0; ks < 10; ++ks) {
      if (ks == 0) asm volatile("s_waitcnt vmcnt(10)" : "+v"(of[0]), "+v"(of[1]) :: "memory");
      if (ks == 2) asm volatile("s_waitcnt vmcnt(7)" : "+v"(of[2]), "+v"(of[3]), "+v"(of[4]) :: "memory");
      if (ks == 5) asm volatile("s_waitcnt vmcnt(4)" : "+v"(of[5]), "+v"(of[6]), "+v"(of[7]) :: "memory");
      if (ks == 8) asm volatile("s_waitcnt vmcnt(0)" : "+v"(of[8]), "+v"(of[9]) :: "memory");
      SB0();
#pragma unroll
      for (int t = 0; t < 5; ++t) {
        acc[t] = mfma16(ks < 8 ? wq[ks < 8 ? ks : 0][t] : w8[ks & 1][t], __builtin_bit_cast(s16x8, of[ks]), acc[t]);
        if (ks == 2 || ks == 3) w8[ks & 1][t] = ldstr(w8a + (ks & 1) * 5120 + t * 32, w8b + (ks & 1) * 5120 + t * 32);
      }
      SB0();
    }
    STAMP(30);
    if (X == 0) {
      u32 tp = (u32)sgr;
      if (!__all((u32)(sgr >> 32) == tag)) tp = poll_gran(ws, WS_TOP2 + (blockIdx.x & 15) * 128, tag);
      tp = uni(tp);
      const u32 o0 = (int)((tp & 0xffff) >> 4) == t16, o1 = (int)(tp >> 20) == t16;
      lds_wait(L_CNT + 20, 4);
#pragma unroll
      for (int t = 0; t < 5; ++t) *lp<f32x4>(resv + t * 1024) = acc[t];
      *lp<u32>(L_YB + 2560 + wl * 4) = (o0 | o1) ? (o0 | (o1 << 1) | ((tp & 15) << 4) | (((tp >> 16) & 15) << 8)) : 0u;
      lds_arrive(L_CNT + 32 + 4 * wl);
      if (o0 | o1) {
        NOUNROLL for (int cidx = 0; cidx < 2; ++cidx) {
          if (!(cidx ? o1 : o0)) continue;
          const u32 off = WS_Y + cidx * 5120 + (80 * ct3 + 2 * (lane < 40 ? lane : 39)) * 8;
          float s0, s1;
          NOUNROLL for (u32 sp = 0; sp < SPIN_CAP; ++sp) {
            u32x4 gv[16]; u32 bad = 0; s0 = 0.f; s1 = 0.f;
#pragma unroll
            for (int k = 0; k < 16; ++k) gv[k] = __builtin_amdgcn_raw_buffer_load_b128(wsr, off + k * 10240, 0, 16);
#pragma unroll
            for (int k = 0; k < 16; ++k) PINV(gv[k]);
#pragma unroll
            for (int k = 0; k < 16; ++k) { bad |= (gv[k][1] ^ tag) | (gv[k][3] ^ tag); s0 += __uint_as_float(gv[k][0]); s1 += __uint_as_float(gv[k][2]); }
            if (__all(bad == 0)) break;
            __builtin_amdgcn_s_sleep(2);
          }
          if (lane < 40) *lp<f32x2>(L_YB + wl * 640 + cidx * 320 + lane * 8) = (f32x2){s0, s1};
        }
        lds_arrive(L_CNT + 32 + 4 * wl);
      }
    } else {
      lds_wait(L_CNT + 32 + 4 * wl, 1);
      const u32 own = uni(*lp<u32>(L_YB + 2560 + wl * 4));
#pragma unroll
      for (int t = 0; t < 5; ++t) acc[t] += *lp<f32x4>(resv + t * 1024);
      if (own) {
        lds_wait(L_CNT + 32 + 4 * wl, 2);
#pragma unroll
        for (int cidx = 0; cidx < 2; ++cidx)
          if (((own >> cidx) & 1) && tk == (int)((own >> (4 + 4 * cidx)) & 15)) {
#pragma unroll
            for (int t = 0; t < 5; ++t) acc[t] += *lp<f32x4>(L_YB + wl * 640 + cidx * 320 + (16 * t + 4 * g) * 4);
          }
      }
      u32 o[10];
#pragma unroll
      for (int t = 0; t < 5; ++t) { o[2 * t] = pk(acc[t][0], acc[t][1]); o[2 * t + 1] = pk(acc[t][2], acc[t][3]); }
#pragma unroll
      for (int t = 0; t < 4; t += 2) {
        auto r0 = __builtin_amdgcn_permlane16_swap(o[2 * t], o[2 * t + 2], false, false);
        auto r1 = __builtin_amdgcn_permlane16_swap(o[2 * t + 1], o[2 * t + 3], false, false);
        *(GAS u32x4*)(out + ofo + 32 * t) = (u32x4){r0[0], r1[0], r0[1], r1[1]};
      }
      *(GAS u32x2*)(out + ofo2) = (u32x2){o[8], o[9]};
    }
    STAMP(31);
  }
}

extern "C" void kernel_launch(void* const* d_in, const int* in_sizes, int n_in, void* d_out, int out_size, void* d_ws, size_t ws_size,
                              hipStream_t stream) {
  (void)in_sizes; (void)n_in; (void)out_size; (void)ws_size;
  (void)hipFuncSetAttribute((const void*)fused_block, hipFuncAttributeMaxDynamicSharedMemorySize, LDS_BYTES);
  fused_block<<<256, 512, LDS_BYTES, stream>>>((const u16*)d_in[0], (const u16*)d_in[1], (const u16*)d_in[4], (char*)d_ws, (const u16*)d_in[2],
                                               (const u16*)d_in[5], (const u16*)d_in[6], (const u16*)d_in[7], (u16*)d_out);
}
```

```cpp
#include <hip/hip_runtime.h>
#include <stdint.h>

typedef unsigned short u16;
typedef unsigned u32;
typedef unsigned long long u64;
typedef u32 u32x2 __attribute__((ext_vector_type(2)));
typedef u32 u32x4 __attribute__((ext_vector_type(4)));
typedef u32 u32x8 __attribute__((ext_vector_type(8)));
typedef short s16x4 __attribute__((ext_vector_type(4)));
typedef short s16x8 __attribute__((ext_vector_type(8)));
typedef float f32x2 __attribute__((ext_vector_type(2)));
typedef float f32x4 __attribute__((ext_vector_type(4)));
typedef float f32x16 __attribute__((ext_vector_type(16)));
typedef __bf16 bf16x2 __attribute__((ext_vector_type(2)));
#define LAS __attribute__((address_space(3)))
#define GAS __attribute__((address_space(1)))
#define DI __device__ __forceinline__
#define RLX __ATOMIC_RELAXED, __HIP_MEMORY_SCOPE_AGENT
#define SB0() __builtin_amdgcn_sched_barrier(0)

#define ALWAYS_MAX 0
#define P1X 0
#define RR_DELAY 340
#define CODE_LINES 230

constexpr u32 WS_EPOCH = 0;
constexpr u32 WS_QFLAG = 4096;
constexpr u32 WS_OFLAG = 65536;
constexpr u32 WS_TOP2  = 327680;
constexpr u32 WS_STATS = 331776;
constexpr u32 WS_RR    = 348160;
constexpr u32 WS_Y     = 393216;
constexpr u32 WS_XN    = 557056;
constexpr u32 WS_GF    = 655360;
constexpr u32 WS_GP    = 13u << 20;
constexpr u32 WS_QKV   = 1u << 20;
constexpr u32 WS_O     = 10u << 20;
constexpr u32 WS_C4    = 12u << 20;
constexpr u32 WS_TS    = 16u << 20;
constexpr u32 WS_SPAN  = 14u << 20;

constexpr u32 SLOT = 32768;
constexpr u32 L_BP = 4 * SLOT;
constexpr u32 L_WOB = 0;
constexpr u32 L_KVA = 51200, L_KVB = 88576, PLANE = 6208;
constexpr u32 L_WOA7 = 125952;
constexpr u32 L_G = 139264;
constexpr u32 L_XR = 141312;
constexpr u32 L_MISC = 155648;
constexpr u32 L_CNT = L_MISC;
constexpr u32 L_ONE = L_MISC + 1664;
constexpr u32 L_PF  = L_MISC + 2560;
constexpr u32 L_YB  = L_MISC + 5376;
constexpr u32 L_CP  = L_MISC + 2816;
constexpr u32 LDS_BYTES = 163840;
static_assert((L_ONE & 255) == 128 && (L_KVA & 255) == 0 && (L_KVB & 255) == 0, "bank quarters");

constexpr u32 SPIN_CAP = 1u << 17;

template <int I> struct IC { static constexpr int v = I; };
template <int I, int N, class F> DI void static_for(F&& f) { if constexpr (I < N) { f(IC<I>{}); static_for<I + 1, N>(f); } }
template <class T> DI LAS T* lp(u32 a) { return (LAS T*)(uintptr_t)a; }
DI u32 pk(float a, float b) { f32x2 v = {a, b}; return __builtin_bit_cast(u32, __builtin_convertvector(v, bf16x2)); }
DI float blo(u32 v) { return __uint_as_float(v << 16); }
DI float bhi(u32 v) { return __uint_as_float(v & 0xffff0000u); }
DI float rbf(float a) { return blo(pk(a, a)); }
DI u32 uni(u32 v) { return __builtin_amdgcn_readfirstlane(v); }
template <int C> DI float dppf(float v) { return __uint_as_float(__builtin_amdgcn_mov_dpp(__float_as_uint(v), C, 0xf, 0xf, true)); }
DI float x16sum(float v) { auto r = __builtin_amdgcn_permlane16_swap(__float_as_uint(v), __float_as_uint(v), false, false); return __uint_as_float(r[0]) + __uint_as_float(r[1]); }
DI float x32sum(float v) { auto r = __builtin_amdgcn_permlane32_swap(__float_as_uint(v), __float_as_uint(v), false, false); return __uint_as_float(r[0]) + __uint_as_float(r[1]); }
DI float wsum(float v) {
  v += dppf<0xB1>(v); v += dppf<0x4E>(v); v += dppf<0x141>(v); v += dppf<0x140>(v);
  return x32sum(x16sum(v));
}
DI float sel(float a, float b, u64 m) { return __builtin_amdgcn_inverse_ballot_w64(m) ? b : a; }
DI float bsel(float a, float b, u32 m) { return __uint_as_float((__float_as_uint(b) & m) | (__float_as_uint(a) & ~m)); }
DI bf16x2 asb(u32 v) { return __builtin_bit_cast(bf16x2, v); }
DI f32x16 mfma32(s16x8 a, s16x8 b, f32x16 c) { if (P1X & 4) { asm volatile("" :: "v"(a), "v"(b)); return c; } return __builtin_amdgcn_mfma_f32_32x32x16_bf16(a, b, c, 0, 0, 0); }
DI f32x4 mfma16(s16x8 a, s16x8 b, f32x4 c) { return __builtin_amdgcn_mfma_f32_16x16x32_bf16(a, b, c, 0, 0, 0); }
DI s16x8 lds128(u32 a) { return *lp<s16x8>(a); }
DI s16x8 ldstr(u32 a, u32 b) {
  s16x4 lo = __builtin_amdgcn_ds_read_tr16_b64_v4i16(lp<s16x4>(a)), hi = __builtin_amdgcn_ds_read_tr16_b64_v4i16(lp<s16x4>(b));
  return __builtin_shufflevector(lo, hi, 0, 1, 2, 3, 4, 5, 6, 7);
}
DI void dma16(u32 lds, u32 voff, const void* sbase) {
  asm volatile("s_mov_b32 m0, %0\n\ts_nop 0\n\tglobal_load_lds_dwordx4 %1, %2" :: "s"(lds), "v"(voff), "s"(sbase) : "memory");
}
DI u32x4 rsrc_of(const void* p, u32 bytes) { const u64 a = (u64)p; return (u32x4){(u32)a, (u32)(a >> 32) & 0xffffu, bytes, 0x00020000u}; }
template <u32 LDS> DI void dma16b(u32 wbase, u32 voff, u32x4 rsrc, u32 soff) {
  asm volatile("s_add_u32 m0, %0, %4\n\ts_nop 0\n\tbuffer_load_dwordx4 %1, %2, %3 offen lds" :: "s"(wbase), "v"(voff), "s"(rsrc), "s"(soff), "i"(LDS) : "memory", "scc");
}
DI void dma16c(u32 lds, u32 voff, const void* sbase) {
  asm volatile("s_mov_b32 m0, %0\n\ts_nop 0\n\tglobal_load_lds_dwordx4 %1, %2 sc1" :: "s"(lds), "v"(voff), "s"(sbase) : "memory");
}
#define WAITV(n) asm volatile("s_waitcnt vmcnt(" #n ")" ::: "memory")
#define WAITBAR(n) asm volatile("s_waitcnt vmcnt(%0) lgkmcnt(0)\n\ts_barrier" :: "i"(n) : "memory")
#define WAITBAR2(n0, n1) asm volatile("s_waitcnt vmcnt(" #n1 ") lgkmcnt(0)\n\ts_cmp_lg_u32 %0, 0\n\ts_cbranch_scc1 1f\n\ts_waitcnt vmcnt(" #n0 ")\n1:\n\ts_barrier" :: "s"(X) : "memory", "scc")
#define WAITBAR1(n1) asm volatile("s_cmp_lg_u32 %0, 0\n\ts_cbranch_scc0 1f\n\ts_waitcnt vmcnt(" #n1 ")\n1:\n\ts_waitcnt lgkmcnt(0)\n\ts_barrier" :: "s"(X) : "memory", "scc")
#define WAITBARV(n) asm volatile("s_waitcnt vmcnt(%0)\n\ts_barrier" :: "i"(n) : "memory")
#define BAR() asm volatile("s_waitcnt lgkmcnt(0)\n\ts_barrier" ::: "memory")
DI u64 gld64(const char* ws, u32 off) { return __hip_atomic_load((GAS u64*)(ws + off), RLX); }
DI u64 gld64w(const char* ws, u32 off) {
  u64 r; asm volatile("global_load_dwordx2 %0, %1, %2 sc1\n\ts_waitcnt vmcnt(0)" : "=&v"(r) : "v"(off), "s"(ws) : "memory"); return r;
}
#define NOUNROLL _Pragma("unroll 1")
DI void gst64(char* ws, u32 off, u64 v) { __hip_atomic_store((GAS u64*)(ws + off), v, RLX); }
DI void lds_arrive(u32 a) {
  u32 va = a, one = 1;
  asm volatile("s_waitcnt lgkmcnt(0)\n\ts_mov_b64 exec, 1\n\tds_add_u32 %0, %1\n\ts_mov_b64 exec, -1" :: "v"(va), "v"(one) : "memory");
}
DI void lds_wait(u32 a, u32 n) {
  u32 va = a, t, c;
  asm volatile("1:\n\tds_read_b32 %0, %2\n\ts_waitcnt lgkmcnt(0)\n\tv_readfirstlane_b32 %1, %0\n\ts_cmp_ge_u32 %1, %3\n\ts_cbranch_scc1 2f\n\ts_sleep 1\n\ts_branch 1b\n2:"
               : "=&v"(t), "=&s"(c) : "v"(va), "s"(n) : "memory", "scc");
}
#define POLL_TXT(T, N, OFF, WS, TAG) "s_mov_b32 " N ", 0x20000\n1:\n\tglobal_load_dwordx2 " T ", " OFF ", " WS " sc1\n\ts_waitcnt vmcnt(0)\n\t" \
    "v_cmp_ne_u64 vcc, " TAG ", " T "\n\ts_cbranch_vccz 2f\n\ts_sleep 1\n\ts_sub_u32 " N ", " N ", 1\n\ts_cbranch_scc0 1b\n2:\n\t"
DI void poll_flags(const char* ws, u32 off, u64 tag) {
  u64 t; u32 n;
  asm volatile(POLL_TXT("%0", "%1", "%2", "%3", "%4") : "=&v"(t), "=&s"(n) : "v"(off), "s"(ws), "s"(tag) : "memory", "scc", "vcc");
}
DI u32 poll_gran(const char* ws, u32 off, u32 tag) {
  u64 g;
  NOUNROLL for (u32 s = 0; s < SPIN_CAP; ++s) { g = gld64w(ws, off); if (__all((u32)(g >> 32) == tag)) break; __builtin_amdgcn_s_sleep(1); }
  return (u32)g;
}
DI void store_flag(char* ws, u32 off, u64 val, u64 mask) {
  asm volatile("s_mov_b64 exec, %3\n\tglobal_store_dwordx2 %0, %1, %2 sc1\n\ts_mov_b64 exec, -1" :: "v"(off), "v"(val), "s"(ws), "s"(mask) : "memory");
}
#define PINV(x) asm volatile("" : "+v"(x))
#define PINS(x) asm volatile("" : "+s"(x))

#define UOFF 0
#define STAMP(i) do {} while (0)

DI void wo_offset(u32 (&wv)[8], int k, int wl, int lane) {
  const int n = k < 5 ? k : k < 7 ? 3 * (wl & 1) + k - 5 : (wl & 1 ? 4 : 2);
  const u32 P = 64 * n + lane, r = P / 10u, pc = P - 10 * r, g = r >> 3, j = (r & 7) ^ ((g & 1) << 2);
  wv[k] = ((k >= 5 ? 40 * g + 16 : 8 * (g + (g >> 1))) + j) * 1280 + pc * 16;
  PINV(wv[k]);
}
template <int PART>
DI void stage_wo(const char* woc, int H, int wl, const u32 (&wv)[8]) {
  auto at = [&](int ks) { return H == 1 ? L_WOB + ks * 5120 : ks < 7 ? L_KVA + ks * 5120 : L_WOA7 + (ks - 7) * 5120; };
  const int n2 = 3 * (wl & 1), k2 = wl >> 1;
  const char *s0 = woc + (320 * H + 40 * wl) * 1280, *s2 = woc + (320 * H + 160 * k2) * 1280;
#define WO_D(M, V, S) "s_add_u32 m0, " M "\n\ts_nop 0\n\tglobal_load_lds_dwordx4 " V ", " S "\n\t"
#define WO_5 "s_nop 4\n\t" WO_D("%7, 0", "%0", "%5") WO_D("m0, 1024", "%1", "%5") WO_D("m0, 1024", "%2", "%5") WO_D("m0, 1024", "%3", "%5") WO_D("m0, 1024", "%4", "%5")
  if (PART != 1)
    asm volatile(WO_5 WO_D("%8, 0", "%9", "%6")
                 :: "v"(wv[0]), "v"(wv[1]), "v"(wv[2]), "v"(wv[3]), "v"(wv[4]), "s"(s0), "s"(s2), "s"(at(wl)), "s"(at(8 + k2) + (wl & 1 ? 4 : 2) * 1024), "v"(wv[7]) : "memory", "scc");
  if (PART != 0)
    asm volatile(WO_5 WO_D("%8, 0", "%9", "%6") WO_D("m0, 1024", "%10", "%6")
                 :: "v"(wv[0]), "v"(wv[1]), "v"(wv[2]), "v"(wv[3]), "v"(wv[4]), "s"(s0 + 160 * 1280), "s"(s2), "s"(at(wl + 4)), "s"(at(8 + k2) + n2 * 1024), "v"(wv[5]), "v"(wv[6]) : "memory", "scc");
}

#define NP 5
constexpr int NT = 10 - NP, ULAST = 10 + NT;
constexpr u32 page_at(int p) { return p >= NP && p < 6 ? L_BP + (p - NP) * 8192 : (p % 4) * SLOT + 24576; }
constexpr int OP_GR = 41, OP_PF = 43;
constexpr int sched_op(int u, int i) {
  if (u < 8 && i < 3) return 4 * (u + 2) + i;
  if (u < 6) return 4 * (u + 2) + 3;
  if (u == 6) return OP_GR;
  if (u == 7) return 4 * 8 + 3;
  if (u == 8) return i == 0 ? 4 * 9 + 3 : i == 1 ? OP_PF : -1;
  return -1;
}
constexpr int younger(int op, int u) {
  int n = op < 8 ? 7 - op + 1 : 0; bool seen = op < 8;
  for (int v = 0; v < u; ++v) for (int i = 0; i < 4; ++i) { const int o = sched_op(v, i); if (o < 0) continue; if (seen) ++n; if (o == op) seen = true; }
  return n;
}
static_assert(NP >= 5 && NP <= 7, "schedule");
template <bool NEXT, bool LOAD>
DI void attn_block(const f32x16& sc, f32x16& sn, f32x16& O, s16x8 q0, s16x8 q1, s16x8& kn0, s16x8& kn1, u32 nka, u32 nkb, u32 va, s16x8& pend, s16x8& vpend) {
  f32x16 z;
#pragma unroll
  for (int i = 0; i < 16; ++i) z[i] = 0.f;
  SB0();
  if (NEXT) sn = mfma32(kn0, q0, z);
  const s16x8 v0 = ldstr(va, va + 128), v1 = ldstr(va + 256, va + 384);
  u32 p[8];
  float ex[16];
#pragma unroll
  for (int i = 0; i < 4; ++i) ex[i] = __builtin_amdgcn_exp2f(sc[i]);
  SB0();
  if (NEXT) sn = mfma32(kn1, q1, sn);
  if (LOAD) { kn0 = lds128(nka); kn1 = lds128(nkb); }
#pragma unroll
  for (int i = 4; i < 8; ++i) ex[i] = __builtin_amdgcn_exp2f(sc[i]);
  p[0] = pk(ex[0], ex[1]); p[1] = pk(ex[2], ex[3]);
  SB0();
  O = mfma32(vpend, pend, O); PINV(O);
  SB0();
#pragma unroll
  for (int i = 8; i < 12; ++i) ex[i] = __builtin_amdgcn_exp2f(sc[i]);
  p[2] = pk(ex[4], ex[5]); p[3] = pk(ex[6], ex[7]);
  SB0();
  O = mfma32(v0, __builtin_bit_cast(s16x8, (u32x4){p[0], p[1], p[2], p[3]}), O); PINV(O);
  SB0();
#pragma unroll
  for (int i = 12; i < 16; ++i) ex[i] = __builtin_amdgcn_exp2f(sc[i]);
  p[4] = pk(ex[8], ex[9]); p[5] = pk(ex[10], ex[11]); p[6] = pk(ex[12], ex[13]); p[7] = pk(ex[14], ex[15]);
  pend = __builtin_bit_cast(s16x8, (u32x4){p[4], p[5], p[6], p[7]}); vpend = v1;
  SB0();
}
DI f32x16 attn_full(s16x8 q0, s16x8 q1, u32 ka, u32 kb, u32 va, u32 vo, u32 vstep, const u32 (&dm)[8], const u32 (&mk)[16]) {
  f32x16 z, O, S[2];
#pragma unroll
  for (int i = 0; i < 16; ++i) z[i] = 0.f;
  s16x8 kn0, kn1, pend, vpend;
  {
    const s16x8 kd0 = lds128(ka + 4096), ko0 = lds128(ka), kd1 = lds128(kb + 4096), ko1 = lds128(kb), k10 = lds128(ka + 3584), k11 = lds128(kb + 3584);
    const s16x8 v0 = ldstr(va, va + 128), vo0 = ldstr(vo, vo + 128), vo1 = ldstr(vo + 256, vo + 384), v1 = ldstr(va + 256, va + 384);
    kn0 = lds128(ka + 3072); kn1 = lds128(kb + 3072);
    SB0();
    f32x16 sd = mfma32(kd0, q0, z), so = mfma32(ko0, q0, z);
    sd = mfma32(kd1, q1, sd); so = mfma32(ko1, q1, so);
    S[1] = mfma32(k10, q0, z);
    SB0();
    u32 p[8], pd[8];
#pragma unroll
    for (int i = 0; i < 8; ++i) {
      p[i] = pk(__builtin_amdgcn_exp2f(bsel(so[2 * i], sd[2 * i], mk[2 * i])), __builtin_amdgcn_exp2f(bsel(so[2 * i + 1], sd[2 * i + 1], mk[2 * i + 1])));
      pd[i] = p[i] & dm[i]; p[i] ^= pd[i];
      if (i == 1) { S[1] = mfma32(k11, q1, S[1]); PINV(S[1]); SB0(); }
      if (i == 3) { O = mfma32(v0, __builtin_bit_cast(s16x8, (u32x4){pd[0], pd[1], pd[2], pd[3]}), z); PINV(O); SB0(); }
      if (i == 5) { O = mfma32(vo0, __builtin_bit_cast(s16x8, (u32x4){p[0], p[1], p[2], p[3]}), O); PINV(O); SB0(); }
    }
    O = mfma32(vo1, __builtin_bit_cast(s16x8, (u32x4){p[4], p[5], p[6], p[7]}), O);
    pend = __builtin_bit_cast(s16x8, (u32x4){pd[4], pd[5], pd[6], pd[7]}); vpend = v1;
  }
  static_for<1, 8>([&](auto ec) __attribute__((always_inline)) {
    constexpr int e = decltype(ec)::v;
    va -= vstep;
    attn_block<(e < 7), (e < 6)>(S[e & 1], S[(e + 1) & 1], O, q0, q1, kn0, kn1, ka + 3072 - e * 512, kb + 3072 - e * 512, va, pend, vpend);
  });
  return mfma32(vpend, pend, O);
}
DI f32x16 attn_short(s16x8 q0, s16x8 q1, u32 ka, u32 kb, u32 va, u32 vstep, const u32 (&dm)[8], int nfull) {
  f32x16 z, O, S0, S1;
#pragma unroll
  for (int i = 0; i < 16; ++i) z[i] = 0.f;
  s16x8 kn0, kn1, pend, vpend;
  {
    const s16x8 kd0 = lds128(ka + 4096), kd1 = lds128(kb + 4096), k10 = lds128(ka + 3584), k11 = lds128(kb + 3584);
    const s16x8 v0 = ldstr(va, va + 128), v1 = ldstr(va + 256, va + 384);
    kn0 = lds128(ka + 3072); kn1 = lds128(kb + 3072);
    SB0();
    const f32x16 sd = mfma32(kd1, q1, mfma32(kd0, q0, z));
    S1 = mfma32(k11, q1, mfma32(k10, q0, z));
    SB0();
    u32 p[8];
#pragma unroll
    for (int i = 0; i < 8; ++i) p[i] = pk(__builtin_amdgcn_exp2f(sd[2 * i]), __builtin_amdgcn_exp2f(sd[2 * i + 1])) & dm[i];
    O = mfma32(v0, __builtin_bit_cast(s16x8, (u32x4){p[0], p[1], p[2], p[3]}), z);
    pend = __builtin_bit_cast(s16x8, (u32x4){p[4], p[5], p[6], p[7]}); vpend = v1;
  }
  NOUNROLL for (int n = nfull; n > 0; n -= 2) {
    va -= vstep;
    attn_block<true, true>(S1, S0, O, q0, q1, kn0, kn1, ka + 2560, kb + 2560, va, pend, vpend);
    if (n == 1) break;
    va -= vstep;
    attn_block<true, true>(S0, S1, O, q0, q1, kn0, kn1, ka + 2048, kb + 2048, va, pend, vpend);
    ka -= 1024; kb -= 1024;
  }
  return mfma32(vpend, pend, O);
}
DI f32x16 attn_shifted(s16x8 q0, s16x8 q1, u32 ka, u32 kb, u32 va, u32 vstep, int nblk, int c, int hh) {
  f32x16 z, O;
#pragma unroll
  for (int i = 0; i < 16; ++i) { z[i] = 0.f; O[i] = 0.f; }
  float m = -__builtin_inff();
  NOUNROLL for (int pass = 0; pass < 2; ++pass) {
    NOUNROLL for (int e = 0; e < nblk; ++e) {
      f32x16 s = mfma32(lds128(kb + 4096 - e * 512), q1, mfma32(lds128(ka + 4096 - e * 512), q0, z));
      int lo = (e == 8 ? c + 1 : 0) - 4 * hh, n = (e == 0 ? c : 31) - 4 * hh - lo;
      PINV(lo); PINV(n);
#pragma unroll
      for (int i = 0; i < 16; ++i) { const int k = (i & 3) + 8 * (i >> 2) - lo; s[i] = (k >= 0 && k <= n) ? s[i] : -__builtin_inff(); }
      if (pass == 0) {
#pragma unroll
        for (int i = 0; i < 16; ++i) m = fmaxf(m, s[i]);
      } else {
        u32 p[8];
#pragma unroll
        for (int i = 0; i < 8; ++i) p[i] = pk(__builtin_amdgcn_exp2f(s[2 * i] - m), __builtin_amdgcn_exp2f(s[2 * i + 1] - m));
        const u32 v = va - e * vstep;
        O = mfma32(ldstr(v, v + 128), __builtin_bit_cast(s16x8, (u32x4){p[0], p[1], p[2], p[3]}), O);
        O = mfma32(ldstr(v + 256, v + 384), __builtin_bit_cast(s16x8, (u32x4){p[4], p[5], p[6], p[7]}), O);
      }
    }
    auto r = __builtin_amdgcn_permlane32_swap(__float_as_uint(m), __float_as_uint(m), false, false);
    m = fmaxf(__uint_as_float(r[0]), __uint_as_float(r[1]));
  }
  return O;
}

DI void row_stats(u32x4 xa, u32 xb, u32x4 wa, u32 wb, char* ws, int srow, u32 tag, int tid) {
  const int lane = tid & 63;
  const u32 xu[5] = {xa[0], xa[1], xa[2], xa[3], xb}, wu5[5] = {wa[0], wa[1], wa[2], wa[3], wb};
  f32x2 v[5], s2 = {0.f, 0.f};
#pragma unroll
  for (int i = 0; i < 5; ++i) { v[i] = (f32x2){blo(xu[i]), bhi(xu[i])}; s2 += v[i]; }
  const float mean = wsum(s2[0] + s2[1]) * (1.f / 640.f), mb = rbf(mean);
  f32x2 q2 = {0.f, 0.f};
  float r = 0.f;
#pragma unroll
  for (int i = 0; i < 5; ++i) {
    const f32x2 d = v[i] - mean, e = v[i] - mb;
    q2 += d * d;
    r = __builtin_amdgcn_fdot2_f32_bf16(asb(pk(e[0], e[1])), asb(wu5[i]), r, false);
  }
  float q = q2[0] + q2[1];
  q += dppf<0xB1>(q); r += dppf<0xB1>(r); q += dppf<0x4E>(q); r += dppf<0x4E>(r);
  q += dppf<0x141>(q); r += dppf<0x141>(r); q += dppf<0x140>(q); r += dppf<0x140>(r);
  { auto t = __builtin_amdgcn_permlane16_swap(__float_as_uint(q), __float_as_uint(r), false, false); q = __uint_as_float(t[0]) + __uint_as_float(t[1]); }
  q = x32sum(q);
  r = __uint_as_float(__builtin_amdgcn_readlane(__float_as_uint(q), 16));
  q = __uint_as_float(__builtin_amdgcn_readlane(__float_as_uint(q), 0));
  const float sd = rbf(__builtin_amdgcn_sqrtf(rbf(rbf(q * (1.f / 640.f)) + 1e-5f)));
  const u32 pay = lane == 0 ? ((__float_as_uint(mb) >> 16) | __float_as_uint(sd)) : __float_as_uint(r * __builtin_amdgcn_rcpf(sd));
  store_flag(ws, (lane == 0 ? WS_STATS : WS_RR) + srow * 8, ((u64)tag << 32) | pay, 3);
  if (tid < 16) *lp<u32>(L_CNT + tid * 4) = 0;
  if (tid < 144) *lp<u32>(L_ONE + tid * 4) = 0x3F803F80u;
}

__global__ void __launch_bounds__(512, 2)
fused_block(const u16* x_, const u16* wqkv_, const u16* wr_, char* ws_, const u16* wo_, const u16* wg_, const u16* wu_, const u16* wd_, u16* out_) {
  (void)x_; (void)wqkv_; (void)wr_; (void)ws_; (void)wo_; (void)wg_; (void)wu_; (void)wd_; (void)out_;
  u32x8 ka;
  asm volatile("s_load_dwordx8 %0, %1, 0x0" : "=s"(ka) : "s"(__builtin_amdgcn_kernarg_segment_ptr()));
  auto karg = [](int i) { const __attribute__((address_space(4))) u64* k = (const __attribute__((address_space(4))) u64*)__builtin_amdgcn_kernarg_segment_ptr(); PINS(k); return (char*)k[i]; };
  extern __shared__ __attribute__((aligned(16))) char smem[];
  (void)smem;
  STAMP(0);
  const int b = blockIdx.x, xc = b & 7, bi = b >> 3;
  const int tid = threadIdx.x, lane = tid & 63, w = uni(tid >> 6);
  const int c = lane & 31, hh = lane >> 5;
  const bool helper = bi >= 30;

  if (w == 7) {
    u64 pc; asm volatile("s_getpc_b64 %0" : "=s"(pc));
    constexpr int PER = (CODE_LINES + 31) / 32;
    u32 ln = bi * PER + (lane % PER); ln = ln < CODE_LINES ? ln : CODE_LINES - 1;
    asm volatile("s_mov_b32 m0, %0\n\ts_nop 0\n\tglobal_load_lds_dword %1, %2" :: "s"(L_PF), "v"(ln * 128), "s"(pc & ~127ull) : "memory");
  }
  int Lx = (xc >> 1) * 30 + (helper ? 0 : bi), uj = Lx >> 3, rtile = (xc & 1) * 8 + (Lx & 7);
  int uA = (uj / 5) * 10 + uj % 5, rot = (rtile * 3 + uj) % 10;
  u32 vx = (8 * w + (lane >> 3)) * 1280 + (((lane & 7) ^ ((4 * w + (lane >> 4)) & 7)) << 4);
  u32 vw = (8 * w + (lane >> 3)) * 3840 + (((lane & 7) ^ ((lane >> 2) & 4)) << 4), l20 = lane * 20;
  PINV(vx); PINV(vw); PINV(l20); PINS(rtile); PINS(uA); PINS(rot);
  asm volatile("s_waitcnt lgkmcnt(0)" : "+s"(ka));
  const u16* x = (const u16*)(((u64)ka[1] << 32) | ka[0]);
  const u16* wqkv = (const u16*)(((u64)ka[3] << 32) | ka[2]);
  const u16* wr = (const u16*)(((u64)ka[5] << 32) | ka[4]);
  char* ws = (char*)(((u64)ka[7] << 32) | ka[6]);
  const int srow = b * 8 + w;
#define ROW_LOADS() u32x4 xa, wa; u32 xb, wb; \
    asm volatile("global_load_dwordx4 %0, %5, %6\n\tglobal_load_dword %1, %5, %6 offset:16\n\t" \
                 "global_load_dwordx4 %2, %5, %7\n\tglobal_load_dword %3, %5, %7 offset:16\n\ts_load_dwordx2 %4, %8, 0x0" \
                 : "=&v"(xa), "=&v"(xb), "=&v"(wa), "=&v"(wb), "=&s"(tag64) : "v"(l20), "s"(x + srow * 640), "s"(wr), "s"(ws) : "memory")
#define ROW_STATS(n) \
    asm volatile("s_waitcnt vmcnt(%5) lgkmcnt(0)" : "+v"(xa), "+v"(xb), "+v"(wa), "+v"(wb), "+s"(tag64) : "i"(n) : "memory"); \
    tag64 = ((u64)uni((u32)((tag64 + 1) >> 32)) << 32) | uni((u32)tag64 + 1); tag = (u32)tag64; \
    row_stats(xa, xb, wa, wb, ws, srow, tag, tid)
  u64 tag64; u32 tag;
  const __amdgpu_buffer_rsrc_t wsr = __builtin_amdgcn_make_buffer_rsrc(ws, 0, WS_SPAN, 0x00020000);
  const int X = w >> 2, wl = w & 3;

  u64 sgr = 0;
#define RES_PREP(bb) u32 xo_[4]; const char* xs_; \
    { const int row_ = 64 * (((bb) & 7) + 8 * ((bb) >> 6)) + 16 * wl; \
      _Pragma("unroll") for (int n_ = 0; n_ < 3; ++n_) { const u32 P_ = 64 * n_ + lane, r_ = P_ / 10u; xo_[n_] = (r_ < 16 ? r_ : 15) * 1280 + (P_ - 10 * r_) * 16; PINV(xo_[n_]); } \
      xo_[3] = X ? WS_STATS + (row_ + (lane & 15)) * 8 : WS_TOP2 + ((bb) & 15) * 128; PINV(xo_[3]); \
      xs_ = karg(0) + (u32)(row_ * 1280 + (((bb) >> 3) & 7) * 160); PINS(xs_); }
#define RES_ISSUE() asm volatile("s_cmp_eq_u32 %8, 0\n\tglobal_load_dwordx2 %0, %1, %2 sc1\n\ts_cbranch_scc1 1f\n\t" \
                 WO_D("%7, 0", "%3", "%6") WO_D("m0, 1024", "%4", "%6") WO_D("m0, 1024", "%5", "%6") "\n1:" \
                 : "=&v"(sgr) : "v"(xo_[3]), "s"(ws), "v"(xo_[0]), "v"(xo_[1]), "v"(xo_[2]), "s"(xs_), "s"(L_XR + wl * 3072), "s"(X) : "memory", "scc")

  u32 ob, cb, pfo, ofo, ofo2, w0a, w0b, w8a, w8b, resv;
  int t16, ct3;
  char* out;
  f32x4 acc[5];
  auto p3_setup = [&]() __attribute__((always_inline)) {
    int b3 = blockIdx.x; PINS(b3);
    const int tk = lane & 15, g = lane >> 4;
    ct3 = (b3 >> 3) & 7; t16 = 4 * ((b3 & 7) + 8 * (b3 >> 6)) + wl;
    const u32 wf_ = (8 * g + ((lane >> 2) & 3)) * 160 + (lane & 3) * 8, wfa = wf_ + (g & 1) * 640, wfb = wf_ + ((g & 1) ^ 1) * 640;
    const u32 wb0 = X == 1 ? L_WOB : L_KVA, wb8 = X == 1 ? L_WOB + 8 * 5120 : L_WOA7 + 5120;
    w0a = wb0 + wfa; w0b = wb0 + wfb; w8a = wb8 + wfa; w8b = wb8 + wfb;
    resv = L_KVB + wl * 5120 + lane * 16;
    ob = WS_O + X * (1u << 20) + t16 * 8192 + lane * 16; cb = WS_C4 + X * (1u << 18) + t16 * 2048 + g * 256 + tk * 8;
    pfo = WS_OFLAG + ((t16 >> 1) * 32 + 16 * X + tk) * 128;
    const u32 ro = (16 * t16 + tk) * 1280 + 160 * ct3;
    ofo = ro + 32 * (g & 1) + 16 * (g >> 1); ofo2 = ro + 128 + 8 * g;
    out = karg(8);
    PINV(ofo); PINV(ofo2); PINV(w0a); PINV(w0b); PINV(w8a); PINV(w8b); PINV(resv); PINV(ob); PINV(cb); PINV(pfo); PINS(out); PINS(t16); PINS(ct3);
    if (X == 1) {
      u32 sg = (u32)sgr;
      if (!__all((u32)(sgr >> 32) == tag)) sg = poll_gran(ws, WS_STATS + (16 * t16 + tk) * 8, tag);
      const float rs = __builtin_amdgcn_rcpf(bhi(sg)), k1 = 1.f + rs, k0 = -blo(sg) * rs;
#pragma unroll
      for (int t = 0; t < 5; ++t) {
        const u32x2 xr = *lp<u32x2>(L_XR + wl * 3072 + tk * 160 + g * 8 + t * 32);
        acc[t] = (f32x4){fmaf(blo(xr[0]), k1, k0), fmaf(bhi(xr[0]), k1, k0), fmaf(blo(xr[1]), k1, k0), fmaf(bhi(xr[1]), k1, k0)};
      }
    } else {
#pragma unroll
      for (int t = 0; t < 5; ++t) acc[t] = (f32x4){0.f, 0.f, 0.f, 0.f};
    }
#pragma unroll
    for (int t = 0; t < 5; ++t) PINV(acc[t]);
  };

  if (!helper) {
    ROW_LOADS();
    const u32x4 rx = rsrc_of((const char*)x + rtile * (128 * 1280), 128 * 1280), rw = rsrc_of((const char*)wqkv + uA * 128, 640 * 3840 - uA * 128);
    const u32 wk = w * 1024;
    u32 vx2 = vx + 64 * 1280, vw2 = vw + 640, so[10], sw[10];
    so[0] = rot; so[1] = rot == 9 ? 0 : rot + 1;
    auto piece = [&](auto opc) __attribute__((always_inline)) {
      constexpr int op = decltype(opc)::v, a = op / 4, p = op % 4;
      if constexpr (p < 2) dma16b<(a % 4) * SLOT + p * 8192>(wk, p ? vx2 : vx, rx, so[a]);
      else if constexpr (p == 2) dma16b<(a % 4) * SLOT + 16384>(wk, vw, rw, sw[a]);
      else dma16b<page_at(a)>(wk, vw2, rw, sw[a]);
    };
    sw[0] = so[0] * (64 * 3840); sw[1] = so[1] * (64 * 3840); so[0] *= 128; so[1] *= 128;
    static_for<0, 8>(piece);
#pragma unroll
    for (int n = 2; n < 10; ++n) { const u32 t = rot + n, sl = t >= 10 ? t - 10 : t; so[n] = sl * 128; sw[n] = sl * (64 * 3840); PINS(so[n]); PINS(sw[n]); }
    const int tt = wl, fh = X;
    const u32 key = (c >> 1) & 7;
    u32 xad[2][4];
#pragma unroll
    for (int i = 0; i < 4; ++i) { xad[0][i] = (32 * tt + c) * 128 + (((2 * i + hh) ^ key) << 4); xad[1][i] = xad[0][i] + 2 * SLOT; PINV(xad[0][i]); PINV(xad[1][i]); }
    const u32 q4 = (lane >> 2) & 3, p4 = lane & 3, g2 = (lane >> 4) & 1;
    u32 wad[3], cad[3];
    wad[0] = (8 * hh + q4) * 128 + (((4 * fh + 2 * (p4 & 1) + g2) ^ ((q4 >> 1) << 2)) << 4) + (p4 >> 1) * 8;
    cad[0] = (32 * X + 8 * (lane >> 4) + q4) * 128 + (((2 * wl + (p4 >> 1)) ^ ((q4 >> 1) << 2)) << 4) + (p4 & 1) * 8;
#pragma unroll
    for (int i = 1; i < 3; ++i) { wad[i] = wad[0] + i * 65536; cad[i] = cad[0] + i * 65536; PINV(wad[i]); PINV(cad[i]); }
    auto wfrag = [&](u32 page, int i) { const u32 a = wad[page >> 16] + (page & 65535) + i * 2048; return ldstr(a, a + 512); };
    auto cfrag = [&](u32 page) { const u32 a = cad[page >> 16] + (page & 65535); return ldstr(a, a + 512); };
    u32x4 onev = {0x3F803F80u, 0x3F803F80u, 0x3F803F80u, 0x3F803F80u};
    PINV(wad[0]); PINV(cad[0]); PINV(onev);
    ROW_STATS(8);
    SB0();
    const int gm = bi * 8 + xc, gk = gm / 24, gc = gm - 24 * gk;
    const char* pfa;
    const char* gsrc[2];
    u32 vg, goff, qoff, csl, cswr, csm, csr, xno, wv[8], qscb;
    u32x4 xn[4];
#define XN_TXT "global_load_dwordx4 %0, %4, %5 sc1\n\tglobal_load_dwordx4 %1, %4, %5 offset:16 sc1\n\tglobal_load_dwordx4 %2, %4, %5 offset:128 sc1\n\tglobal_load_dwordx4 %3, %4, %5 offset:144 sc1"
    auto chores = [&](int k) __attribute__((always_inline)) {
      if (k < 8) wo_offset(wv, k, wl, lane);
      if (k == 8) {
        const u32 k0 = (bi * 8 + w) * 14 + (lane < 13 ? lane : 13), kk = k0 < 3200 ? k0 : 3199;
        const char *wo = karg(4), *wgu = karg(5 + X);
        pfa = lane < 48 ? wo + (3200 + kk) * 128 : wgu + (u32)((64 * gk + 16 * wl + (lane & 15)) * 3072 + gc * 128);
        PINV(pfa);
      }
      if (k == 9) {
        goff = WS_STATS + (rtile * 128 + 32 * tt + c) * 8;
        qoff = WS_QKV + (8 * uA + 4 * fh + 2 * hh) * 32768 + (rtile * 128 + 32 * tt + c) * 16;
        vg = (8 * w + (lane >> 3)) * 3072 + (((lane & 7) ^ ((lane >> 2) & 4)) << 4);
        PINV(goff); PINV(qoff); PINV(vg);
      }
      if (k == 10) {
        csl = L_CP + (32 * fh + c) * 4; cswr = L_CP + X * 256 + (16 * wl + (lane & 15)) * 4;
        csm = L_CP + 1024 + w * 128 + c * 4; csr = L_CP + 1024 + w * 128 + hh * 64;
        xno = WS_XN + (b & 15) * 5120 + (lane & 1) * 2560 + (32 * gk + 4 * (lane >> 4)) * 8;
        PINV(csl); PINV(cswr); PINV(csm); PINV(csr); PINV(xno);
      }
      if (k == 11) {
        gsrc[0] = karg(5) + (u32)(64 * gk * 3072 + gc * 128); gsrc[1] = karg(6) + (u32)(64 * gk * 3072 + gc * 128);
        qscb = uA < 10 ? 0x3ea52b5cu : 0x3f800000u;
        PINS(gsrc[0]); PINS(gsrc[1]); PINS(qscb);
      }
    };
    s16x8 xf[40], fa[2][4], fb[2][4], wt[8], cf, cg, ct[NT];
    const s16x8 ones = __builtin_bit_cast(s16x8, onev);
    f32x16 aca, acb;
    f32x4 acs = {0.f, 0.f, 0.f, 0.f}, act = acs;
#pragma unroll
    for (int i = 0; i < 16; ++i) { aca[i] = 0.f; acb[i] = 0.f; }
    u64 gr = 0;
    float mneg = 0.f, rsq = 0.f, c0 = 0.f, c1 = 0.f;
    u32 eo[8];
    f32x4 cp[4];
    auto epi2 = [&](float a0, float a1, float k0, float k1) {
      u32 r; float t;
      asm("v_fma_f32 %0, %2, %4, %6\n\tv_fma_f32 %1, %2, %5, %7\n\tv_mul_f32 %0, %3, %0\n\tv_mul_f32 %1, %3, %1\n\tv_cvt_pk_bf16_f32 %0, %0, %1"
          : "=&v"(r), "=&v"(t) : "v"(mneg), "v"(rsq), "v"(k0), "v"(k1), "v"(a0), "v"(a1));
      return r;
    };
    auto epi_store = [&](int second, int k) {
      __builtin_amdgcn_raw_buffer_store_b128((u32x4){eo[4 * k], eo[4 * k + 1], eo[4 * k + 2], eo[4 * k + 3]}, wsr, qoff + k * 32768 + second * (40 * 32768), 0, 16);
    };
    static_for<0, ULAST + 1>([&](auto uc) __attribute__((always_inline)) {
      constexpr int u = decltype(uc)::v;
      if constexpr (u < 10) WAITBARV(younger(4 * u + (u < NP ? 3 : 2), u));
      if constexpr (u == 10) WAITBARV(younger(4 * 9 + 3, 10));
      if constexpr (u == 11 || u == 13) asm volatile("s_waitcnt lgkmcnt(%0)\n\ts_barrier" :: "i"(u == 11 ? 6 : NT > 3 ? 4 : 0) : "memory");
      if constexpr (u == 10) {
        PINV(gr);
        u32 pay = (u32)gr;
        if (!P1X && !__all((u32)(gr >> 32) == tag)) pay = poll_gran(ws, goff, tag);
        mneg = -blo(pay); rsq = __builtin_amdgcn_rcpf(bhi(pay)) * __uint_as_float(qscb); PINV(mneg); PINV(rsq);
      }
      SB0();
      static_for<0, 4>([&](auto ic) __attribute__((always_inline)) {
        constexpr int i = decltype(ic)::v;
        if constexpr (u > 0 && u <= 10) aca = mfma32(fa[(u - 1) & 1][i], xf[4 * (u - 1) + i], aca);
        if constexpr (u > 10) acb = mfma32(wt[(4 * (u - 11) + i) & 7], xf[4 * (NP + u - 11) + i], acb);
        SB0();
        if constexpr (u < 10) {
          if (i == 0) { cf = cfrag((u % 4) * SLOT + 16384); if constexpr (u < NP) cg = cfrag(page_at(u)); }
          xf[4 * u + i] = lds128(xad[(u >> 1) & 1][i] + (u & 1) * SLOT);
          fa[u & 1][i] = wfrag((u % 4) * SLOT + 16384, i);
        }
        constexpr int op = sched_op(u, i);
        if constexpr (!(P1X & 16) && op >= 0 && op < 40) piece(IC<(op >= 0 && op < 40) ? op : 0>{});
        if (op == OP_GR) asm volatile("global_load_dwordx2 %0, %1, %2 sc1" : "=v"(gr) : "v"(goff), "s"(ws) : "memory");
        if (op == OP_PF) asm volatile("s_mov_b32 m0, %0\n\ts_nop 0\n\tglobal_load_lds_dword %1, off" :: "s"(L_PF), "v"(pfa) : "memory");
        if constexpr (u > 0 && u <= NP) { SB0(); acb = mfma32(fb[(u - 1) & 1][i], xf[4 * (u - 1) + i], acb); SB0(); }
        if constexpr (u < NP) fb[u & 1][i] = wfrag(page_at(u), i);
        if constexpr (u >= 1 && u <= 3) chores(4 * (u - 1) + i);
        if constexpr (u < 10) { if (i == 3) { acs = mfma16(ones, cf, acs); if constexpr (u < NP) act = mfma16(ones, cg, act); } }
        if constexpr (u >= 10) {
          auto rd = [&](int f) { wt[f & 7] = wfrag(page_at(NP + f / 4), f & 3); };
          if constexpr (u == 10) { rd(i == 0 ? 0 : i == 1 ? 2 : i == 2 ? 3 : 5); if (!(i & 1)) rd(i == 0 ? 1 : 4); }
          else if constexpr (4 * (u - 11) + i + 6 < 4 * NT) rd(4 * (u - 11) + i + 6);
        }
        if constexpr (u == 10) {
          if (i == 0) { static_for<0, NT>([&](auto kc) __attribute__((always_inline)) { ct[decltype(kc)::v] = cfrag(page_at(NP + decltype(kc)::v)); }); }
          if (i == 1) *lp<float>(cswr) = acs[0];
        }
        if constexpr (u == 11) static_for<0, NT>([&](auto kc) __attribute__((always_inline)) { constexpr int k = decltype(kc)::v; if ((k * 4) / NT == i) act = mfma16(ones, ct[k], act); });
        if constexpr (u == 12) { if (i == 1) *lp<float>(cswr + 512) = act[0]; }
        if constexpr (u == 11 || u == 13) {
          constexpr u32 o = u == 11 ? 0 : 512;
          if (i == 0) { c0 = *lp<float>(csl + o); c1 = *lp<float>(csl + o + 256); }
          if (i == 2) *lp<float>(csm) = c0 + c1;
          if (i == 3) {
#pragma unroll
            for (int k = 0; k < 4; ++k) cp[k] = *lp<f32x4>(csr + 16 * k);
          }
        }
        if constexpr (u == 12) {
          eo[2 * i] = epi2(aca[4 * i], aca[4 * i + 1], cp[i][0], cp[i][1]); eo[2 * i + 1] = epi2(aca[4 * i + 2], aca[4 * i + 3], cp[i][2], cp[i][3]);
          if (i & 1) epi_store(0, i >> 1);
        }
        if constexpr (u == 13 && i < 2) dma16(L_G + i * 8192 + wk, vg, gsrc[i]);
        if constexpr (u == 13 && i == 2) asm volatile("s_cmp_lg_u32 %6, 0\n\ts_cbranch_scc1 1f\n\t" XN_TXT "\n1:" : "=&v"(xn[0]), "=&v"(xn[1]), "=&v"(xn[2]), "=&v"(xn[3]) : "v"(xno), "s"(ws), "s"(X) : "memory", "scc");
        SB0();
      });
    });
    STAMP(4);
    u32 fl = WS_QFLAG + (rtile * 30 + uA) * 128; u64 tagv = tag64;
    PINV(fl); PINV(tagv);
#pragma unroll
    for (int i = 0; i < 4; ++i) {
      eo[2 * i]     = pk(fmaf(mneg, cp[i][0], acb[4 * i]) * rsq, fmaf(mneg, cp[i][1], acb[4 * i + 1]) * rsq);
      eo[2 * i + 1] = pk(fmaf(mneg, cp[i][2], acb[4 * i + 2]) * rsq, fmaf(mneg, cp[i][3], acb[4 * i + 3]) * rsq);
      if (i & 1) epi_store(1, i >> 1);
    }
    SB0();
    STAMP(3);
    asm volatile("s_waitcnt vmcnt(2) lgkmcnt(0)\n\ts_barrier" : "+v"(xn[0]), "+v"(xn[1]), "+v"(xn[2]), "+v"(xn[3]) :: "memory");
    if (w == 4) store_flag(ws, fl, tagv, 1);
    const char* woc = karg(4) + (bi & 7) * 160; PINS(woc);
    if (X == 1) stage_wo<0>(woc, 1, wl, wv);
    else {
      NOUNROLL for (u32 sp = 0; sp < SPIN_CAP; ++sp) {
        u32 bad = 0;
#pragma unroll
        for (int i = 0; i < 4; ++i) bad |= (xn[i][1] ^ tag) | (xn[i][3] ^ tag);
        if (__all(bad == 0)) break;
        asm volatile("s_sleep 2\n\t" XN_TXT "\n\ts_waitcnt vmcnt(0)" : "=&v"(xn[0]), "=&v"(xn[1]), "=&v"(xn[2]), "=&v"(xn[3]) : "v"(xno), "s"(ws) : "memory");
      }
      const u32 ga = L_G + (8 * (lane >> 4) + q4) * 128 + (((2 * wl + (p4 >> 1)) ^ ((q4 >> 1) << 2)) << 4) + (p4 & 1) * 8;
      const u32 go = (lane & 14) ? 0x80000000u : WS_GP + gk * 49152 + (lane & 1) * 12288 + (64 * gc + 16 * wl + 4 * (lane >> 4)) * 8;
#pragma unroll
      for (int m = 0; m < 2; ++m) {
        f32x4 y = {0.f, 0.f, 0.f, 0.f};
#pragma unroll
        for (int ks = 0; ks < 2; ++ks)
          y = mfma16(ldstr(ga + m * 8192 + ks * 4096, ga + m * 8192 + ks * 4096 + 512), __builtin_bit_cast(s16x8, (u32x4){xn[2 * ks][0], xn[2 * ks][2], xn[2 * ks + 1][0], xn[2 * ks + 1][2]}), y);
        __builtin_amdgcn_raw_buffer_store_b128((u32x4){__float_as_uint(y[0]), tag, __float_as_uint(y[1]), tag}, wsr, go + m * 24576, 0, 16);
        __builtin_amdgcn_raw_buffer_store_b128((u32x4){__float_as_uint(y[2]), tag, __float_as_uint(y[3]), tag}, wsr, go + m * 24576 + 16, 0, 16);
      }
      store_flag(ws, WS_GF + (gk * 96 + 4 * gc + wl) * 128, tagv, 1);
      const u32 l = (bi * 4 + wl) * 27 + (lane < 26 ? lane : 26);
      asm volatile("s_mov_b32 m0, %0\n\ts_nop 4\n\tglobal_load_lds_dword %1, %2" :: "s"(L_PF), "v"((l < 3200 ? l : 3199) * 128), "s"(woc - (bi & 7) * 160) : "memory");
    }
    {
    int b2 = blockIdx.x; PINS(b2);
    const int xc = b2 & 7, bi = b2 >> 3;
    const int odd = bi >= 15, hl = 2 * xc + odd, grp = bi - 15 * odd, head = 16 * X + hl;
    const int oc0 = 5 * (8 * X + xc);
    const int oA = oc0 + 3 * odd, oG = oc0 + 2;
    const u32 kv = X == 0 ? L_KVA : L_KVB;
    const int g0 = grp == 0;
    u32 pollo;
    {
      const int l4 = lane & 15, isq = (l4 + 4) >> 4, b1 = (l4 >> 1) & 1, ri = l4 >> 2;
      const int oct = oc0 + 2 * odd + 2 * (l4 & 1) + (80 + 80 * b1) * (1 - isq);
      const int rt = g0 ? (isq ? b1 : (ri + 1) >> 1) : grp - 1 + ri - isq;
      pollo = WS_QFLAG + (rt * 30 + (oct >> 3)) * 128;
    }
    const int t0 = g0 ? wl : 4 * (grp + 1) + wl, t1 = g0 ? 7 - wl : t0;
    u32 qo0 = WS_QKV + (oA + hh) * 32768 + c * 16 + t0 * 512, qo1 = WS_QKV + oG * 32768 + c * 16 + t0 * 512, l16 = lane * 16;
    u32 qd = (t1 - t0) * 512;
    const int tp0 = (3 - g0) * (wl >> 1);
    const char* kvs = ws + (u32)(WS_QKV + 80 * (1 + (wl & 1)) * 32768 + ((g0 ? 0 : 128 * (grp - 1)) + 64 * tp0) * 16);
    const char* sA = kvs + (u32)(oA * 32768); const char* sB = sA + 32768; const char* sG = kvs + (u32)(oG * 32768);
    u32 mA = kv + 3 * (wl & 1) * PLANE + tp0 * 1024;
    RES_PREP(b2);
    fl += 5 * 128;
    PINV(pollo); PINV(qo0); PINV(qo1); PINV(l16); PINV(fl); PINS(sA); PINS(sB); PINS(sG); PINS(mA); PINS(qd);
    WAITBAR(6);
    if (w == 1) store_flag(ws, fl, tagv, 1);
    STAMP(5);
    RES_ISSUE();
    u32 mk[16], dm[8], hmask, kml, kmh, vlane, vstep, vold, kla, klb, oo, co4, ofl;
    auto att_setup = [&]() __attribute__((always_inline)) {
      int d = 4 * hh - 1 - c;
#pragma unroll
      for (int i = 0; i < 16; ++i) asm("v_add_u32 %0, %2, %1\n\tv_ashrrev_i32 %0, 31, %0" : "=&v"(mk[i]) : "v"(d), "i"((i & 3) + 8 * (i >> 2)));
#pragma unroll
      for (int i = 0; i < 8; ++i) { dm[i] = (mk[2 * i] & 0xffffu) | (mk[2 * i + 1] & 0xffff0000u); PINV(dm[i]); }
      hmask = (u32)-hh; kml = (u32)((hh | odd) - 1); kmh = (u32)((hh | (odd ^ 1)) - 1);
      const u32 realv = (u32)(((g2 & ((p4 | (p4 >> 1)) & 1))) - 1);
      vlane = hh * 64 + q4 * 16 + ((realv & (kv + (3 + 2 * g2 + (p4 & 1)) * PLANE + (g2 * odd + (p4 >> 1)) * 8)) | (~realv & L_ONE));
      vstep = realv & 512u;
      const int dl0 = g0 ? 0 : 8 + wl;
      vold = vlane + (realv & (u32)(wl * 512));
      vlane += realv & (u32)(dl0 * 512);
      kla = kv + hh * PLANE + c * 16 + (dl0 - 8) * 512; klb = kv + 2 * PLANE + c * 16 + (dl0 - 8) * 512;
      oo = WS_O + X * (1u << 20) + t0 * 16384 + (c >> 4) * 8192 + xc * 1024 + (2 * odd + hh) * 256 + (c & 15) * 16;
      co4 = (WS_C4 + X * (1u << 18) + t0 * 4096 + (c >> 4) * 2048 + hl * 128 + (c & 15) * 8) | (hmask & 0x80000000u);
      ofl = WS_OFLAG + (t0 * 32 + head) * 128 + (lane & 1) * (qd * 8);
      PINV(hmask); PINV(kml); PINV(kmh); PINV(vlane); PINV(vstep); PINV(vold); PINV(kla); PINV(klb); PINV(oo); PINV(co4); PINV(ofl);
    };
    if (X == 1) { att_setup(); asm volatile("s_waitcnt vmcnt(0)" : "+v"(sgr) :: "memory"); p3_setup(); }
    __builtin_amdgcn_s_setprio(1);
    u32x4 qa, qb, qa2, qb2;
    {
      u64 pt; u32 pn;
      asm volatile(POLL_TXT("%4", "%5", "%6", "%7", "%8")
                   "s_mov_b32 m0, %9\n\tglobal_load_dwordx4 %0, %14, %7 sc1\n\tglobal_load_lds_dwordx4 %13, %10 sc1\n\tglobal_load_lds_dwordx4 %13, %10 offset:1024 sc1\n\t"
                   "s_add_u32 m0, %9, 6208\n\tglobal_load_dwordx4 %1, %15, %7 sc1\n\tglobal_load_lds_dwordx4 %13, %11 sc1\n\tglobal_load_lds_dwordx4 %13, %11 offset:1024 sc1\n\t"
                   "s_add_u32 m0, %9, 12416\n\ts_cmp_lg_u32 %16, 0\n\tglobal_load_lds_dwordx4 %13, %12 sc1\n\tglobal_load_lds_dwordx4 %13, %12 offset:1024 sc1\n\t"
                   "s_cbranch_scc1 3f\n\t"
                   "global_load_lds_dwordx4 %13, %12 offset:2048 sc1\n\ts_mov_b32 m0, %9\n\ts_nop 0\n\tglobal_load_lds_dwordx4 %13, %10 offset:2048 sc1\n\t"
                   "s_add_u32 m0, %9, 6208\n\ts_nop 0\n\tglobal_load_lds_dwordx4 %13, %11 offset:2048 sc1\n\ts_branch 4f\n"
                   "3:\n\tv_add_u32 %14, %16, %14\n\tv_add_u32 %15, %16, %15\n\tglobal_load_dwordx4 %2, %14, %7 sc1\n\tglobal_load_dwordx4 %3, %15, %7 sc1\n"
                   "4:"
                   : "=&v"(qa), "=&v"(qb), "=&v"(qa2), "=&v"(qb2), "=&v"(pt), "=&s"(pn), "+v"(pollo), "+s"(ws), "+s"(tag64), "+s"(mA), "+s"(sA), "+s"(sB), "+s"(sG),
                     "+v"(l16), "+v"(qo0), "+v"(qo1), "+s"(qd)
                   :: "memory", "scc", "vcc");
    }
    if (X == 0) { stage_wo<1>(woc, 1, wl, wv); att_setup(); PINV(sgr); p3_setup(); }
    u32 cnta = L_CNT + 4 * X, one = 1, tmpv, tmps;
    asm volatile("s_waitcnt vmcnt(7)\n\ts_cmp_eq_u32 %8, 0\n\ts_cbranch_scc1 6f\n\ts_waitcnt vmcnt(0)\n6:\n\ts_mov_b64 exec, 1\n\tds_add_u32 %2, %3\n\ts_mov_b64 exec, -1\n"
                 "5:\n\tds_read_b32 %0, %2\n\ts_waitcnt lgkmcnt(0)\n\tv_readfirstlane_b32 %1, %0\n\ts_cmp_ge_u32 %1, 4\n\ts_cbranch_scc0 5b"
                 : "=&v"(tmpv), "=&s"(tmps), "+v"(cnta), "+v"(one), "+v"(qa), "+v"(qb), "+v"(qa2), "+v"(qb2)
                 : "s"(X) : "memory", "scc");
    STAMP(7);
    {
      auto in_range = [&](const f32x16& O) {
        const float l = bsel(O[12], O[8], hmask);
        const float t = ((O[0] + O[1]) + (O[2] + O[3])) + ((O[4] + O[5]) + (O[6] + O[7])) + ((O[8] + O[9]) + (O[10] + O[11]));
        return __all(l > 0x1p-40f && l < 0x1p40f && __builtin_fabsf(t) < __builtin_inff());
      };
      auto put = [&](const f32x16& O, u32 ao, u32 ac) {
        const float inv = __builtin_amdgcn_rcpf(bsel(O[12], O[8], hmask));
        __builtin_amdgcn_raw_buffer_store_b128((u32x4){pk(O[0] * inv, O[1] * inv), pk(O[2] * inv, O[3] * inv), pk(O[4] * inv, O[5] * inv), pk(O[6] * inv, O[7] * inv)}, wsr, ao, 0, 16);
        __builtin_amdgcn_raw_buffer_store_b64((u32x2){pk(O[8] * inv, O[9] * inv), pk(O[10] * inv, O[11] * inv)}, wsr, ac, 0, 16);
      };
      u32 todo = ALWAYS_MAX ? (g0 ? 3u : 1u) : 0u;
      const s16x8 q0 = __builtin_bit_cast(s16x8, qa), q1 = __builtin_bit_cast(s16x8, (u32x4){qb[0] & kml, qb[1] & kml, qb[2] & kmh, qb[3] & kmh});
      if (!ALWAYS_MAX) {
        if (!g0) {
          const f32x16 O = attn_full(q0, q1, kla, klb, vlane, vold, vstep, dm, mk);
          STAMP(12);
          if (in_range(O)) put(O, oo, co4); else todo = 1;
        } else {
          s16x8 r0 = q0, r1 = q1;
          NOUNROLL for (int it = 0; it < 2; ++it) {
            const int tq = it ? t1 : t0;
            const f32x16 O = attn_short(r0, r1, kla + tq * 512, klb + tq * 512, vlane + vstep * tq, vstep, dm, tq);
            if (in_range(O)) put(O, oo + it * (qd * 32), co4 + it * (qd * 8)); else todo |= 1u << it;
            r0 = __builtin_bit_cast(s16x8, qa2); r1 = __builtin_bit_cast(s16x8, (u32x4){qb2[0] & kml, qb2[1] & kml, qb2[2] & kmh, qb2[3] & kmh});
          }
        }
      }
      if (todo) {
        int cc = c, hc = hh; PINV(cc); PINV(hc);
        NOUNROLL for (int it = 0; it < 2; ++it) {
          if (!((todo >> it) & 1)) continue;
          const int tq = g0 ? (it ? t1 : t0) : 0;
          const u32x4 ra = it ? qa2 : qa, rb = it ? qb2 : qb;
          const f32x16 O = attn_shifted(__builtin_bit_cast(s16x8, ra), __builtin_bit_cast(s16x8, (u32x4){rb[0] & kml, rb[1] & kml, rb[2] & kmh, rb[3] & kmh}),
                                        kla + tq * 512, klb + tq * 512, vlane + vstep * tq, vstep, g0 ? tq + 1 : 9, cc, hc);
          put(O, oo + it * (qd * 32), co4 + it * (qd * 8));
        }
      }
    }
    STAMP(8);
    WAITV(0);
    store_flag(ws, ofl, tagv, g0 ? 3 : 1);
    lds_arrive(X == 0 ? L_CNT + 8 : L_CNT + 20);
    STAMP(9);
    if (X == 0) {
      __builtin_amdgcn_s_setprio(0);
      lds_wait(L_CNT + 8, 4);
      stage_wo<2>(woc, 0, wl, wv);
      __builtin_amdgcn_s_setprio(1);
    }
    }
  } else {
    ROW_LOADS();
    const u64 hstart = __builtin_amdgcn_s_memrealtime();
    const int hg = xc * 2 + (bi - 30);
    const char* wd = karg(7) + hg * (96 * 1280);
    const int rg = lane >> 4;
    {
      u32 r = (w * 64 + lane) / 80u, p = w * 64 + lane - 80 * r;
#pragma unroll
      for (int k = 0; k < 15; ++k) {
        dma16((k * 8 + w) * 1024, r * 1280 + ((p ^ (2 * (r & 3) + (r & 8))) << 4), wd);
        p += 32; const u32 cy = p >= 80; p -= 80 * cy; r += 6 + cy;
        if (k == 3) { ROW_STATS(4); }
      }
    }
    auto mkey = [](u32 f, u32 row) { return ((f ^ (((int)f >> 31) | 0x80000000u)) & 0xfffff800u) | (2047u - row); };
    auto mx = [](u32 p, u32 q) { return p > q ? p : q; };
    auto mn = [](u32 p, u32 q) { return p < q ? p : q; };
    u32 k1, k2;
#define TOP2_STEP(o1, o2) do { const u32 p_ = (o1), q_ = (o2); k2 = mx(mn(k1, p_), mx(k2, q_)); k1 = mx(k1, p_); } while (0)
#define TOP2_DPP(C) TOP2_STEP(__builtin_amdgcn_mov_dpp(k1, C, 0xf, 0xf, true), __builtin_amdgcn_mov_dpp(k2, C, 0xf, 0xf, true))
    NOUNROLL for (int sp = 0; sp < 64 && __builtin_amdgcn_s_memrealtime() - hstart < RR_DELAY; ++sp) __builtin_amdgcn_s_sleep(4);
    {
      u32x4 ra, rb;
      const u32 roff = WS_RR + tid * 32;
      NOUNROLL for (u32 sp = 0; sp < SPIN_CAP; ++sp) {
        asm volatile("s_nop 4\n\tglobal_load_dwordx4 %0, %2, %3 sc1\n\tglobal_load_dwordx4 %1, %2, %3 offset:16 sc1\n\ts_waitcnt vmcnt(0)" : "=&v"(ra), "=&v"(rb) : "v"(roff), "s"(ws) : "memory");
        if (__all(ra[1] == tag && ra[3] == tag && rb[1] == tag && rb[3] == tag)) break;
      }
      const u32 e0 = mkey(ra[0], 4 * tid), e1 = mkey(ra[2], 4 * tid + 1), e2 = mkey(rb[0], 4 * tid + 2), e3 = mkey(rb[2], 4 * tid + 3);
      k1 = mx(e0, e1); k2 = mn(e0, e1);
      TOP2_STEP(mx(e2, e3), mn(e2, e3));
    }
    TOP2_DPP(0xB1); TOP2_DPP(0x4E); TOP2_DPP(0x141); TOP2_DPP(0x140);
    LAS u32x2* kk = lp<u32x2>(L_CP);
    if ((lane & 15) == 0) kk[4 * w + rg] = (u32x2){k1, k2};
    BAR();
    { const u32x2 e = kk[lane & 15], f = kk[16 + (lane & 15)]; k1 = e[0]; k2 = e[1]; TOP2_STEP(f[0], f[1]); }
    TOP2_DPP(0xB1); TOP2_DPP(0x4E); TOP2_DPP(0x141); TOP2_DPP(0x140);
    k1 = uni(k1); k2 = uni(k2);
    STAMP(3);
    const u32 i0 = 2047u - (k1 & 2047u), i1 = 2047u - (k2 & 2047u);
    if (w < 2) {
      const u32 row = w ? i1 : i0;
      u32 xrow[5];
#pragma unroll
      for (int i = 0; i < 5; ++i) xrow[i] = ((const GAS u32*)x)[row * 320 + i * 64 + lane];
      const u32 sg = poll_gran(ws, WS_STATS + row * 8, tag);
      const float mb = blo(sg), rs = __builtin_amdgcn_rcpf(bhi(sg));
#pragma unroll
      for (int i = 0; i < 5; ++i) {
        const u32 t = pk(blo(xrow[i]) - mb, bhi(xrow[i]) - mb);
        __builtin_amdgcn_raw_buffer_store_b64((u32x2){pk(blo(t) * rs, bhi(t) * rs), tag}, wsr, WS_XN + hg * 5120 + w * 2560 + (i * 64 + lane) * 8, 0, 16);
      }
      if (tid == 0) { gst64(ws, WS_TOP2 + hg * 128, ((u64)tag << 32) | i0 | (i1 << 16)); if (hg == 15) gst64(ws, WS_EPOCH, tag64); }
    }
    STAMP(4);
    WAITBAR(0);
    s16x8 df[5][3];
    {
      const u32 q = (lane >> 2) & 3, key = 2 * q + 8 * (rg & 1);
#pragma unroll
      for (int t = 0; t < 5; ++t) {
        const u32 ad = (8 * rg + q) * 1280 + (((10 * w + 2 * t + ((lane >> 1) & 1)) ^ key) << 4) + (lane & 1) * 8;
#pragma unroll
        for (int ks = 0; ks < 3; ++ks) { df[t][ks] = ldstr(ad + ks * 40960, ad + ks * 40960 + 5120); PINV(df[t][ks]); }
      }
    }
    BAR();
    STAMP(2);
    u32 wv[8];
#pragma unroll
    for (int k = 0; k < 8; ++k) wo_offset(wv, k, wl, lane);
    RES_PREP(b);
    RES_ISSUE();
    stage_wo<2>(karg(4) + (bi & 7) * 160, X, wl, wv);
    asm volatile("s_waitcnt vmcnt(13)" : "+v"(sgr) :: "memory");
    p3_setup();
    sgr = ((u64)tag << 32) | i0 | (i1 << 16);
    STAMP(5);
    const u32 kq = ((lane & 1) ? k2 : k1) & 0xfffff800u;
    float gate = rbf(__builtin_amdgcn_rcpf(1.f + __expf(-rbf(__uint_as_float(kq ^ ((kq >> 31) ? 0x80000000u : ~0u))))));
    u32 yo = ((lane & 14) ? 0x80000000u : WS_Y + hg * 10240 + (lane & 1) * 5120 + (80 * w + 4 * rg) * 8);
    const u32 tc = 32 * (w < 6 ? w : 5) + (lane >> 1), gtok = tc >= 96;
    u32 gpo = WS_GP + (lane & 1) * 24576 + gtok * 12288 + (96 * hg + tc - 96 * gtok) * 8;
    const int gi = lane < 60 ? lane : 59;
    u32 gfo = WS_GF + ((gi / 6) * 96 + 6 * hg + gi % 6) * 128;
    PINV(gate); PINV(yo); PINV(gpo); PINV(gfo);
    WAITV(0);
    poll_flags(ws, gfo, tag64);
    STAMP(6);
    if (w < 6) {
      float sum;
      NOUNROLL for (u32 sp = 0; sp < SPIN_CAP; ++sp) {
        u64 gv[10];
#define GP_LD(i) "global_load_dwordx2 %" #i ", %10, %11 sc1\n\tv_add_u32 %10, 0xc000, %10\n\t"
        u32 o = gpo;
        asm volatile(GP_LD(0) GP_LD(1) GP_LD(2) GP_LD(3) GP_LD(4) GP_LD(5) GP_LD(6) GP_LD(7) GP_LD(8) GP_LD(9) "s_waitcnt vmcnt(0)"
                     : "=&v"(gv[0]), "=&v"(gv[1]), "=&v"(gv[2]), "=&v"(gv[3]), "=&v"(gv[4]), "=&v"(gv[5]), "=&v"(gv[6]), "=&v"(gv[7]), "=&v"(gv[8]), "=&v"(gv[9]), "+v"(o) : "s"(ws) : "memory");
        u32 bad = 0; sum = 0.f;
#pragma unroll
        for (int k = 0; k < 10; ++k) { bad |= (u32)(gv[k] >> 32) ^ tag; sum += __uint_as_float((u32)gv[k]); }
        if (__all(bad == 0)) break;
      }
      const float g = rbf(sum), u = dppf<0xB1>(g);
      const float hval = rbf(g * __builtin_amdgcn_rcpf(1.f + __expf(-g))) * u;
      if (!(lane & 1)) *lp<u16>(L_CP + 1536 + tc * 2) = (u16)pk(hval, hval);
    }
    BAR();
    STAMP(7);
    {
      s16x8 hf[3];
#pragma unroll
      for (int ks = 0; ks < 3; ++ks) hf[ks] = lds128(L_CP + 1536 + (lane & 1) * 192 + rg * 16 + ks * 64);
      f32x4 y[5];
#pragma unroll
      for (int t = 0; t < 5; ++t) y[t] = (f32x4){0.f, 0.f, 0.f, 0.f};
#pragma unroll
      for (int ks = 0; ks < 3; ++ks)
#pragma unroll
        for (int t = 0; t < 5; ++t) y[t] = mfma16(df[t][ks], hf[ks], y[t]);
#pragma unroll
      for (int t = 0; t < 5; ++t) {
        __builtin_amdgcn_raw_buffer_store_b128((u32x4){__float_as_uint(y[t][0] * gate), tag, __float_as_uint(y[t][1] * gate), tag}, wsr, yo + t * 128, 0, 16);
        __builtin_amdgcn_raw_buffer_store_b128((u32x4){__float_as_uint(y[t][2] * gate), tag, __float_as_uint(y[t][3] * gate), tag}, wsr, yo + t * 128 + 16, 0, 16);
      }
    }
    STAMP(8);
    lds_arrive(X == 0 ? L_CNT + 8 : L_CNT + 20);
    STAMP(9);
  }

  {
    const int tk = lane & 15, g = lane >> 4;
    STAMP(1);
    if (X == 0) { WAITV(0); lds_arrive(L_CNT + 12); }
    lds_wait(X == 0 ? L_CNT + 12 : L_CNT + 8, 4);
    STAMP(2);
    s16x8 wq[8][5];
#pragma unroll
    for (int ks = 0; ks < 8; ++ks)
#pragma unroll
      for (int t = 0; t < 5; ++t) wq[ks][t] = ldstr((ks < 7 ? w0a + ks * 5120 : w8a - 5120) + t * 32, (ks < 7 ? w0b + ks * 5120 : w8b - 5120) + t * 32);
    u32 ob2 = ob + 4096;
    STAMP(10);
    u32x4 of[10];
    {
      u64 pt; u32 pn;
      asm volatile(POLL_TXT("%10", "%11", "%12", "%13", "%14")
                   "global_load_dwordx4 %0, %15, %13 sc1\n\tglobal_load_dwordx4 %1, %15, %13 offset:1024 sc1\n\tglobal_load_dwordx4 %2, %15, %13 offset:2048 sc1\n\t"
                   "global_load_dwordx4 %3, %15, %13 offset:3072 sc1\n\tglobal_load_dwordx4 %4, %16, %13 sc1\n\tglobal_load_dwordx4 %5, %16, %13 offset:1024 sc1\n\t"
                   "global_load_dwordx4 %6, %16, %13 offset:2048 sc1\n\tglobal_load_dwordx4 %7, %16, %13 offset:3072 sc1\n\t"
                   "global_load_dwordx2 v[240:241], %17, %13 sc1\n\tglobal_load_dwordx2 v[242:243], %17, %13 offset:128 sc1\n\t"
                   "global_load_dwordx2 v[244:245], %17, %13 offset:1024 sc1\n\tglobal_load_dwordx2 v[246:247], %17, %13 offset:1152 sc1\n\t"
                   : "=&v"(of[0]), "=&v"(of[1]), "=&v"(of[2]), "=&v"(of[3]), "=&v"(of[4]), "=&v"(of[5]), "=&v"(of[6]), "=&v"(of[7]),
                     "=&{v[240:243]}"(of[8]), "=&{v[244:247]}"(of[9]), "=&v"(pt), "=&s"(pn), "+v"(pfo), "+s"(ws), "+s"(tag64), "+v"(ob), "+v"(ob2), "+v"(cb)
                   :: "memory", "scc", "vcc");
    }
    s16x8 w8[2][5];
#pragma unroll
    for (int ks = 0; ks < 10; ++ks) {
      if (ks == 0) asm volatile("s_waitcnt vmcnt(10)" : "+v"(of[0]), "+v"(of[1]) :: "memory");
      if (ks == 2) asm volatile("s_waitcnt vmcnt(7)" : "+v"(of[2]), "+v"(of[3]), "+v"(of[4]) :: "memory");
      if (ks == 5) asm volatile("s_waitcnt vmcnt(4)" : "+v"(of[5]), "+v"(of[6]), "+v"(of[7]) :: "memory");
      if (ks == 8) asm volatile("s_waitcnt vmcnt(0)" : "+v"(of[8]), "+v"(of[9]) :: "memory");
      SB0();
#pragma unroll
      for (int t = 0; t < 5; ++t) {
        acc[t] = mfma16(ks < 8 ? wq[ks < 8 ? ks : 0][t] : w8[ks & 1][t], __builtin_bit_cast(s16x8, of[ks]), acc[t]);
        if (ks == 2 || ks == 3) w8[ks & 1][t] = ldstr(w8a + (ks & 1) * 5120 + t * 32, w8b + (ks & 1) * 5120 + t * 32);
      }
      SB0();
    }
    STAMP(30);
    if (X == 0) {
      u32 tp = (u32)sgr;
      if (!__all((u32)(sgr >> 32) == tag)) tp = poll_gran(ws, WS_TOP2 + (blockIdx.x & 15) * 128, tag);
      tp = uni(tp);
      const u32 o0 = (int)((tp & 0xffff) >> 4) == t16, o1 = (int)(tp >> 20) == t16;
      lds_wait(L_CNT + 20, 4);
#pragma unroll
      for (int t = 0; t < 5; ++t) *lp<f32x4>(resv + t * 1024) = acc[t];
      *lp<u32>(L_YB + 2560 + wl * 4) = (o0 | o1) ? (o0 | (o1 << 1) | ((tp & 15) << 4) | (((tp >> 16) & 15) << 8)) : 0u;
      lds_arrive(L_CNT + 32 + 4 * wl);
      if (o0 | o1) {
        NOUNROLL for (int cidx = 0; cidx < 2; ++cidx) {
          if (!(cidx ? o1 : o0)) continue;
          const u32 off = WS_Y + cidx * 5120 + (80 * ct3 + 2 * (lane < 40 ? lane : 39)) * 8;
          float s0, s1;
          NOUNROLL for (u32 sp = 0; sp < SPIN_CAP; ++sp) {
            u32x4 gv[16]; u32 bad = 0; s0 = 0.f; s1 = 0.f;
#pragma unroll
            for (int k = 0; k < 16; ++k) gv[k] = __builtin_amdgcn_raw_buffer_load_b128(wsr, off + k * 10240, 0, 16);
#pragma unroll
            for (int k = 0; k < 16; ++k) PINV(gv[k]);
#pragma unroll
            for (int k = 0; k < 16; ++k) { bad |= (gv[k][1] ^ tag) | (gv[k][3] ^ tag); s0 += __uint_as_float(gv[k][0]); s1 += __uint_as_float(gv[k][2]); }
            if (__all(bad == 0)) break;
            __builtin_amdgcn_s_sleep(2);
          }
          if (lane < 40) *lp<f32x2>(L_YB + wl * 640 + cidx * 320 + lane * 8) = (f32x2){s0, s1};
        }
        lds_arrive(L_CNT + 32 + 4 * wl);
      }
    } else {
      lds_wait(L_CNT + 32 + 4 * wl, 1);
      STAMP(29);
      const u32 own = uni(*lp<u32>(L_YB + 2560 + wl * 4));
#pragma unroll
      for (int t = 0; t < 5; ++t) acc[t] += *lp<f32x4>(resv + t * 1024);
      if (own) {
        lds_wait(L_CNT + 32 + 4 * wl, 2);
#pragma unroll
        for (int cidx = 0; cidx < 2; ++cidx)
          if (((own >> cidx) & 1) && tk == (int)((own >> (4 + 4 * cidx)) & 15)) {
#pragma unroll
            for (int t = 0; t < 5; ++t) acc[t] += *lp<f32x4>(L_YB + wl * 640 + cidx * 320 + (16 * t + 4 * g) * 4);
          }
      }
      STAMP(28);
      u32 o[10];
#pragma unroll
      for (int t = 0; t < 5; ++t) { o[2 * t] = pk(acc[t][0], acc[t][1]); o[2 * t + 1] = pk(acc[t][2], acc[t][3]); }
#pragma unroll
      for (int t = 0; t < 4; t += 2) {
        auto r0 = __builtin_amdgcn_permlane16_swap(o[2 * t], o[2 * t + 2], false, false);
        auto r1 = __builtin_amdgcn_permlane16_swap(o[2 * t + 1], o[2 * t + 3], false, false);
        *(GAS u32x4*)(out + ofo + 32 * t) = (u32x4){r0[0], r1[0], r0[1], r1[1]};
      }
      *(GAS u32x2*)(out + ofo2) = (u32x2){o[8], o[9]};
    }
    STAMP(31);
  }
}

extern "C" void kernel_launch(void* const* d_in, const int* in_sizes, int n_in, void* d_out, int out_size, void* d_ws, size_t ws_size,
                              hipStream_t stream) {
  (void)in_sizes; (void)n_in; (void)out_size; (void)ws_size;
  (void)hipFuncSetAttribute((const void*)fused_block, hipFuncAttributeMaxDynamicSharedMemorySize, LDS_BYTES);
  fused_block<<<256, 512, LDS_BYTES, stream>>>((const u16*)d_in[0], (const u16*)d_in[1], (const u16*)d_in[4], (char*)d_ws, (const u16*)d_in[2],
                                               (const u16*)d_in[5], (const u16*)d_in[6], (const u16*)d_in[7], (u16*)d_out);
}
```
